```python
import torch

def _softmax(x, axis=-1):
    return torch.softmax(x, dim=axis)

def _gelu(x):
    return torch.nn.functional.gelu(x, approximate='tanh')

def _silu(x):
    return torch.nn.functional.silu(x)

def _sigmoid(x):
    return torch.sigmoid(x)

def _layernorm(x, axis=-1, eps=1e-05):
    m = x.mean(dim=axis, keepdim=True)
    v = x.var(dim=axis, keepdim=True, unbiased=False)
    return (x - m) / torch.sqrt(v + eps)

def _scatter_add(tgt, idx, src, axis=0):
    return tgt.clone().index_add_(axis, idx, src)

def _segment_sum(src, seg, n):
    out = torch.zeros((n,) + src.shape[1:], dtype=src.dtype)
    return out.index_add_(0, seg, src)

def _pad(x, pw):
    flat = tuple((v for pair in reversed(pw) for v in pair))
    return torch.nn.functional.pad(x, flat)
kernel_name = 'standalone__autoregressive_conv_wavernn__speech'

def setup_inputs():

    _w = lambda a: (lambda t: t.to(torch.bfloat16) if t.is_floating_point() else t)(torch.as_tensor(a))
    B, T, D, K = (2, 16000, 256, 15)
    return {'x': _w((lambda *s: torch.randn(*s, dtype=torch.bfloat16) * 0.1)(B, T, D)), 'w': _w((lambda *s: torch.randn(*s, dtype=torch.bfloat16) * 0.1)(K, D, 2 * D))}

def reference(x, w):
    B, T, D = x.shape
    K = w.shape[0]
    dil = 2
    pad = (K - 1) * dil
    xp = _pad(x, ((0, 0), (pad, 0), (0, 0)))
    h = torch.einsum('btd,de->bte', xp[:, 0:T, :], w[0])
    for i in range(1, K):
        h = h + torch.einsum('btd,de->bte', xp[:, i * dil:i * dil + T, :], w[i])
    f, g = (h[..., :D].clone(), h[..., D:].clone())
    return torch.tanh(f) * _sigmoid(g)
```

```cpp
#include <hip/hip_runtime.h>
#include <hip/hip_bf16.h>
typedef short bf16x8 __attribute__((ext_vector_type(8)));
typedef float f32x4 __attribute__((ext_vector_type(4)));
constexpr int B_=2, T_=16000, D_=256, K_=15;

__global__ void prep_w(const unsigned short* __restrict__ w, unsigned short* __restrict__ wt) {
    int idx = blockIdx.x * 256 + threadIdx.x;
    int d = idx & 255, e = (idx >> 8) & 511, i = idx >> 17;
    wt[idx] = w[(i * 256 + d) * 512 + e];
}

__global__ __launch_bounds__(256) void conv_simple(const unsigned short* __restrict__ x, const unsigned short* __restrict__ wt, __hip_bfloat16* __restrict__ out) {
    int wid = threadIdx.x >> 6, lane = threadIdx.x & 63, fr = lane & 15, fq = lane >> 4;
    int mt = blockIdx.x >> 2;
    int nt = (blockIdx.x & 3) * 4 + wid;
    int row0 = mt * 64;
    int b = row0 / T_, t0 = row0 % T_;
    f32x4 accf[4] = {}, accg[4] = {};
    for (int i = 0; i < K_; ++i) {
        for (int kc = 0; kc < 8; ++kc) {
            bf16x8 bf = *(const bf16x8*)(wt + ((size_t)(i * 512 + nt * 16 + fr) * 256 + kc * 32 + fq * 8));
            bf16x8 bg = *(const bf16x8*)(wt + ((size_t)(i * 512 + 256 + nt * 16 + fr) * 256 + kc * 32 + fq * 8));
            #pragma unroll
            for (int m = 0; m < 4; ++m) {
                int t = t0 + m * 16 + fr + 2 * i - 28;
                bf16x8 a = {};
                if (t >= 0) a = *(const bf16x8*)(x + ((size_t)(b * T_ + t) * 256 + kc * 32 + fq * 8));
                accf[m] = __builtin_amdgcn_mfma_f32_16x16x32_bf16(a, bf, accf[m], 0, 0, 0);
                accg[m] = __builtin_amdgcn_mfma_f32_16x16x32_bf16(a, bg, accg[m], 0, 0, 0);
            }
        }
    }
    for (int m = 0; m < 4; ++m)
        for (int j = 0; j < 4; ++j) {
            int r = row0 + m * 16 + fq * 4 + j;
            float f = accf[m][j], g = accg[m][j];
            float o = tanhf(f) * (1.f / (1.f + expf(-g)));
            out[(size_t)r * 256 + nt * 16 + fr] = __float2bfloat16(o);
        }
}

extern "C" void kernel_launch(void* const* d_in, const int* in_sizes, int n_in,
                              void* d_out, int out_size, void* d_ws, size_t ws_size,
                              hipStream_t stream) {
    const unsigned short* x = (const unsigned short*)d_in[0];
    const unsigned short* w = (const unsigned short*)d_in[1];
    unsigned short* wt = (unsigned short*)d_ws;
    prep_w<<<15 * 512 * 256 / 256, 256, 0, stream>>>(w, wt);
    conv_simple<<<500 * 4, 256, 0, stream>>>(x, wt, (__hip_bfloat16*)d_out);
}
```

```cpp
#include <hip/hip_runtime.h>
#include <hip/hip_bf16.h>

typedef unsigned u32x4 __attribute__((ext_vector_type(4)));
typedef unsigned long long u64x4 __attribute__((ext_vector_type(4)));

constexpr int T_ = 16000;
constexpr unsigned STREAM = 320 * 1024;
constexpr unsigned ROUND = 8u << 20, HS_OFF = 16u << 20, OOB = 0x80000000u;
constexpr unsigned XR = 28672, HALO = 8448, WHI = 83968;

__device__ __forceinline__ u32x4 desc(unsigned long long p, unsigned n) { return u32x4{(unsigned)p, (unsigned)(p >> 32) & 0xffffu, n, 0x20000u}; }

__global__ __launch_bounds__(256, 1) void conv_main(const unsigned char*, const unsigned char*, unsigned char*, unsigned char*
) {
    __shared__ __attribute__((aligned(16))) unsigned char lds[163840];
    const unsigned ldsbase = (unsigned)(size_t)lds;
    const auto kp = __builtin_amdgcn_kernarg_segment_ptr();
    u64x4 ka;
    unsigned vDum = threadIdx.x;
    asm volatile("s_load_dwordx8 %0, %1, 0" : "={s[48:55]}"(ka) : "s"(kp));
    asm volatile(
    "s_bfe_u32 s38, %1, 0x30003\n\t"
    "v_lshrrev_b32 v191, 6, %0\n\t"
    "v_and_b32 %0, 63, %0\n\t"
    "s_mul_i32 s38, s38, 56\n\t"
    "v_min_u32 %0, 13, %0\n\t"
    "v_mad_u32_u24 %0, v191, 14, %0\n\t"
    "s_getpc_b64 s[36:37]\n\t"
    "L_pc0:\n\t"
    "v_add_lshl_u32 %0, %0, s38, 7\n\t"
    "v_min_u32 %0, L_codeend-L_pc0-4, %0\n\t"
    "global_load_dword %0, %0, s[36:37]\n\t"
        : "+{v170}"(vDum) : "s"(blockIdx.x) : "s36", "s37", "s38", "scc", "v191");
    const unsigned w_ = __builtin_amdgcn_readfirstlane(threadIdx.x >> 6);
    unsigned force = 0, forcew = 0;
    #pragma nounroll
    for (unsigned first = 1;; first = 0) {
        unsigned l, bid = blockIdx.x, w = w_;
        asm volatile("v_mbcnt_lo_u32_b32 %0, -1, 0\n\tv_mbcnt_hi_u32_b32 %0, -1, %0" : "=v"(l));
        asm volatile("" : "+s"(bid), "+s"(w));
        const unsigned trow = 1000 * (((bid & 1) << 3) + (bid >> 5)) + ((bid >> 1) & 1);
        auto xoff = [&](unsigned m, unsigned byte) { return ((trow - 32 + 8 * m) * 512u + byte) & 0xbfffffffu; };
        u32x4 xd = desc(((bid >> 2) & 1) * (T_ * 512u), T_ * 512u), wd = desc(0, 15u * 262144u);
        const unsigned w4352 = w * 4352, w1024 = w * 1024;
        const unsigned rot = 2 * ((bid >> 3) & 3), rot64 = rot * 64;
        unsigned vWd = (32 * rot + 8 * w + (l >> 3)) * 1024 + ((bid >> 3) & 3) * 128 + ((l & 7) ^ ((l >> 3) & 2) ^ (4 * (w & 1))) * 16;
        unsigned vXr1 = xoff(32 * w + (l >> 3) + 8, ((l & 7) * 16) ^ ((w & 1) * 32));
        unsigned vXr0 = (vXr1 - 32768) & 0xbfffffffu, vXr2 = vXr1 + 32768, vXr3 = vXr1 + 65536;
        unsigned vXh = xoff(128, (l & 31) * 16 + (l < 32 ? 3072 : 0));
        u32x4 hx0;
        asm volatile("" : "+{v176}"(vXr0), "+{v177}"(vXr1), "+{v178}"(vXr2), "+{v179}"(vXr3), "+{v168}"(vXh), "+{v186}"(vWd), "+{v170}"(vDum) : "{s20}"(w4352), "{s92}"(w1024), "{s96}"(rot64));
        asm volatile(
    "s_waitcnt lgkmcnt(0)\n\t"
    "s_mov_b32 s84, s50\n\t"
    "s_and_b32 s85, s51, 0xffff\n\t"
    "s_add_u32 s8, s8, s48\n\t"
    "s_addc_u32 s9, s49, 0\n\t"
    "s_and_b32 s9, s9, 0xffff\n\t"
            : "+{s[48:55]}"(ka), "+{s[8:11]}"(xd), "+{s[84:87]}"(wd) :: "scc");
        asm volatile(
    "s_mov_b32 s65, 0x100000\n\t"
    "s_mov_b32 s67, 0x200000\n\t"
    "s_mov_b32 s69, 0x300000\n\t"
    "s_add_u32 m0, s92, 83968\n\t"
    "s_nop 0\n\t"
    "buffer_load_dwordx4 v186, s[84:87], 0 offen offset:0 lds\n\t"
    "s_add_u32 m0, s92, 88064\n\t"
    "s_nop 0\n\t"
    "buffer_load_dwordx4 v186, s[84:87], s65 offen offset:0 lds\n\t"
    "s_add_u32 m0, s92, 92160\n\t"
    "s_nop 0\n\t"
    "buffer_load_dwordx4 v186, s[84:87], s67 offen offset:0 lds\n\t"
    "s_add_u32 m0, s92, 96256\n\t"
    "s_nop 0\n\t"
    "buffer_load_dwordx4 v186, s[84:87], s69 offen offset:0 lds\n\t"
    "s_add_u32 m0, s20, 44032\n\t"
    "s_nop 0\n\t"
    "buffer_load_dwordx4 v176, s[8:11], s96 offen offset:3072 lds\n\t"
    "s_add_u32 m0, s20, 45120\n\t"
    "s_nop 0\n\t"
    "buffer_load_dwordx4 v177, s[8:11], s96 offen offset:3072 lds\n\t"
    "s_add_u32 m0, s20, 46208\n\t"
    "s_nop 0\n\t"
    "buffer_load_dwordx4 v178, s[8:11], s96 offen offset:3072 lds\n\t"
    "s_add_u32 m0, s20, 47296\n\t"
    "s_nop 0\n\t"
    "buffer_load_dwordx4 v179, s[8:11], s96 offen offset:3072 lds\n\t"
    "buffer_load_dwordx4 v[136:139], v168, s[8:11], 0 offen offset:0\n\t"
    "s_add_u32 m0, s92, 99840\n\t"
    "s_nop 0\n\t"
    "buffer_load_dwordx4 v186, s[84:87], 0 offen offset:512 lds\n\t"
    "s_add_u32 m0, s92, 103936\n\t"
    "s_nop 0\n\t"
    "buffer_load_dwordx4 v186, s[84:87], s65 offen offset:512 lds\n\t"
    "s_add_u32 m0, s92, 108032\n\t"
    "s_nop 0\n\t"
    "buffer_load_dwordx4 v186, s[84:87], s67 offen offset:512 lds\n\t"
    "s_add_u32 m0, s92, 112128\n\t"
    "s_nop 0\n\t"
    "buffer_load_dwordx4 v186, s[84:87], s69 offen offset:512 lds\n\t"
            : "+{v176}"(vXr0), "+{v177}"(vXr1), "+{v178}"(vXr2), "+{v179}"(vXr3), "+{v168}"(vXh), "+{v186}"(vWd), "+{v170}"(vDum), "={v[136:139]}"(hx0), "+v"(l)
            : "{s20}"(w4352), "{s92}"(w1024), "{s96}"(rot64), "{s[8:11]}"(xd), "{s[84:87]}"(wd)
            : "memory", "scc", "s62", "s63", "s64", "s65", "s66", "s67", "s68", "s69", "s78", "s79", "s80", "s81");
        asm volatile("" : "+s"(bid), "+s"(w));
        const unsigned char* ws = (const unsigned char*)ka.z;
        const unsigned char* out = (const unsigned char*)ka.w;
        const unsigned nt = (bid >> 3) & 3, mt = (bid & 7) * 8 + (bid >> 5);
        const u32x4 od = desc((unsigned long long)(out + ((mt >> 5) * T_ + trow) * 512u), 125u * 4096u);
        const u32x4 hd = desc((unsigned long long)(ws + HS_OFF + nt * 32768), 16640u);
        const unsigned w2112 = w * 2112;
        const unsigned char* wso = ws + (forcew ? ROUND : 0u) + nt * 4 * STREAM;
        const unsigned char* wsp = wso + w * STREAM;
        const unsigned pm = w ? 0u : forcew ? 0xffffff00u : 0xfffffe00u;
        const unsigned r = l & 15, kq = l >> 4, q = l & 3, r1 = (r + 1) & 15, rho = l >> 3, r4 = (l >> 2) & 3;
        unsigned vXt1 = xoff(32 * w + 2 * (l & 4) + rho + 16, q * 16), vXt0 = (vXt1 - 65536) & 0xbfffffffu;
        const unsigned D = 32 * w + (l & 31), hk = (l >> 2) & 7;
        unsigned vXd = xoff(128, D * 4);
        unsigned vW = l * 16 + 4096, vL16 = WHI + l * 256 + (l & 15) * 16;
        unsigned vA0 = XR + r * 1088 + 16 * (kq ^ ((r >> 1) & 2));
        unsigned vA1 = XR + r1 * 1088 + 16 * (kq ^ ((r1 >> 1) & 2));
        unsigned vM15 = (r + 1) >> 4;
        unsigned vA2 = vA1 + vM15 * 17408;
        unsigned vB0 = (r >> 1) * 1056 + (r & 1) * 16 + kq * 256;
        unsigned vB1 = (r1 >> 1) * 1056 + (r1 & 1) * 16 + kq * (256 - 240 * vM15);
        const unsigned hs = 2 * (((hk >> 1) - (rot >> 1)) & 3) + ((hk ^ rot) & 1) + (l >> 5) * 8;
        unsigned vHwR = XR + 17408 + q * 16 + (hs & 1) * 64 + ((hs >> 1) + 1) % 3 * 18432 + hs / 6 * 256;
        unsigned vHwT = HALO + (D >> 4) * 64 + q * 16 + r4 * 4;
        unsigned vTr0 = kq * 1024 + r4 * 128 + ((2 * w + (q >> 1)) ^ (r4 & 2) ^ (4 * (kq & 1))) * 16 + (q & 1) * 8, vTr1 = vTr0 + 65536, vTr2 = vTr0 + 131072;
        const unsigned pkc = mt >> 3, pkq = (mt >> 1) & 3, pc = mt & 1;
        unsigned vP = (1024 * w + 32 * pkc + 8 * (rho >> 1) + 2 * pkq + (rho & 1)) * 1024 + pc * 512 + nt * 128 + (l & 7) * 16;
        unsigned vP3 = w == 3 ? OOB : vP;
        unsigned vPr = w1024 + r4 * 128 + kq * 32 + q * 8;
        unsigned vPs = kq * STREAM + (pkc * 40 + pc * 4 + w) * 1024 + (pkq * 16 + r) * 16;
        const unsigned off = w ? OOB : 0u;
        unsigned vQa = off + 16384 + l * 4, vCa = off + l * 256 + mt * 4, vRa = off + mt * 256 + l * 4, vQs = l ? OOB : off + 16384 + mt * 4;
        unsigned vOut = rho * 32768 + nt * 128 + (l & 7) * 16 - 4096;
        unsigned lim, mx, ew;
        asm volatile(
    "s_mov_b32 s62, 0x40000\n\t"
    "s_mov_b32 s63, 0x80000\n\t"
    "s_mov_b32 s64, 0xc0000\n\t"
    "s_mov_b32 s65, 0x100000\n\t"
    "s_mov_b32 s66, 0x1c0000\n\t"
    "s_mov_b32 s67, 0x200000\n\t"
    "s_mov_b32 s68, 0x2c0000\n\t"
    "s_mov_b32 s69, 0x300000\n\t"
    "v_xor_b32 v186, 0x8000, v186\n\t"
    "s_add_u32 m0, s92, 116736\n\t"
    "s_nop 0\n\t"
    "buffer_load_dwordx4 v186, s[84:87], 0 offen offset:0 lds\n\t"
    "s_add_u32 m0, s92, 120832\n\t"
    "s_nop 0\n\t"
    "buffer_load_dwordx4 v186, s[84:87], s65 offen offset:0 lds\n\t"
    "s_add_u32 m0, s92, 124928\n\t"
    "s_nop 0\n\t"
    "buffer_load_dwordx4 v186, s[84:87], s67 offen offset:0 lds\n\t"
    "s_add_u32 m0, s92, 129024\n\t"
    "s_nop 0\n\t"
    "buffer_load_dwordx4 v186, s[84:87], s69 offen offset:0 lds\n\t"
    "s_add_u32 m0, s92, 132608\n\t"
    "s_nop 0\n\t"
    "buffer_load_dwordx4 v186, s[84:87], 0 offen offset:512 lds\n\t"
    "s_add_u32 m0, s92, 136704\n\t"
    "s_nop 0\n\t"
    "buffer_load_dwordx4 v186, s[84:87], s65 offen offset:512 lds\n\t"
    "s_add_u32 m0, s92, 140800\n\t"
    "s_nop 0\n\t"
    "buffer_load_dwordx4 v186, s[84:87], s67 offen offset:512 lds\n\t"
    "s_add_u32 m0, s92, 144896\n\t"
    "s_nop 0\n\t"
    "buffer_load_dwordx4 v186, s[84:87], s69 offen offset:512 lds\n\t"
    "s_mov_b32 s24, 0x7fff7fff\n\t"
    "v_mov_b32 v249, 0\n\t"
    "s_waitcnt vmcnt(12)\n\t"
    "ds_write_b128 v169, v[136:139]\n\t"
    "s_waitcnt lgkmcnt(0)\n\t"
    "s_barrier\n\t"
    "ds_read_b64_tr_b16 a[0:1], v188 offset:18432\n\t"
    "ds_read_b64_tr_b16 a[2:3], v188 offset:18944\n\t"
    "ds_read_b64_tr_b16 a[4:5], v188 offset:22528\n\t"
    "ds_read_b64_tr_b16 a[6:7], v188 offset:23040\n\t"
    "ds_read_b64_tr_b16 a[8:9], v188 offset:26624\n\t"
    "ds_read_b64_tr_b16 a[10:11], v188 offset:27136\n\t"
    "ds_read_b64_tr_b16 a[12:13], v188 offset:30720\n\t"
    "ds_read_b64_tr_b16 a[14:15], v188 offset:31232\n\t"
    "ds_read_b128 a[208:211], v181 offset:18432\n\t"
    "ds_read_b128 a[212:215], v181 offset:18560\n\t"
    "ds_read_b128 a[216:219], v181 offset:18688\n\t"
    "ds_read_b128 a[220:223], v181 offset:18816\n\t"
    "ds_read_b128 a[224:227], v181 offset:18944\n\t"
    "ds_read_b128 a[228:231], v181 offset:19072\n\t"
    "ds_read_b128 a[232:235], v181 offset:19200\n\t"
    "ds_read_b128 a[236:239], v181 offset:19328\n\t"
    "ds_read_b128 a[240:243], v183 offset:18432\n\t"
    "ds_read_b128 a[244:247], v182 offset:18560\n\t"
    "ds_read_b128 a[248:251], v182 offset:18688\n\t"
    "s_waitcnt lgkmcnt(6)\n\t"
    ".p2align 3\n\t"
    "v_mfma_f32_16x16x32_bf16 v[72:75], a[0:3], a[208:211], 0\n\t"
    "s_mov_b32 s27, 256\n\t"
    "s_add_u32 s57, s96, 128\n\t"
    "v_mfma_f32_16x16x32_bf16 v[72:75], a[4:7], a[212:215], v[72:75]\n\t"
    "s_and_b32 s57, s57, 0x1c0\n\t"
    "s_add_u32 m0, s20, 62464\n\t"
    "v_mfma_f32_16x16x32_bf16 v[72:75], a[8:11], a[216:219], v[72:75]\n\t"
    "ds_read_b128 v[140:143], v181 offset:18944\n\t"
    "v_mfma_f32_16x16x32_bf16 v[72:75], a[12:15], a[220:223], v[72:75]\n\t"
    "buffer_load_dwordx4 v176, s[8:11], s57 offen offset:3072 lds\n\t"
    "v_mfma_f32_16x16x32_bf16 v[80:83], a[12:15], a[224:227], 0\n\t"
    "ds_read_b128 v[216:219], v175\n\t"
    "v_mfma_f32_16x16x32_bf16 v[80:83], a[8:11], a[220:223], v[80:83]\n\t"
    "s_add_u32 m0, s20, 63552\n\t"
    "v_mfma_f32_16x16x32_bf16 v[80:83], a[4:7], a[216:219], v[80:83]\n\t"
    "buffer_load_dwordx4 v177, s[8:11], s57 offen offset:3072 lds\n\t"
    "v_mfma_f32_16x16x32_bf16 v[80:83], a[0:3], a[212:215], v[80:83]\n\t"
    "s_waitcnt lgkmcnt(6)\n\t"
    "s_nop 0\n\t"
    "v_mfma_f32_16x16x32_bf16 v[88:91], a[0:3], a[216:219], 0\n\t"
    "s_add_u32 m0, s20, 64640\n\t"
    "v_mfma_f32_16x16x32_bf16 v[88:91], a[4:7], a[220:223], v[88:91]\n\t"
    "buffer_load_dwordx4 v178, s[8:11], s57 offen offset:3072 lds\n\t"
    "v_mfma_f32_16x16x32_bf16 v[88:91], a[8:11], a[224:227], v[88:91]\n\t"
    "v_mfma_f32_16x16x32_bf16 v[88:91], a[12:15], a[228:231], v[88:91]\n\t"
    "s_add_u32 m0, s20, 65728\n\t"
    "v_mfma_f32_16x16x32_bf16 v[96:99], a[12:15], a[232:235], 0\n\t"
    "buffer_load_dwordx4 v179, s[8:11], s57 offen offset:3072 lds\n\t"
    "v_mfma_f32_16x16x32_bf16 v[96:99], a[8:11], a[228:231], v[96:99]\n\t"
    "s_waitcnt vmcnt(12) lgkmcnt(0)\n\t"
    "s_barrier\n\t"
    "v_mfma_f32_16x16x32_bf16 v[96:99], a[4:7], a[224:227], v[96:99]\n\t"
    "ds_read_b64_tr_b16 a[16:17], v188 offset:34816\n\t"
    "ds_read_b64_tr_b16 a[18:19], v188 offset:35328\n\t"
    "v_mfma_f32_16x16x32_bf16 v[96:99], a[0:3], a[220:223], v[96:99]\n\t"
    "s_add_u32 m0, s92, 0\n\t"
    "v_pk_maximum3_f16 v185, v136, v136, 0 neg_lo:[0,1,0] neg_hi:[0,1,0]\n\t"
    "v_mfma_f32_16x16x32_bf16 v[104:107], a[0:3], a[224:227], 0\n\t"
    "buffer_load_dwordx4 v172, s[84:87], 0 offen offset:0 lds\n\t"
    "v_mfma_f32_16x16x32_bf16 v[104:107], a[4:7], a[228:231], v[104:107]\n\t"
    "ds_read_b64_tr_b16 a[20:21], v188 offset:38912\n\t"
    "ds_read_b64_tr_b16 a[22:23], v188 offset:39424\n\t"
    "v_mfma_f32_16x16x32_bf16 v[104:107], a[8:11], a[232:235], v[104:107]\n\t"
    "s_add_u32 m0, s92, 4096\n\t"
    "v_pk_maximum3_f16 v185, v137, v137, v185 neg_lo:[0,1,0] neg_hi:[0,1,0]\n\t"
    "v_mfma_f32_16x16x32_bf16 v[104:107], a[12:15], a[236:239], v[104:107]\n\t"
    "buffer_load_dwordx4 v172, s[84:87], s62 offen offset:0 lds\n\t"
    "v_mfma_f32_16x16x32_bf16 v[112:115], a[12:15], a[240:243], 0\n\t"
    "ds_read_b64_tr_b16 a[24:25], v188 offset:43008\n\t"
    "ds_read_b64_tr_b16 a[26:27], v188 offset:43520\n\t"
    "v_mfma_f32_16x16x32_bf16 v[112:115], a[8:11], a[236:239], v[112:115]\n\t"
    "s_add_u32 m0, s92, 8192\n\t"
    "v_pk_maximum3_f16 v185, v138, v138, v185 neg_lo:[0,1,0] neg_hi:[0,1,0]\n\t"
    "v_mfma_f32_16x16x32_bf16 v[112:115], a[4:7], a[232:235], v[112:115]\n\t"
    "buffer_load_dwordx4 v172, s[84:87], s63 offen offset:0 lds\n\t"
    "v_mfma_f32_16x16x32_bf16 v[112:115], a[0:3], a[228:231], v[112:115]\n\t"
    "ds_read_b64_tr_b16 a[28:29], v188 offset:47104\n\t"
    "ds_read_b64_tr_b16 a[30:31], v188 offset:47616\n\t"
    "v_mfma_f32_16x16x32_bf16 v[120:123], a[0:3], a[232:235], 0\n\t"
    "s_add_u32 m0, s92, 12288\n\t"
    "v_pk_maximum3_f16 v185, v139, v139, v185 neg_lo:[0,1,0] neg_hi:[0,1,0]\n\t"
    "v_mfma_f32_16x16x32_bf16 v[120:123], a[4:7], a[236:239], v[120:123]\n\t"
    "buffer_load_dwordx4 v173, s[84:87], s64 offen offset:0 lds\n\t"
    "v_mfma_f32_16x16x32_bf16 v[120:123], a[8:11], a[240:243], v[120:123]\n\t"
    "v_pk_maximum3_f16 v185, v140, v140, v185 neg_lo:[0,1,0] neg_hi:[0,1,0]\n\t"
    "v_pk_maximum3_f16 v185, v141, v141, v185 neg_lo:[0,1,0] neg_hi:[0,1,0]\n\t"
    "v_mfma_f32_16x16x32_bf16 v[120:123], a[12:15], a[244:247], v[120:123]\n\t"
    "v_xor_b32 v186, 0x8000, v186\n\t"
    "v_add_u32 v186, 0x10000, v186\n\t"
    "v_mfma_f32_16x16x32_bf16 v[128:131], a[12:15], a[248:251], 0\n\t"
    "v_and_b32 v186, 0x3ffff, v186\n\t"
    "s_add_u32 m0, s92, 149504\n\t"
    "v_mfma_f32_16x16x32_bf16 v[128:131], a[8:11], a[244:247], v[128:131]\n\t"
    "buffer_load_dwordx4 v186, s[84:87], 0 offen offset:0 lds\n\t"
    "v_mfma_f32_16x16x32_bf16 v[128:131], a[4:7], a[240:243], v[128:131]\n\t"
    "v_pk_maximum3_f16 v185, v142, v142, v185 neg_lo:[0,1,0] neg_hi:[0,1,0]\n\t"
    "v_pk_maximum3_f16 v185, v143, v143, v185 neg_lo:[0,1,0] neg_hi:[0,1,0]\n\t"
    "v_mfma_f32_16x16x32_bf16 v[128:131], a[0:3], a[236:239], v[128:131]\n\t"
    "s_waitcnt vmcnt(13) lgkmcnt(0)\n\t"
    "s_barrier\n\t"
    "v_mfma_f32_16x16x32_bf16 v[76:79], a[16:19], a[208:211], 0\n\t"
    "ds_read_b128 a[208:211], v181 offset:18496\n\t"
    "s_add_u32 m0, s92, 153600\n\t"
    "v_mfma_f32_16x16x32_bf16 v[76:79], a[20:23], a[212:215], v[76:79]\n\t"
    "ds_read_b64_tr_b16 a[0:1], v188 offset:51200\n\t"
    "ds_read_b64_tr_b16 a[2:3], v188 offset:51712\n\t"
    "v_mfma_f32_16x16x32_bf16 v[76:79], a[24:27], a[216:219], v[76:79]\n\t"
    "buffer_load_dwordx4 v186, s[84:87], s65 offen offset:0 lds\n\t"
    "v_mfma_f32_16x16x32_bf16 v[76:79], a[28:31], a[220:223], v[76:79]\n\t"
    "v_lshrrev_b32 v191, 16, v185\n\t"
    "v_and_b32 v185, 0xffff, v185\n\t"
    "v_mfma_f32_16x16x32_bf16 v[84:87], a[28:31], a[224:227], 0\n\t"
    "s_add_u32 m0, s92, 157696\n\t"
    "v_max_u32_e64 v185, v185, v191\n\t"
    "v_mfma_f32_16x16x32_bf16 v[84:87], a[24:27], a[220:223], v[84:87]\n\t"
    "buffer_load_dwordx4 v186, s[84:87], s67 offen offset:0 lds\n\t"
    "v_mfma_f32_16x16x32_bf16 v[84:87], a[20:23], a[216:219], v[84:87]\n\t"
    "v_max_u32_dpp v185, v185, v185 row_ror:8 row_mask:0xf bank_mask:0xf\n\t"
    "v_mfma_f32_16x16x32_bf16 v[84:87], a[16:19], a[212:215], v[84:87]\n\t"
    "ds_read_b128 a[212:215], v181 offset:18624\n\t"
    "s_add_u32 m0, s92, 16384\n\t"
    "v_mfma_f32_16x16x32_bf16 v[92:95], a[16:19], a[216:219], 0\n\t"
    "ds_read_b128 a[216:219], v181 offset:18752\n\t"
    "v_max_u32_dpp v185, v185, v185 row_ror:4 row_mask:0xf bank_mask:0xf\n\t"
    "v_mfma_f32_16x16x32_bf16 v[92:95], a[20:23], a[220:223], v[92:95]\n\t"
    "ds_read_b64_tr_b16 a[4:5], v188 offset:55296\n\t"
    "ds_read_b64_tr_b16 a[6:7], v188 offset:55808\n\t"
    "v_mfma_f32_16x16x32_bf16 v[92:95], a[24:27], a[224:227], v[92:95]\n\t"
    "buffer_load_dwordx4 v186, s[84:87], s69 offen offset:0 lds\n\t"
    "v_mfma_f32_16x16x32_bf16 v[92:95], a[28:31], a[228:231], v[92:95]\n\t"
    "v_max_u32_dpp v185, v185, v185 row_ror:2 row_mask:0xf bank_mask:0xf\n\t"
    "v_mfma_f32_16x16x32_bf16 v[100:103], a[28:31], a[232:235], 0\n\t"
    "s_add_u32 m0, s92, 19968\n\t"
    "v_mfma_f32_16x16x32_bf16 v[100:103], a[24:27], a[228:231], v[100:103]\n\t"
    "ds_read_b64_tr_b16 a[8:9], v188 offset:59392\n\t"
    "ds_read_b64_tr_b16 a[10:11], v188 offset:59904\n\t"
    "v_mfma_f32_16x16x32_bf16 v[100:103], a[20:23], a[224:227], v[100:103]\n\t"
    "buffer_load_dwordx4 v186, s[84:87], 0 offen offset:512 lds\n\t"
    "v_mfma_f32_16x16x32_bf16 v[100:103], a[16:19], a[220:223], v[100:103]\n\t"
    "ds_read_b128 a[220:223], v181 offset:18880\n\t"
    "v_max_u32_dpp v185, v185, v185 row_ror:1 row_mask:0xf bank_mask:0xf\n\t"
    "v_mfma_f32_16x16x32_bf16 v[108:111], a[16:19], a[224:227], 0\n\t"
    "ds_read_b128 a[224:227], v181 offset:19008\n\t"
    "s_add_u32 m0, s92, 24064\n\t"
    "v_mfma_f32_16x16x32_bf16 v[108:111], a[20:23], a[228:231], v[108:111]\n\t"
    "buffer_load_dwordx4 v186, s[84:87], s65 offen offset:512 lds\n\t"
    "v_mfma_f32_16x16x32_bf16 v[108:111], a[24:27], a[232:235], v[108:111]\n\t"
    "v_readlane_b32 s36, v185, 0\n\t"
    "v_readlane_b32 s37, v185, 16\n\t"
    "v_mfma_f32_16x16x32_bf16 v[108:111], a[28:31], a[236:239], v[108:111]\n\t"
    "ds_read_b64_tr_b16 a[12:13], v188 offset:63488\n\t"
    "ds_read_b64_tr_b16 a[14:15], v188 offset:64000\n\t"
    "v_mfma_f32_16x16x32_bf16 v[116:119], a[28:31], a[240:243], 0\n\t"
    "buffer_load_dword v197, v193, s[88:91], 0 offen offset:0 sc1\n\t"
    "v_readlane_b32 s38, v185, 32\n\t"
    "v_mfma_f32_16x16x32_bf16 v[116:119], a[24:27], a[236:239], v[116:119]\n\t"
    "v_readlane_b32 s39, v185, 48\n\t"
    "v_mfma_f32_16x16x32_bf16 v[116:119], a[20:23], a[232:235], v[116:119]\n\t"
    "v_mfma_f32_16x16x32_bf16 v[116:119], a[16:19], a[228:231], v[116:119]\n\t"
    "ds_read_b128 a[228:231], v181 offset:19136\n\t"
    "s_max_u32 s29, s36, s37\n\t"
    "v_mfma_f32_16x16x32_bf16 v[124:127], a[16:19], a[232:235], 0\n\t"
    "ds_read_b128 a[232:235], v181 offset:19264\n\t"
    "s_max_u32 s29, s29, s38\n\t"
    "v_mfma_f32_16x16x32_bf16 v[124:127], a[20:23], a[236:239], v[124:127]\n\t"
    "buffer_load_dword v250, v194, s[88:91], 0 offen offset:0 sc1\n\t"
    "s_max_u32 s29, s29, s39\n\t"
    "v_mfma_f32_16x16x32_bf16 v[124:127], a[24:27], a[240:243], v[124:127]\n\t"
    "s_nop 0\n\t"
    "v_mfma_f32_16x16x32_bf16 v[124:127], a[28:31], a[244:247], v[124:127]\n\t"
    "s_add_u32 m0, s92, 83456\n\t"
    "v_mfma_f32_16x16x32_bf16 v[132:135], a[28:31], a[248:251], 0\n\t"
    "ds_read_b128 a[248:251], v182 offset:18752\n\t"
    "v_mfma_f32_16x16x32_bf16 v[132:135], a[24:27], a[244:247], v[132:135]\n\t"
    "ds_read_b128 a[244:247], v182 offset:18624\n\t"
    "v_mfma_f32_16x16x32_bf16 v[132:135], a[20:23], a[240:243], v[132:135]\n\t"
    "ds_read_b128 a[240:243], v183 offset:18496\n\t"
    "v_mfma_f32_16x16x32_bf16 v[132:135], a[16:19], a[236:239], v[132:135]\n\t"
    "s_waitcnt vmcnt(16) lgkmcnt(5)\n\t"
    "s_barrier\n\t"
    "v_mfma_f32_16x16x32_bf16 v[72:75], a[0:3], a[208:211], v[72:75]\n\t"
    "ds_read_b128 a[236:239], v181 offset:19392\n\t"
    "v_mfma_f32_16x16x32_bf16 v[72:75], a[4:7], a[212:215], v[72:75]\n\t"
    "ds_read_b64_tr_b16 a[16:17], v189 offset:2048\n\t"
    "ds_read_b64_tr_b16 a[18:19], v189 offset:2560\n\t"
    "v_mfma_f32_16x16x32_bf16 v[72:75], a[8:11], a[216:219], v[72:75]\n\t"
    "buffer_load_dwordx4 v186, s[84:87], s67 offen offset:512 lds\n\t"
    "v_mfma_f32_16x16x32_bf16 v[72:75], a[12:15], a[220:223], v[72:75]\n\t"
    "v_mfma_f32_16x16x32_bf16 v[80:83], a[12:15], a[224:227], v[80:83]\n\t"
    "s_add_u32 m0, s92, 87552\n\t"
    "v_mfma_f32_16x16x32_bf16 v[80:83], a[8:11], a[220:223], v[80:83]\n\t"
    "buffer_load_dwordx4 v186, s[84:87], s69 offen offset:512 lds\n\t"
    "v_mfma_f32_16x16x32_bf16 v[80:83], a[4:7], a[216:219], v[80:83]\n\t"
    "v_mfma_f32_16x16x32_bf16 v[80:83], a[0:3], a[212:215], v[80:83]\n\t"
    "s_waitcnt lgkmcnt(6)\n\t"
    "ds_read_b64_tr_b16 a[20:21], v189 offset:6144\n\t"
    "v_mfma_f32_16x16x32_bf16 v[88:91], a[0:3], a[216:219], v[88:91]\n\t"
    "ds_read_b64_tr_b16 a[22:23], v189 offset:6656\n\t"
    "v_xor_b32 v186, 0x8000, v186\n\t"
    "v_mfma_f32_16x16x32_bf16 v[88:91], a[4:7], a[220:223], v[88:91]\n\t"
    "s_add_u32 m0, s92, 92160\n\t"
    "s_nop 0\n\t"
    "v_mfma_f32_16x16x32_bf16 v[88:91], a[8:11], a[224:227], v[88:91]\n\t"
    "buffer_load_dwordx4 v186, s[84:87], 0 offen offset:0 lds\n\t"
    "v_mfma_f32_16x16x32_bf16 v[88:91], a[12:15], a[228:231], v[88:91]\n\t"
    "v_mfma_f32_16x16x32_bf16 v[96:99], a[12:15], a[232:235], v[96:99]\n\t"
    "s_add_u32 m0, s92, 96256\n\t"
    "v_mfma_f32_16x16x32_bf16 v[96:99], a[8:11], a[228:231], v[96:99]\n\t"
    "s_waitcnt lgkmcnt(4)\n\t"
    "ds_read_b64_tr_b16 a[24:25], v189 offset:10240\n\t"
    "v_mfma_f32_16x16x32_bf16 v[96:99], a[4:7], a[224:227], v[96:99]\n\t"
    "ds_read_b64_tr_b16 a[26:27], v189 offset:10752\n\t"
    "s_nop 0\n\t"
    "v_mfma_f32_16x16x32_bf16 v[96:99], a[0:3], a[220:223], v[96:99]\n\t"
    "buffer_load_dwordx4 v186, s[84:87], s65 offen offset:0 lds\n\t"
    "v_mfma_f32_16x16x32_bf16 v[104:107], a[0:3], a[224:227], v[104:107]\n\t"
    "v_mfma_f32_16x16x32_bf16 v[104:107], a[4:7], a[228:231], v[104:107]\n\t"
    "s_add_u32 s57, s57, 128\n\t"
    "s_and_b32 s57, s57, 0x1c0\n\t"
    "v_mfma_f32_16x16x32_bf16 v[104:107], a[8:11], a[232:235], v[104:107]\n\t"
    "s_add_u32 m0, s20, 25600\n\t"
    "v_mfma_f32_16x16x32_bf16 v[104:107], a[12:15], a[236:239], v[104:107]\n\t"
    "ds_read_b64_tr_b16 a[28:29], v189 offset:14336\n\t"
    "ds_read_b64_tr_b16 a[30:31], v189 offset:14848\n\t"
    "v_mfma_f32_16x16x32_bf16 v[112:115], a[12:15], a[240:243], v[112:115]\n\t"
    "buffer_load_dwordx4 v176, s[8:11], s57 offen offset:3072 lds\n\t"
    "v_mfma_f32_16x16x32_bf16 v[112:115], a[8:11], a[236:239], v[112:115]\n\t"
    "v_mfma_f32_16x16x32_bf16 v[112:115], a[4:7], a[232:235], v[112:115]\n\t"
    "s_add_u32 m0, s20, 26688\n\t"
    "v_mfma_f32_16x16x32_bf16 v[112:115], a[0:3], a[228:231], v[112:115]\n\t"
    "buffer_load_dwordx4 v177, s[8:11], s57 offen offset:3072 lds\n\t"
    "v_mfma_f32_16x16x32_bf16 v[120:123], a[0:3], a[232:235], v[120:123]\n\t"
    "v_mfma_f32_16x16x32_bf16 v[120:123], a[4:7], a[236:239], v[120:123]\n\t"
    "s_add_u32 m0, s20, 27776\n\t"
    "v_mfma_f32_16x16x32_bf16 v[120:123], a[8:11], a[240:243], v[120:123]\n\t"
    "buffer_load_dwordx4 v178, s[8:11], s57 offen offset:3072 lds\n\t"
    "v_mfma_f32_16x16x32_bf16 v[120:123], a[12:15], a[244:247], v[120:123]\n\t"
    "v_mfma_f32_16x16x32_bf16 v[128:131], a[12:15], a[248:251], v[128:131]\n\t"
    "s_add_u32 m0, s20, 28864\n\t"
    "v_mfma_f32_16x16x32_bf16 v[128:131], a[8:11], a[244:247], v[128:131]\n\t"
    "buffer_load_dwordx4 v179, s[8:11], s57 offen offset:3072 lds\n\t"
    "v_mfma_f32_16x16x32_bf16 v[128:131], a[4:7], a[240:243], v[128:131]\n\t"
    "v_mfma_f32_16x16x32_bf16 v[128:131], a[0:3], a[236:239], v[128:131]\n\t"
    "s_waitcnt vmcnt(12) lgkmcnt(0)\n\t"
    "s_barrier\n\t"
    "v_mfma_f32_16x16x32_bf16 v[76:79], a[16:19], a[208:211], v[76:79]\n\t"
    "ds_read_b128 a[208:211], v181 offset:36864\n\t"
    "s_add_u32 m0, s92, 100352\n\t"
    "v_mfma_f32_16x16x32_bf16 v[76:79], a[20:23], a[212:215], v[76:79]\n\t"
    "ds_read_b64_tr_b16 a[0:1], v189 offset:18432\n\t"
    "ds_read_b64_tr_b16 a[2:3], v189 offset:18944\n\t"
    "v_mfma_f32_16x16x32_bf16 v[76:79], a[24:27], a[216:219], v[76:79]\n\t"
    "buffer_load_dwordx4 v186, s[84:87], s67 offen offset:0 lds\n\t"
    "v_mfma_f32_16x16x32_bf16 v[76:79], a[28:31], a[220:223], v[76:79]\n\t"
    "v_mfma_f32_16x16x32_bf16 v[84:87], a[28:31], a[224:227], v[84:87]\n\t"
    "s_add_u32 m0, s92, 104448\n\t"
    "v_mfma_f32_16x16x32_bf16 v[84:87], a[24:27], a[220:223], v[84:87]\n\t"
    "buffer_load_dwordx4 v186, s[84:87], s69 offen offset:0 lds\n\t"
    "v_mfma_f32_16x16x32_bf16 v[84:87], a[20:23], a[216:219], v[84:87]\n\t"
    "v_mfma_f32_16x16x32_bf16 v[84:87], a[16:19], a[212:215], v[84:87]\n\t"
    "ds_read_b128 a[212:215], v181 offset:36992\n\t"
    "s_add_u32 m0, s92, 108032\n\t"
    "v_mfma_f32_16x16x32_bf16 v[92:95], a[16:19], a[216:219], v[92:95]\n\t"
    "ds_read_b128 a[216:219], v181 offset:37120\n\t"
    "v_mfma_f32_16x16x32_bf16 v[92:95], a[20:23], a[220:223], v[92:95]\n\t"
    "ds_read_b64_tr_b16 a[4:5], v189 offset:22528\n\t"
    "ds_read_b64_tr_b16 a[6:7], v189 offset:23040\n\t"
    "v_mfma_f32_16x16x32_bf16 v[92:95], a[24:27], a[224:227], v[92:95]\n\t"
    "buffer_load_dwordx4 v186, s[84:87], 0 offen offset:512 lds\n\t"
    "v_mfma_f32_16x16x32_bf16 v[92:95], a[28:31], a[228:231], v[92:95]\n\t"
    "v_mfma_f32_16x16x32_bf16 v[100:103], a[28:31], a[232:235], v[100:103]\n\t"
    "s_add_u32 m0, s92, 112128\n\t"
    "v_mfma_f32_16x16x32_bf16 v[100:103], a[24:27], a[228:231], v[100:103]\n\t"
    "ds_read_b64_tr_b16 a[8:9], v189 offset:26624\n\t"
    "ds_read_b64_tr_b16 a[10:11], v189 offset:27136\n\t"
    "v_mfma_f32_16x16x32_bf16 v[100:103], a[20:23], a[224:227], v[100:103]\n\t"
    "buffer_load_dwordx4 v186, s[84:87], s65 offen offset:512 lds\n\t"
    "v_mfma_f32_16x16x32_bf16 v[100:103], a[16:19], a[220:223], v[100:103]\n\t"
    "ds_read_b128 a[220:223], v181 offset:37248\n\t"
    "v_mfma_f32_16x16x32_bf16 v[108:111], a[16:19], a[224:227], v[108:111]\n\t"
    "ds_read_b128 a[224:227], v181 offset:37376\n\t"
    "s_add_u32 m0, s92, 116224\n\t"
    "v_mfma_f32_16x16x32_bf16 v[108:111], a[20:23], a[228:231], v[108:111]\n\t"
    "buffer_load_dwordx4 v186, s[84:87], s67 offen offset:512 lds\n\t"
    "v_mfma_f32_16x16x32_bf16 v[108:111], a[24:27], a[232:235], v[108:111]\n\t"
    "v_mfma_f32_16x16x32_bf16 v[108:111], a[28:31], a[236:239], v[108:111]\n\t"
    "ds_read_b64_tr_b16 a[12:13], v187 offset:16384\n\t"
    "ds_read_b64_tr_b16 a[14:15], v187 offset:16896\n\t"
    "v_mfma_f32_16x16x32_bf16 v[116:119], a[28:31], a[240:243], v[116:119]\n\t"
    "s_add_u32 m0, s92, 120320\n\t"
    "v_mfma_f32_16x16x32_bf16 v[116:119], a[24:27], a[236:239], v[116:119]\n\t"
    "buffer_load_dwordx4 v186, s[84:87], s69 offen offset:512 lds\n\t"
    "v_mfma_f32_16x16x32_bf16 v[116:119], a[20:23], a[232:235], v[116:119]\n\t"
    "v_mfma_f32_16x16x32_bf16 v[116:119], a[16:19], a[228:231], v[116:119]\n\t"
    "ds_read_b128 a[228:231], v181 offset:37504\n\t"
    "v_xor_b32 v186, 0x8000, v186\n\t"
    "v_mfma_f32_16x16x32_bf16 v[124:127], a[16:19], a[232:235], v[124:127]\n\t"
    "ds_read_b128 a[232:235], v181 offset:37632\n\t"
    "v_add_u32 v186, 0x10000, v186\n\t"
    "v_mfma_f32_16x16x32_bf16 v[124:127], a[20:23], a[236:239], v[124:127]\n\t"
    "v_and_b32 v186, 0x3ffff, v186\n\t"
    "s_add_u32 m0, s92, 124928\n\t"
    "v_mfma_f32_16x16x32_bf16 v[124:127], a[24:27], a[240:243], v[124:127]\n\t"
    "buffer_load_dwordx4 v186, s[84:87], 0 offen offset:0 lds\n\t"
    "v_mfma_f32_16x16x32_bf16 v[124:127], a[28:31], a[244:247], v[124:127]\n\t"
    "v_mfma_f32_16x16x32_bf16 v[132:135], a[28:31], a[248:251], v[132:135]\n\t"
    "ds_read_b128 a[248:251], v182 offset:37120\n\t"
    "s_add_u32 m0, s92, 129024\n\t"
    "v_mfma_f32_16x16x32_bf16 v[132:135], a[24:27], a[244:247], v[132:135]\n\t"
    "ds_read_b128 a[244:247], v182 offset:36992\n\t"
    "v_mfma_f32_16x16x32_bf16 v[132:135], a[20:23], a[240:243], v[132:135]\n\t"
    "ds_read_b128 a[240:243], v183 offset:36864\n\t"
    "v_mfma_f32_16x16x32_bf16 v[132:135], a[16:19], a[236:239], v[132:135]\n\t"
    "s_waitcnt vmcnt(13) lgkmcnt(5)\n\t"
    "s_barrier\n\t"
    "v_mfma_f32_16x16x32_bf16 v[72:75], a[0:3], a[208:211], v[72:75]\n\t"
    "ds_read_b128 a[236:239], v181 offset:37760\n\t"
    "v_pk_maximum3_f16 v185, v216, v216, 0 neg_lo:[0,1,0] neg_hi:[0,1,0]\n\t"
    "v_mfma_f32_16x16x32_bf16 v[72:75], a[4:7], a[212:215], v[72:75]\n\t"
    "ds_read_b64_tr_b16 a[16:17], v187 offset:20480\n\t"
    "ds_read_b64_tr_b16 a[18:19], v187 offset:20992\n\t"
    "v_mfma_f32_16x16x32_bf16 v[72:75], a[8:11], a[216:219], v[72:75]\n\t"
    "buffer_load_dwordx4 v186, s[84:87], s65 offen offset:0 lds\n\t"
    "v_mfma_f32_16x16x32_bf16 v[72:75], a[12:15], a[220:223], v[72:75]\n\t"
    "v_pk_maximum3_f16 v185, v217, v217, v185 neg_lo:[0,1,0] neg_hi:[0,1,0]\n\t"
    "v_pk_maximum3_f16 v185, v218, v218, v185 neg_lo:[0,1,0] neg_hi:[0,1,0]\n\t"
    "v_mfma_f32_16x16x32_bf16 v[80:83], a[12:15], a[224:227], v[80:83]\n\t"
    "s_add_u32 m0, s92, 133120\n\t"
    "v_mfma_f32_16x16x32_bf16 v[80:83], a[8:11], a[220:223], v[80:83]\n\t"
    "buffer_load_dwordx4 v186, s[84:87], s67 offen offset:0 lds\n\t"
    "v_mfma_f32_16x16x32_bf16 v[80:83], a[4:7], a[216:219], v[80:83]\n\t"
    "v_pk_maximum3_f16 v185, v219, v219, v185 neg_lo:[0,1,0] neg_hi:[0,1,0]\n\t"
    "v_mfma_f32_16x16x32_bf16 v[80:83], a[0:3], a[212:215], v[80:83]\n\t"
    "s_waitcnt lgkmcnt(6)\n\t"
    "ds_read_b64_tr_b16 a[20:21], v187 offset:24576\n\t"
    "v_mfma_f32_16x16x32_bf16 v[88:91], a[0:3], a[216:219], v[88:91]\n\t"
    "ds_read_b64_tr_b16 a[22:23], v187 offset:25088\n\t"
    "s_add_u32 m0, s92, 137216\n\t"
    "v_mfma_f32_16x16x32_bf16 v[88:91], a[4:7], a[220:223], v[88:91]\n\t"
    "buffer_load_dwordx4 v186, s[84:87], s69 offen offset:0 lds\n\t"
    "v_mfma_f32_16x16x32_bf16 v[88:91], a[8:11], a[224:227], v[88:91]\n\t"
    "v_lshrrev_b32 v191, 16, v185\n\t"
    "v_and_b32 v185, 0xffff, v185\n\t"
    "v_mfma_f32_16x16x32_bf16 v[88:91], a[12:15], a[228:231], v[88:91]\n\t"
    "s_add_u32 m0, s92, 140800\n\t"
    "v_max_u32_e64 v185, v185, v191\n\t"
    "v_mfma_f32_16x16x32_bf16 v[96:99], a[12:15], a[232:235], v[96:99]\n\t"
    "buffer_load_dwordx4 v186, s[84:87], 0 offen offset:512 lds\n\t"
    "v_mfma_f32_16x16x32_bf16 v[96:99], a[8:11], a[228:231], v[96:99]\n\t"
    "s_waitcnt lgkmcnt(4)\n\t"
    "ds_read_b64_tr_b16 a[24:25], v188 offset:18432\n\t"
    "v_mfma_f32_16x16x32_bf16 v[96:99], a[4:7], a[224:227], v[96:99]\n\t"
    "ds_read_b64_tr_b16 a[26:27], v188 offset:18944\n\t"
    "s_add_u32 m0, s92, 144896\n\t"
    "v_mfma_f32_16x16x32_bf16 v[96:99], a[0:3], a[220:223], v[96:99]\n\t"
    "buffer_load_dwordx4 v186, s[84:87], s65 offen offset:512 lds\n\t"
    "v_mfma_f32_16x16x32_bf16 v[104:107], a[0:3], a[224:227], v[104:107]\n\t"
    "v_max_u32_dpp v185, v185, v185 row_ror:8 row_mask:0xf bank_mask:0xf\n\t"
    "s_nop 0\n\t"
    "v_mfma_f32_16x16x32_bf16 v[104:107], a[4:7], a[228:231], v[104:107]\n\t"
    "s_add_u32 s57, s57, 128\n\t"
    "s_and_b32 s57, s57, 0x1c0\n\t"
    "v_mfma_f32_16x16x32_bf16 v[104:107], a[8:11], a[232:235], v[104:107]\n\t"
    "s_add_u32 m0, s20, 44032\n\t"
    "v_max_u32_dpp v185, v185, v185 row_ror:4 row_mask:0xf bank_mask:0xf\n\t"
    "v_mfma_f32_16x16x32_bf16 v[104:107], a[12:15], a[236:239], v[104:107]\n\t"
    "ds_read_b64_tr_b16 a[28:29], v188 offset:22528\n\t"
    "ds_read_b64_tr_b16 a[30:31], v188 offset:23040\n\t"
    "v_mfma_f32_16x16x32_bf16 v[112:115], a[12:15], a[240:243], v[112:115]\n\t"
    "buffer_load_dwordx4 v176, s[8:11], s57 offen offset:3072 lds\n\t"
    "v_mfma_f32_16x16x32_bf16 v[112:115], a[8:11], a[236:239], v[112:115]\n\t"
    "v_max_u32_dpp v185, v185, v185 row_ror:2 row_mask:0xf bank_mask:0xf\n\t"
    "v_mfma_f32_16x16x32_bf16 v[112:115], a[4:7], a[232:235], v[112:115]\n\t"
    "s_add_u32 m0, s20, 45120\n\t"
    "v_mfma_f32_16x16x32_bf16 v[112:115], a[0:3], a[228:231], v[112:115]\n\t"
    "buffer_load_dwordx4 v177, s[8:11], s57 offen offset:3072 lds\n\t"
    "v_mfma_f32_16x16x32_bf16 v[120:123], a[0:3], a[232:235], v[120:123]\n\t"
    "v_max_u32_dpp v185, v185, v185 row_ror:1 row_mask:0xf bank_mask:0xf\n\t"
    "v_mfma_f32_16x16x32_bf16 v[120:123], a[4:7], a[236:239], v[120:123]\n\t"
    "s_add_u32 m0, s92, 148992\n\t"
    "v_mfma_f32_16x16x32_bf16 v[120:123], a[8:11], a[240:243], v[120:123]\n\t"
    "buffer_load_dwordx4 v186, s[84:87], s67 offen offset:512 lds\n\t"
    "v_mfma_f32_16x16x32_bf16 v[120:123], a[12:15], a[244:247], v[120:123]\n\t"
    "v_readlane_b32 s36, v185, 0\n\t"
    "v_readlane_b32 s37, v185, 16\n\t"
    "v_mfma_f32_16x16x32_bf16 v[128:131], a[12:15], a[248:251], v[128:131]\n\t"
    "s_add_u32 m0, s92, 153088\n\t"
    "v_readlane_b32 s38, v185, 32\n\t"
    "v_mfma_f32_16x16x32_bf16 v[128:131], a[8:11], a[244:247], v[128:131]\n\t"
    "buffer_load_dwordx4 v186, s[84:87], s69 offen offset:512 lds\n\t"
    "v_mfma_f32_16x16x32_bf16 v[128:131], a[4:7], a[240:243], v[128:131]\n\t"
    "v_readlane_b32 s39, v185, 48\n\t"
    "v_mfma_f32_16x16x32_bf16 v[128:131], a[0:3], a[236:239], v[128:131]\n\t"
    "s_waitcnt vmcnt(14) lgkmcnt(0)\n\t"
    "s_barrier\n\t"
    "v_mfma_f32_16x16x32_bf16 v[76:79], a[16:19], a[208:211], v[76:79]\n\t"
    "ds_read_b128 a[208:211], v181 offset:36928\n\t"
    "v_xor_b32 v186, 0x8000, v186\n\t"
    "v_mfma_f32_16x16x32_bf16 v[76:79], a[20:23], a[212:215], v[76:79]\n\t"
    "ds_read_b64_tr_b16 a[0:1], v188 offset:26624\n\t"
    "ds_read_b64_tr_b16 a[2:3], v188 offset:27136\n\t"
    "v_mfma_f32_16x16x32_bf16 v[76:79], a[24:27], a[216:219], v[76:79]\n\t"
    "s_add_u32 m0, s92, 157696\n\t"
    "s_max_u32 s40, s36, s37\n\t"
    "v_mfma_f32_16x16x32_bf16 v[76:79], a[28:31], a[220:223], v[76:79]\n\t"
    "buffer_load_dwordx4 v186, s[84:87], 0 offen offset:0 lds\n\t"
    "v_mfma_f32_16x16x32_bf16 v[84:87], a[28:31], a[224:227], v[84:87]\n\t"
    "s_max_u32 s40, s40, s38\n\t"
    "s_max_u32 s40, s40, s39\n\t"
    "v_mfma_f32_16x16x32_bf16 v[84:87], a[24:27], a[220:223], v[84:87]\n\t"
    "s_add_u32 m0, s92, 16384\n\t"
    "s_lshr_b32 s47, s40, 7\n\t"
    "v_mfma_f32_16x16x32_bf16 v[84:87], a[20:23], a[216:219], v[84:87]\n\t"
    "buffer_load_dwordx4 v186, s[84:87], s65 offen offset:0 lds\n\t"
    "v_mfma_f32_16x16x32_bf16 v[84:87], a[16:19], a[212:215], v[84:87]\n\t"
    "ds_read_b128 a[212:215], v181 offset:37056\n\t"
    "s_max_u32 s47, s47, 20\n\t"
    "v_mfma_f32_16x16x32_bf16 v[92:95], a[16:19], a[216:219], v[92:95]\n\t"
    "ds_read_b128 a[216:219], v181 offset:37184\n\t"
    "s_add_u32 m0, s20, 46208\n\t"
    "v_mfma_f32_16x16x32_bf16 v[92:95], a[20:23], a[220:223], v[92:95]\n\t"
    "ds_read_b64_tr_b16 a[4:5], v188 offset:30720\n\t"
    "ds_read_b64_tr_b16 a[6:7], v188 offset:31232\n\t"
    "v_mfma_f32_16x16x32_bf16 v[92:95], a[24:27], a[224:227], v[92:95]\n\t"
    "buffer_load_dwordx4 v178, s[8:11], s57 offen offset:3072 lds\n\t"
    "v_mfma_f32_16x16x32_bf16 v[92:95], a[28:31], a[228:231], v[92:95]\n\t"
    "s_add_u32 s47, s47, 17\n\t"
    "s_max_u32 s47, s47, s23\n\t"
    "v_mfma_f32_16x16x32_bf16 v[100:103], a[28:31], a[232:235], v[100:103]\n\t"
    "s_add_u32 m0, s20, 47296\n\t"
    "s_min_u32 s47, s47, 255\n\t"
    "v_mfma_f32_16x16x32_bf16 v[100:103], a[24:27], a[228:231], v[100:103]\n\t"
    "ds_read_b64_tr_b16 a[8:9], v188 offset:34816\n\t"
    "ds_read_b64_tr_b16 a[10:11], v188 offset:35328\n\t"
    "v_mfma_f32_16x16x32_bf16 v[100:103], a[20:23], a[224:227], v[100:103]\n\t"
    "buffer_load_dwordx4 v179, s[8:11], s57 offen offset:3072 lds\n\t"
    "v_mfma_f32_16x16x32_bf16 v[100:103], a[16:19], a[220:223], v[100:103]\n\t"
    "ds_read_b128 a[220:223], v181 offset:37312\n\t"
    "s_sub_i32 s36, 138, s47\n\t"
    "v_mfma_f32_16x16x32_bf16 v[108:111], a[16:19], a[224:227], v[108:111]\n\t"
    "ds_read_b128 a[224:227], v181 offset:37440\n\t"
    "s_add_u32 m0, s92, 20480\n\t"
    "v_mfma_f32_16x16x32_bf16 v[108:111], a[20:23], a[228:231], v[108:111]\n\t"
    "buffer_load_dwordx4 v186, s[84:87], s67 offen offset:0 lds\n\t"
    "v_mfma_f32_16x16x32_bf16 v[108:111], a[24:27], a[232:235], v[108:111]\n\t"
    "s_lshl_b32 s36, s36, 23\n\t"
    "s_add_u32 s74, s36, 0x3f800000\n\t"
    "v_mfma_f32_16x16x32_bf16 v[108:111], a[28:31], a[236:239], v[108:111]\n\t"
    "ds_read_b64_tr_b16 a[12:13], v188 offset:38912\n\t"
    "ds_read_b64_tr_b16 a[14:15], v188 offset:39424\n\t"
    "v_mfma_f32_16x16x32_bf16 v[116:119], a[28:31], a[240:243], v[116:119]\n\t"
    "s_add_u32 m0, s92, 24576\n\t"
    "s_add_u32 s75, s36, 0x40000000\n\t"
    "v_mfma_f32_16x16x32_bf16 v[116:119], a[24:27], a[236:239], v[116:119]\n\t"
    "buffer_load_dwordx4 v186, s[84:87], s69 offen offset:0 lds\n\t"
    "v_mfma_f32_16x16x32_bf16 v[116:119], a[20:23], a[232:235], v[116:119]\n\t"
    "s_waitcnt vmcnt(30)\n\t"
    "v_readlane_b32 s31, v197, s93\n\t"
    "v_mfma_f32_16x16x32_bf16 v[116:119], a[16:19], a[228:231], v[116:119]\n\t"
    "ds_read_b128 a[228:231], v181 offset:37568\n\t"
    "s_add_u32 m0, s92, 83456\n\t"
    "v_mfma_f32_16x16x32_bf16 v[124:127], a[16:19], a[232:235], v[124:127]\n\t"
    "ds_read_b128 a[232:235], v181 offset:37696\n\t"
    "v_and_b32 v250, 0xfffffe00, v250\n\t"
    "v_mfma_f32_16x16x32_bf16 v[124:127], a[20:23], a[236:239], v[124:127]\n\t"
    "buffer_load_dwordx4 v186, s[84:87], 0 offen offset:512 lds\n\t"
    "v_mfma_f32_16x16x32_bf16 v[124:127], a[24:27], a[240:243], v[124:127]\n\t"
    "s_and_b32 s31, s31, 0xfffffe00\n\t"
    "s_cmp_eq_u32 s95, 0\n\ts_cbranch_scc1 L_n1_%=\n\tL_n0_%=:\n\ts_add_u32 s31, s31, 512\n\tv_cmp_eq_u32 vcc, s31, v250\n\ts_cbranch_vccnz L_n0_%=\n\tL_n1_%=:\n\t"
    "v_mfma_f32_16x16x32_bf16 v[124:127], a[28:31], a[244:247], v[124:127]\n\t"
    "s_add_u32 m0, s92, 87552\n\t"
    "s_nop 0\n\t"
    "v_mfma_f32_16x16x32_bf16 v[132:135], a[28:31], a[248:251], v[132:135]\n\t"
    "ds_read_b128 a[248:251], v182 offset:37184\n\t"
    "v_mov_b32_e64 v197, s31\n\t"
    "v_mfma_f32_16x16x32_bf16 v[132:135], a[24:27], a[244:247], v[132:135]\n\t"
    "ds_read_b128 a[244:247], v182 offset:37056\n\t"
    "v_mfma_f32_16x16x32_bf16 v[132:135], a[20:23], a[240:243], v[132:135]\n\t"
    "ds_read_b128 a[240:243], v183 offset:36928\n\t"
    "v_mfma_f32_16x16x32_bf16 v[132:135], a[16:19], a[236:239], v[132:135]\n\t"
    "s_waitcnt vmcnt(17) lgkmcnt(5)\n\t"
    "s_barrier\n\t"
    "v_mfma_f32_16x16x32_bf16 v[72:75], a[0:3], a[208:211], v[72:75]\n\t"
    "ds_read_b128 a[236:239], v181 offset:37824\n\t"
    "s_waitcnt vmcnt(39)\n\t"
    "v_mfma_f32_16x16x32_bf16 v[72:75], a[4:7], a[212:215], v[72:75]\n\t"
    "ds_read_b64_tr_b16 a[16:17], v188 offset:43008\n\t"
    "ds_read_b64_tr_b16 a[18:19], v188 offset:43520\n\t"
    "v_mfma_f32_16x16x32_bf16 v[72:75], a[8:11], a[216:219], v[72:75]\n\t"
    "buffer_load_dwordx4 v186, s[84:87], s65 offen offset:512 lds\n\t"
    "v_mfma_f32_16x16x32_bf16 v[72:75], a[12:15], a[220:223], v[72:75]\n\t"
    "ds_read_b64_tr_b16 v[200:201], v174 offset:0\n\t"
    "ds_read_b64_tr_b16 v[202:203], v174 offset:512\n\t"
    "v_mfma_f32_16x16x32_bf16 v[80:83], a[12:15], a[224:227], v[80:83]\n\t"
    "s_add_u32 m0, s92, 91648\n\t"
    "ds_read_b64_tr_b16 v[204:205], v174 offset:4096\n\t"
    "v_mfma_f32_16x16x32_bf16 v[80:83], a[8:11], a[220:223], v[80:83]\n\t"
    "buffer_load_dwordx4 v186, s[84:87], s67 offen offset:512 lds\n\t"
    "v_mfma_f32_16x16x32_bf16 v[80:83], a[4:7], a[216:219], v[80:83]\n\t"
    "ds_read_b64_tr_b16 v[206:207], v174 offset:4608\n\t"
    "ds_read_b64_tr_b16 v[208:209], v174 offset:8192\n\t"
    "v_mfma_f32_16x16x32_bf16 v[80:83], a[0:3], a[212:215], v[80:83]\n\t"
    "s_waitcnt lgkmcnt(11)\n\t"
    "ds_read_b64_tr_b16 a[20:21], v188 offset:47104\n\t"
    "v_mfma_f32_16x16x32_bf16 v[88:91], a[0:3], a[216:219], v[88:91]\n\t"
    "ds_read_b64_tr_b16 a[22:23], v188 offset:47616\n\t"
    "s_add_u32 m0, s92, 95744\n\t"
    "v_mfma_f32_16x16x32_bf16 v[88:91], a[4:7], a[220:223], v[88:91]\n\t"
    "buffer_load_dwordx4 v186, s[84:87], s69 offen offset:512 lds\n\t"
    "v_mfma_f32_16x16x32_bf16 v[88:91], a[8:11], a[224:227], v[88:91]\n\t"
    "ds_read_b64_tr_b16 v[210:211], v174 offset:8704\n\t"
    "ds_read_b64_tr_b16 v[212:213], v174 offset:12288\n\t"
    "v_mfma_f32_16x16x32_bf16 v[88:91], a[12:15], a[228:231], v[88:91]\n\t"
    "ds_read_b64_tr_b16 v[214:215], v174 offset:12800\n\t"
    "v_mfma_f32_16x16x32_bf16 v[96:99], a[12:15], a[232:235], v[96:99]\n\t"
    "buffer_store_dword v197, v195, s[88:91], 0 offen sc1\n\t"
    "v_mfma_f32_16x16x32_bf16 v[96:99], a[8:11], a[228:231], v[96:99]\n\t"
    "s_waitcnt lgkmcnt(12)\n\t"
    "ds_read_b64_tr_b16 a[24:25], v188 offset:51200\n\t"
    "v_mfma_f32_16x16x32_bf16 v[96:99], a[4:7], a[224:227], v[96:99]\n\t"
    "ds_read_b64_tr_b16 a[26:27], v188 offset:51712\n\t"
    "s_nop 0\n\t"
    "v_mfma_f32_16x16x32_bf16 v[96:99], a[0:3], a[220:223], v[96:99]\n\t"
    "v_xor_b32 v186, 0x8000, v186\n\t"
    "v_add_u32 v186, 0x10000, v186\n\t"
    "v_mfma_f32_16x16x32_bf16 v[104:107], a[0:3], a[224:227], v[104:107]\n\t"
    "v_and_b32 v186, 0x3ffff, v186\n\t"
    "s_add_u32 m0, s92, 100352\n\t"
    "v_mfma_f32_16x16x32_bf16 v[104:107], a[4:7], a[228:231], v[104:107]\n\t"
    "buffer_load_dwordx4 v186, s[84:87], 0 offen offset:0 lds\n\t"
    "v_mfma_f32_16x16x32_bf16 v[104:107], a[8:11], a[232:235], v[104:107]\n\t"
    "s_waitcnt lgkmcnt(2)\n\t"
    "v_pk_maximum3_f16 v185, v200, v200, 0 neg_lo:[0,1,0] neg_hi:[0,1,0]\n\t"
    "v_mfma_f32_16x16x32_bf16 v[104:107], a[12:15], a[236:239], v[104:107]\n\t"
    "ds_read_b64_tr_b16 a[28:29], v188 offset:55296\n\t"
    "ds_read_b64_tr_b16 a[30:31], v188 offset:55808\n\t"
    "v_mfma_f32_16x16x32_bf16 v[112:115], a[12:15], a[240:243], v[112:115]\n\t"
    "v_pk_maximum3_f16 v185, v201, v201, v185 neg_lo:[0,1,0] neg_hi:[0,1,0]\n\t"
    "v_pk_maximum3_f16 v185, v202, v202, v185 neg_lo:[0,1,0] neg_hi:[0,1,0]\n\t"
    "v_mfma_f32_16x16x32_bf16 v[112:115], a[8:11], a[236:239], v[112:115]\n\t"
    "v_pk_maximum3_f16 v185, v203, v203, v185 neg_lo:[0,1,0] neg_hi:[0,1,0]\n\t"
    "v_pk_maximum3_f16 v185, v204, v204, v185 neg_lo:[0,1,0] neg_hi:[0,1,0]\n\t"
    "v_mfma_f32_16x16x32_bf16 v[112:115], a[4:7], a[232:235], v[112:115]\n\t"
    "s_add_u32 m0, s92, 104448\n\t"
    "v_pk_maximum3_f16 v185, v205, v205, v185 neg_lo:[0,1,0] neg_hi:[0,1,0]\n\t"
    "v_mfma_f32_16x16x32_bf16 v[112:115], a[0:3], a[228:231], v[112:115]\n\t"
    "buffer_load_dwordx4 v186, s[84:87], s65 offen offset:0 lds\n\t"
    "v_mfma_f32_16x16x32_bf16 v[120:123], a[0:3], a[232:235], v[120:123]\n\t"
    "v_pk_maximum3_f16 v185, v206, v206, v185 neg_lo:[0,1,0] neg_hi:[0,1,0]\n\t"
    "v_pk_maximum3_f16 v185, v207, v207, v185 neg_lo:[0,1,0] neg_hi:[0,1,0]\n\t"
    "v_mfma_f32_16x16x32_bf16 v[120:123], a[4:7], a[236:239], v[120:123]\n\t"
    "v_pk_maximum3_f16 v185, v208, v208, v185 neg_lo:[0,1,0] neg_hi:[0,1,0]\n\t"
    "v_pk_maximum3_f16 v185, v209, v209, v185 neg_lo:[0,1,0] neg_hi:[0,1,0]\n\t"
    "v_mfma_f32_16x16x32_bf16 v[120:123], a[8:11], a[240:243], v[120:123]\n\t"
    "v_pk_maximum3_f16 v185, v210, v210, v185 neg_lo:[0,1,0] neg_hi:[0,1,0]\n\t"
    "v_pk_maximum3_f16 v185, v211, v211, v185 neg_lo:[0,1,0] neg_hi:[0,1,0]\n\t"
    "v_mfma_f32_16x16x32_bf16 v[120:123], a[12:15], a[244:247], v[120:123]\n\t"
    "v_pk_maximum3_f16 v185, v212, v212, v185 neg_lo:[0,1,0] neg_hi:[0,1,0]\n\t"
    "v_pk_maximum3_f16 v185, v213, v213, v185 neg_lo:[0,1,0] neg_hi:[0,1,0]\n\t"
    "v_mfma_f32_16x16x32_bf16 v[128:131], a[12:15], a[248:251], v[128:131]\n\t"
    "v_pk_maximum3_f16 v185, v214, v214, v185 neg_lo:[0,1,0] neg_hi:[0,1,0]\n\t"
    "v_pk_maximum3_f16 v185, v215, v215, v185 neg_lo:[0,1,0] neg_hi:[0,1,0]\n\t"
    "v_mfma_f32_16x16x32_bf16 v[128:131], a[8:11], a[244:247], v[128:131]\n\t"
    "v_lshlrev_b32 v216, 16, v200\n\t"
    "v_lshlrev_b32_e64 v217, 16, v204\n\t"
    "v_mfma_f32_16x16x32_bf16 v[128:131], a[4:7], a[240:243], v[128:131]\n\t"
    "v_lshlrev_b32 v218, 16, v208\n\t"
    "v_lshlrev_b32 v219, 16, v212\n\t"
    "v_mfma_f32_16x16x32_bf16 v[128:131], a[0:3], a[236:239], v[128:131]\n\t"
    "s_waitcnt vmcnt(19) lgkmcnt(0)\n\t"
    "s_barrier\n\t"
    "v_mfma_f32_16x16x32_bf16 v[76:79], a[16:19], a[208:211], v[76:79]\n\t"
    "ds_read_b128 a[208:211], v181 offset:0\n\t"
    "s_add_u32 m0, s92, 0\n\t"
    "v_mfma_f32_16x16x32_bf16 v[76:79], a[20:23], a[212:215], v[76:79]\n\t"
    "ds_read_b64_tr_b16 a[0:1], v188 offset:59392\n\t"
    "ds_read_b64_tr_b16 a[2:3], v188 offset:59904\n\t"
    "v_mfma_f32_16x16x32_bf16 v[76:79], a[24:27], a[216:219], v[76:79]\n\t"
    "buffer_load_dwordx4 v186, s[84:87], s67 offen offset:0 lds\n\t"
    "v_mfma_f32_16x16x32_bf16 v[76:79], a[28:31], a[220:223], v[76:79]\n\t"
    "v_add_f32 v220, v219, v217\n\t"
    "v_add_f32_e64 v221, v218, v216\n\t"
    "v_mfma_f32_16x16x32_bf16 v[84:87], a[28:31], a[224:227], v[84:87]\n\t"
    "s_add_u32 m0, s92, 4096\n\t"
    "v_fma_f32 v222, v217, 4.0, v219\n\t"
    "v_mfma_f32_16x16x32_bf16 v[84:87], a[24:27], a[220:223], v[84:87]\n\t"
    "buffer_load_dwordx4 v186, s[84:87], s69 offen offset:0 lds\n\t"
    "v_mfma_f32_16x16x32_bf16 v[84:87], a[20:23], a[216:219], v[84:87]\n\t"
    "v_fma_f32 v223, v216, 4.0, v218\n\t"
    "v_fma_f32 v216, v216, 0.5, v217\n\t"
    "v_mfma_f32_16x16x32_bf16 v[84:87], a[16:19], a[212:215], v[84:87]\n\t"
    "ds_read_b128 a[212:215], v181 offset:128\n\t"
    "s_add_u32 m0, s92, 7680\n\t"
    "v_mfma_f32_16x16x32_bf16 v[92:95], a[16:19], a[216:219], v[92:95]\n\t"
    "ds_read_b128 a[216:219], v181 offset:256\n\t"
    "v_mul_f32_e64 v220, s74, v220\n\t"
    "v_mfma_f32_16x16x32_bf16 v[92:95], a[20:23], a[220:223], v[92:95]\n\t"
    "ds_read_b64_tr_b16 a[4:5], v188 offset:63488\n\t"
    "ds_read_b64_tr_b16 a[6:7], v188 offset:64000\n\t"
    "v_mfma_f32_16x16x32_bf16 v[92:95], a[24:27], a[224:227], v[92:95]\n\t"
    "buffer_load_dwordx4 v186, s[84:87], 0 offen offset:512 lds\n\t"
    "v_mfma_f32_16x16x32_bf16 v[92:95], a[28:31], a[228:231], v[92:95]\n\t"
    "v_fma_f32 v216, v216, 0.5, v218\n\t"
    "v_mul_f32_e64 v222, s74, v222\n\t"
    "v_mfma_f32_16x16x32_bf16 v[100:103], a[28:31], a[232:235], v[100:103]\n\t"
    "s_add_u32 m0, s92, 11776\n\t"
    "v_fma_f32 v216, v216, 0.5, v219\n\t"
    "v_mfma_f32_16x16x32_bf16 v[100:103], a[24:27], a[228:231], v[100:103]\n\t"
    "ds_read_b64_tr_b16 a[8:9], v189 offset:2048\n\t"
    "ds_read_b64_tr_b16 a[10:11], v189 offset:2560\n\t"
    "v_mfma_f32_16x16x32_bf16 v[100:103], a[20:23], a[224:227], v[100:103]\n\t"
    "buffer_load_dwordx4 v186, s[84:87], s65 offen offset:512 lds\n\t"
    "v_mfma_f32_16x16x32_bf16 v[100:103], a[16:19], a[220:223], v[100:103]\n\t"
    "ds_read_b128 a[220:223], v181 offset:384\n\t"
    "v_fma_mixlo_f16 v152, v221, s74, v220\n\t"
    "v_mfma_f32_16x16x32_bf16 v[108:111], a[16:19], a[224:227], v[108:111]\n\t"
    "ds_read_b128 a[224:227], v181 offset:512\n\t"
    "s_add_u32 m0, s92, 108032\n\t"
    "v_mfma_f32_16x16x32_bf16 v[108:111], a[20:23], a[228:231], v[108:111]\n\t"
    "buffer_load_dwordx4 v186, s[84:87], s67 offen offset:512 lds\n\t"
    "v_mfma_f32_16x16x32_bf16 v[108:111], a[24:27], a[232:235], v[108:111]\n\t"
    "v_fma_mixlo_f16 v156, -v221, s74, v220\n\t"
    "v_fma_mixlo_f16 v160, v223, s75, v222\n\t"
    "v_mfma_f32_16x16x32_bf16 v[108:111], a[28:31], a[236:239], v[108:111]\n\t"
    "ds_read_b64_tr_b16 a[12:13], v189 offset:6144\n\t"
    "ds_read_b64_tr_b16 a[14:15], v189 offset:6656\n\t"
    "v_mfma_f32_16x16x32_bf16 v[116:119], a[28:31], a[240:243], v[116:119]\n\t"
    "s_add_u32 m0, s92, 112128\n\t"
    "v_fma_mixlo_f16 v164, -v223, s75, v222\n\t"
    "v_mfma_f32_16x16x32_bf16 v[116:119], a[24:27], a[236:239], v[116:119]\n\t"
    "buffer_load_dwordx4 v186, s[84:87], s69 offen offset:512 lds\n\t"
    "v_mfma_f32_16x16x32_bf16 v[116:119], a[20:23], a[232:235], v[116:119]\n\t"
    "v_fma_mixlo_f16 v168, v216, s74, 0\n\t"
    "v_and_b32 v224, 0xffff0000, v200\n\t"
    "v_mfma_f32_16x16x32_bf16 v[116:119], a[16:19], a[228:231], v[116:119]\n\t"
    "ds_read_b128 a[228:231], v181 offset:640\n\t"
    "v_xor_b32 v186, 0x8000, v186\n\t"
    "v_mfma_f32_16x16x32_bf16 v[124:127], a[16:19], a[232:235], v[124:127]\n\t"
    "ds_read_b128 a[232:235], v181 offset:768\n\t"
    "s_add_u32 m0, s92, 116736\n\t"
    "v_mfma_f32_16x16x32_bf16 v[124:127], a[20:23], a[236:239], v[124:127]\n\t"
    "buffer_load_dwordx4 v186, s[84:87], 0 offen offset:0 lds\n\t"
    "v_mfma_f32_16x16x32_bf16 v[124:127], a[24:27], a[240:243], v[124:127]\n\t"
    "v_and_b32 v225, 0xffff0000, v204\n\t"
    "v_and_b32 v226, 0xffff0000, v208\n\t"
    "v_mfma_f32_16x16x32_bf16 v[124:127], a[28:31], a[244:247], v[124:127]\n\t"
    "s_add_u32 m0, s92, 120832\n\t"
    "v_and_b32 v227, 0xffff0000, v212\n\t"
    "v_mfma_f32_16x16x32_bf16 v[132:135], a[28:31], a[248:251], v[132:135]\n\t"
    "ds_read_b128 a[248:251], v182 offset:256\n\t"
    "v_add_f32_e64 v228, v227, v225\n\t"
    "v_mfma_f32_16x16x32_bf16 v[132:135], a[24:27], a[244:247], v[132:135]\n\t"
    "ds_read_b128 a[244:247], v182 offset:128\n\t"
    "v_add_f32_e64 v229, v226, v224\n\t"
    "v_mfma_f32_16x16x32_bf16 v[132:135], a[20:23], a[240:243], v[132:135]\n\t"
    "ds_read_b128 a[240:243], v183 offset:0\n\t"
    "v_fma_f32 v230, v225, 4.0, v227\n\t"
    "v_mfma_f32_16x16x32_bf16 v[132:135], a[16:19], a[236:239], v[132:135]\n\t"
    "s_waitcnt vmcnt(20) lgkmcnt(5)\n\t"
    "s_barrier\n\t"
    "v_mfma_f32_16x16x32_bf16 v[72:75], a[0:3], a[208:211], v[72:75]\n\t"
    "ds_read_b128 a[236:239], v181 offset:896\n\t"
    "v_fma_f32 v231, v224, 4.0, v226\n\t"
    "v_mfma_f32_16x16x32_bf16 v[72:75], a[4:7], a[212:215], v[72:75]\n\t"
    "ds_read_b64_tr_b16 a[16:17], v189 offset:10240\n\t"
    "ds_read_b64_tr_b16 a[18:19], v189 offset:10752\n\t"
    "v_mfma_f32_16x16x32_bf16 v[72:75], a[8:11], a[216:219], v[72:75]\n\t"
    "buffer_load_dwordx4 v186, s[84:87], s65 offen offset:0 lds\n\t"
    "v_mfma_f32_16x16x32_bf16 v[72:75], a[12:15], a[220:223], v[72:75]\n\t"
    "v_fma_f32 v224, v224, 0.5, v225\n\t"
    "v_mul_f32_e64 v228, s74, v228\n\t"
    "v_mfma_f32_16x16x32_bf16 v[80:83], a[12:15], a[224:227], v[80:83]\n\t"
    "s_add_u32 s57, s57, 128\n\t"
    "s_and_b32 s57, s57, 0x1c0\n\t"
    "v_mfma_f32_16x16x32_bf16 v[80:83], a[8:11], a[220:223], v[80:83]\n\t"
    "s_add_u32 m0, s20, 65536\n\t"
    "v_fma_f32 v224, v224, 0.5, v226\n\t"
    "v_mfma_f32_16x16x32_bf16 v[80:83], a[4:7], a[216:219], v[80:83]\n\t"
    "buffer_load_dwordx4 v176, s[8:11], s57 offen offset:0 lds\n\t"
    "v_mfma_f32_16x16x32_bf16 v[80:83], a[0:3], a[212:215], v[80:83]\n\t"
    "s_waitcnt lgkmcnt(6)\n\t"
    "ds_read_b64_tr_b16 a[20:21], v189 offset:14336\n\t"
    "v_mfma_f32_16x16x32_bf16 v[88:91], a[0:3], a[216:219], v[88:91]\n\t"
    "ds_read_b64_tr_b16 a[22:23], v189 offset:14848\n\t"
    "s_add_u32 m0, s20, 66624\n\t"
    "v_mfma_f32_16x16x32_bf16 v[88:91], a[4:7], a[220:223], v[88:91]\n\t"
    "buffer_load_dwordx4 v177, s[8:11], s57 offen offset:0 lds\n\t"
    "v_mfma_f32_16x16x32_bf16 v[88:91], a[8:11], a[224:227], v[88:91]\n\t"
    "v_mul_f32 v230, s74, v230\n\t"
    "v_fma_f32 v224, v224, 0.5, v227\n\t"
    "v_mfma_f32_16x16x32_bf16 v[88:91], a[12:15], a[228:231], v[88:91]\n\t"
    "s_add_u32 m0, s92, 124928\n\t"
    "v_fma_mixhi_f16 v152, v229, s74, v228\n\t"
    "v_mfma_f32_16x16x32_bf16 v[96:99], a[12:15], a[232:235], v[96:99]\n\t"
    "buffer_load_dwordx4 v186, s[84:87], s67 offen offset:0 lds\n\t"
    "v_mfma_f32_16x16x32_bf16 v[96:99], a[8:11], a[228:231], v[96:99]\n\t"
    "s_waitcnt lgkmcnt(4)\n\t"
    "ds_read_b64_tr_b16 a[24:25], v189 offset:18432\n\t"
    "v_mfma_f32_16x16x32_bf16 v[96:99], a[4:7], a[224:227], v[96:99]\n\t"
    "ds_read_b64_tr_b16 a[26:27], v189 offset:18944\n\t"
    "s_add_u32 m0, s92, 129024\n\t"
    "v_mfma_f32_16x16x32_bf16 v[96:99], a[0:3], a[220:223], v[96:99]\n\t"
    "buffer_load_dwordx4 v186, s[84:87], s69 offen offset:0 lds\n\t"
    "v_mfma_f32_16x16x32_bf16 v[104:107], a[0:3], a[224:227], v[104:107]\n\t"
    "v_fma_mixhi_f16 v156, -v229, s74, v228\n\t"
    "v_fma_mixhi_f16 v160, v231, s75, v230\n\t"
    "v_mfma_f32_16x16x32_bf16 v[104:107], a[4:7], a[228:231], v[104:107]\n\t"
    "s_add_u32 m0, s92, 132608\n\t"
    "v_fma_mixhi_f16 v164, -v231, s75, v230\n\t"
    "v_mfma_f32_16x16x32_bf16 v[104:107], a[8:11], a[232:235], v[104:107]\n\t"
    "buffer_load_dwordx4 v186, s[84:87], 0 offen offset:512 lds\n\t"
    "v_mfma_f32_16x16x32_bf16 v[104:107], a[12:15], a[236:239], v[104:107]\n\t"
    "ds_read_b64_tr_b16 a[28:29], v189 offset:22528\n\t"
    "ds_read_b64_tr_b16 a[30:31], v189 offset:23040\n\t"
    "v_mfma_f32_16x16x32_bf16 v[112:115], a[12:15], a[240:243], v[112:115]\n\t"
    "s_add_u32 m0, s92, 136704\n\t"
    "v_fma_mixhi_f16 v168, v224, s74, 0\n\t"
    "v_mfma_f32_16x16x32_bf16 v[112:115], a[8:11], a[236:239], v[112:115]\n\t"
    "buffer_load_dwordx4 v186, s[84:87], s65 offen offset:512 lds\n\t"
    "v_mfma_f32_16x16x32_bf16 v[112:115], a[4:7], a[232:235], v[112:115]\n\t"
    "v_lshlrev_b32 v216, 16, v201\n\t"
    "v_lshlrev_b32_e64 v217, 16, v205\n\t"
    "v_mfma_f32_16x16x32_bf16 v[112:115], a[0:3], a[228:231], v[112:115]\n\t"
    "s_add_u32 m0, s20, 67712\n\t"
    "v_lshlrev_b32_e64 v218, 16, v209\n\t"
    "v_mfma_f32_16x16x32_bf16 v[120:123], a[0:3], a[232:235], v[120:123]\n\t"
    "buffer_load_dwordx4 v178, s[8:11], s57 offen offset:0 lds\n\t"
    "v_mfma_f32_16x16x32_bf16 v[120:123], a[4:7], a[236:239], v[120:123]\n\t"
    "v_lshlrev_b32 v219, 16, v213\n\t"
    "v_add_f32 v220, v219, v217\n\t"
    "v_mfma_f32_16x16x32_bf16 v[120:123], a[8:11], a[240:243], v[120:123]\n\t"
    "s_add_u32 m0, s20, 68800\n\t"
    "v_add_f32_e64 v221, v218, v216\n\t"
    "v_mfma_f32_16x16x32_bf16 v[120:123], a[12:15], a[244:247], v[120:123]\n\t"
    "buffer_load_dwordx4 v179, s[8:11], s57 offen offset:0 lds\n\t"
    "v_mfma_f32_16x16x32_bf16 v[128:131], a[12:15], a[248:251], v[128:131]\n\t"
    "v_fma_f32 v222, v217, 4.0, v219\n\t"
    "v_fma_f32 v223, v216, 4.0, v218\n\t"
    "v_mfma_f32_16x16x32_bf16 v[128:131], a[8:11], a[244:247], v[128:131]\n\t"
    "v_fma_f32 v216, v216, 0.5, v217\n\t"
    "v_mul_f32_e64 v220, s74, v220\n\t"
    "v_mfma_f32_16x16x32_bf16 v[128:131], a[4:7], a[240:243], v[128:131]\n\t"
    "v_fma_f32 v216, v216, 0.5, v218\n\t"
    "v_mul_f32_e64 v222, s74, v222\n\t"
    "v_mfma_f32_16x16x32_bf16 v[128:131], a[0:3], a[236:239], v[128:131]\n\t"
    "s_waitcnt vmcnt(23) lgkmcnt(0)\n\t"
    "s_barrier\n\t"
    "v_mfma_f32_16x16x32_bf16 v[76:79], a[16:19], a[208:211], v[76:79]\n\t"
    "ds_read_b128 a[208:211], v181 offset:64\n\t"
    "s_add_u32 m0, s92, 140800\n\t"
    "v_mfma_f32_16x16x32_bf16 v[76:79], a[20:23], a[212:215], v[76:79]\n\t"
    "ds_read_b64_tr_b16 a[0:1], v189 offset:26624\n\t"
    "ds_read_b64_tr_b16 a[2:3], v189 offset:27136\n\t"
    "v_mfma_f32_16x16x32_bf16 v[76:79], a[24:27], a[216:219], v[76:79]\n\t"
    "buffer_load_dwordx4 v186, s[84:87], s67 offen offset:512 lds\n\t"
    "v_mfma_f32_16x16x32_bf16 v[76:79], a[28:31], a[220:223], v[76:79]\n\t"
    "v_fma_f32 v216, v216, 0.5, v219\n\t"
    "v_fma_mixlo_f16 v153, v221, s74, v220\n\t"
    "v_mfma_f32_16x16x32_bf16 v[84:87], a[28:31], a[224:227], v[84:87]\n\t"
    "v_fma_mixlo_f16 v157, -v221, s74, v220\n\t"
    "v_fma_mixlo_f16 v161, v223, s75, v222\n\t"
    "v_mfma_f32_16x16x32_bf16 v[84:87], a[24:27], a[220:223], v[84:87]\n\t"
    "v_fma_mixlo_f16 v165, -v223, s75, v222\n\t"
    "v_fma_mixlo_f16 v169, v216, s74, 0\n\t"
    "v_mfma_f32_16x16x32_bf16 v[84:87], a[20:23], a[216:219], v[84:87]\n\t"
    "v_and_b32 v224, 0xffff0000, v201\n\t"
    "v_and_b32 v225, 0xffff0000, v205\n\t"
    "v_mfma_f32_16x16x32_bf16 v[84:87], a[16:19], a[212:215], v[84:87]\n\t"
    "ds_read_b128 a[212:215], v181 offset:192\n\t"
    "s_add_u32 m0, s92, 144896\n\t"
    "v_mfma_f32_16x16x32_bf16 v[92:95], a[16:19], a[216:219], v[92:95]\n\t"
    "ds_read_b128 a[216:219], v181 offset:320\n\t"
    "v_and_b32 v226, 0xffff0000, v209\n\t"
    "v_mfma_f32_16x16x32_bf16 v[92:95], a[20:23], a[220:223], v[92:95]\n\t"
    "ds_read_b64_tr_b16 a[4:5], v187 offset:16384\n\t"
    "ds_read_b64_tr_b16 a[6:7], v187 offset:16896\n\t"
    "v_mfma_f32_16x16x32_bf16 v[92:95], a[24:27], a[224:227], v[92:95]\n\t"
    "buffer_load_dwordx4 v186, s[84:87], s69 offen offset:512 lds\n\t"
    "v_mfma_f32_16x16x32_bf16 v[92:95], a[28:31], a[228:231], v[92:95]\n\t"
    "v_and_b32 v227, 0xffff0000, v213\n\t"
    "v_add_f32_e64 v228, v227, v225\n\t"
    "v_mfma_f32_16x16x32_bf16 v[100:103], a[28:31], a[232:235], v[100:103]\n\t"
    "v_add_f32_e64 v229, v226, v224\n\t"
    "v_fma_f32 v230, v225, 4.0, v227\n\t"
    "v_mfma_f32_16x16x32_bf16 v[100:103], a[24:27], a[228:231], v[100:103]\n\t"
    "ds_read_b64_tr_b16 a[8:9], v187 offset:20480\n\t"
    "ds_read_b64_tr_b16 a[10:11], v187 offset:20992\n\t"
    "v_mfma_f32_16x16x32_bf16 v[100:103], a[20:23], a[224:227], v[100:103]\n\t"
    "v_fma_f32 v231, v224, 4.0, v226\n\t"
    "v_fma_f32 v224, v224, 0.5, v225\n\t"
    "v_mfma_f32_16x16x32_bf16 v[100:103], a[16:19], a[220:223], v[100:103]\n\t"
    "ds_read_b128 a[220:223], v181 offset:448\n\t"
    "v_xor_b32 v186, 0x8000, v186\n\t"
    "v_mfma_f32_16x16x32_bf16 v[108:111], a[16:19], a[224:227], v[108:111]\n\t"
    "ds_read_b128 a[224:227], v181 offset:576\n\t"
    "v_add_u32 v186, 0x10000, v186\n\t"
    "v_mfma_f32_16x16x32_bf16 v[108:111], a[20:23], a[228:231], v[108:111]\n\t"
    "v_and_b32 v186, 0x3ffff, v186\n\t"
    "s_add_u32 m0, s92, 149504\n\t"
    "v_mfma_f32_16x16x32_bf16 v[108:111], a[24:27], a[232:235], v[108:111]\n\t"
    "buffer_load_dwordx4 v186, s[84:87], s64 offen offset:0 lds\n\t"
    "v_mfma_f32_16x16x32_bf16 v[108:111], a[28:31], a[236:239], v[108:111]\n\t"
    "ds_read_b64_tr_b16 a[12:13], v187 offset:24576\n\t"
    "ds_read_b64_tr_b16 a[14:15], v187 offset:25088\n\t"
    "v_mfma_f32_16x16x32_bf16 v[116:119], a[28:31], a[240:243], v[116:119]\n\t"
    "v_mul_f32_e64 v228, s74, v228\n\t"
    "v_fma_f32 v224, v224, 0.5, v226\n\t"
    "v_mfma_f32_16x16x32_bf16 v[116:119], a[24:27], a[236:239], v[116:119]\n\t"
    "v_mul_f32_e64 v230, s74, v230\n\t"
    "v_fma_f32 v224, v224, 0.5, v227\n\t"
    "v_mfma_f32_16x16x32_bf16 v[116:119], a[20:23], a[232:235], v[116:119]\n\t"
    "s_add_u32 m0, s92, 153600\n\t"
    "v_fma_mixhi_f16 v153, v229, s74, v228\n\t"
    "v_mfma_f32_16x16x32_bf16 v[116:119], a[16:19], a[228:231], v[116:119]\n\t"
    "ds_read_b128 a[228:231], v181 offset:704\n\t"
    "v_fma_mixhi_f16 v157, -v229, s74, v228\n\t"
    "v_mfma_f32_16x16x32_bf16 v[124:127], a[16:19], a[232:235], v[124:127]\n\t"
    "ds_read_b128 a[232:235], v181 offset:832\n\t"
    "v_fma_mixhi_f16 v161, v231, s75, v230\n\t"
    "v_mfma_f32_16x16x32_bf16 v[124:127], a[20:23], a[236:239], v[124:127]\n\t"
    "buffer_load_dwordx4 v186, s[84:87], s66 offen offset:0 lds\n\t"
    "v_mfma_f32_16x16x32_bf16 v[124:127], a[24:27], a[240:243], v[124:127]\n\t"
    "v_fma_mixhi_f16 v165, -v231, s75, v230\n\t"
    "v_fma_mixhi_f16 v169, v224, s74, 0\n\t"
    "v_mfma_f32_16x16x32_bf16 v[124:127], a[28:31], a[244:247], v[124:127]\n\t"
    "v_lshlrev_b32 v216, 16, v202\n\t"
    "v_lshlrev_b32 v217, 16, v206\n\t"
    "v_mfma_f32_16x16x32_bf16 v[132:135], a[28:31], a[248:251], v[132:135]\n\t"
    "ds_read_b128 a[248:251], v182 offset:320\n\t"
    "v_lshlrev_b32_e64 v218, 16, v210\n\t"
    "v_mfma_f32_16x16x32_bf16 v[132:135], a[24:27], a[244:247], v[132:135]\n\t"
    "ds_read_b128 a[244:247], v182 offset:192\n\t"
    "v_lshlrev_b32_e64 v219, 16, v214\n\t"
    "v_mfma_f32_16x16x32_bf16 v[132:135], a[20:23], a[240:243], v[132:135]\n\t"
    "ds_read_b128 a[240:243], v183 offset:64\n\t"
    "v_add_f32_e64 v220, v219, v217\n\t"
    "v_mfma_f32_16x16x32_bf16 v[132:135], a[16:19], a[236:239], v[132:135]\n\t"
    "s_waitcnt vmcnt(23) lgkmcnt(5)\n\t"
    "s_barrier\n\t"
    "v_mfma_f32_16x16x32_bf16 v[72:75], a[0:3], a[208:211], v[72:75]\n\t"
    "ds_read_b128 a[236:239], v181 offset:960\n\t"
    "s_add_u32 m0, s92, 157696\n\t"
    "v_mfma_f32_16x16x32_bf16 v[72:75], a[4:7], a[212:215], v[72:75]\n\t"
    "ds_read_b64_tr_b16 a[16:17], v188 offset:18432\n\t"
    "ds_read_b64_tr_b16 a[18:19], v188 offset:18944\n\t"
    "v_mfma_f32_16x16x32_bf16 v[72:75], a[8:11], a[216:219], v[72:75]\n\t"
    "buffer_load_dwordx4 v186, s[84:87], s68 offen offset:0 lds\n\t"
    "v_mfma_f32_16x16x32_bf16 v[72:75], a[12:15], a[220:223], v[72:75]\n\t"
    "v_accvgpr_write_b32 a192, 0\n\t"
    "v_accvgpr_write_b32 a193, 0\n\t"
    "v_mfma_f32_16x16x32_bf16 v[80:83], a[12:15], a[224:227], v[80:83]\n\t"
    "v_add_f32_e64 v221, v218, v216\n\t"
    "v_fma_f32 v222, v217, 4.0, v219\n\t"
    "v_mfma_f32_16x16x32_bf16 v[80:83], a[8:11], a[220:223], v[80:83]\n\t"
    "v_fma_f32 v223, v216, 4.0, v218\n\t"
    "v_fma_f32 v216, v216, 0.5, v217\n\t"
    "v_mfma_f32_16x16x32_bf16 v[80:83], a[4:7], a[216:219], v[80:83]\n\t"
    "v_accvgpr_write_b32 a194, 0\n\t"
    "v_mul_f32_e64 v220, s74, v220\n\t"
    "v_mfma_f32_16x16x32_bf16 v[80:83], a[0:3], a[212:215], v[80:83]\n\t"
    "s_waitcnt lgkmcnt(6)\n\t"
    "ds_read_b64_tr_b16 a[20:21], v188 offset:22528\n\t"
    "v_mfma_f32_16x16x32_bf16 v[88:91], a[0:3], a[216:219], v[88:91]\n\t"
    "ds_read_b64_tr_b16 a[22:23], v188 offset:23040\n\t"
    "s_add_u32 m0, s92, 15872\n\t"
    "v_mfma_f32_16x16x32_bf16 v[88:91], a[4:7], a[220:223], v[88:91]\n\t"
    "buffer_load_dwordx4 v186, s[84:87], s64 offen offset:512 lds\n\t"
    "v_mfma_f32_16x16x32_bf16 v[88:91], a[8:11], a[224:227], v[88:91]\n\t"
    "v_accvgpr_write_b32 a195, 0\n\t"
    "v_fma_f32 v216, v216, 0.5, v218\n\t"
    "v_mfma_f32_16x16x32_bf16 v[88:91], a[12:15], a[228:231], v[88:91]\n\t"
    "v_mul_f32 v222, s74, v222\n\t"
    "v_fma_f32 v216, v216, 0.5, v219\n\t"
    "v_mfma_f32_16x16x32_bf16 v[96:99], a[12:15], a[232:235], v[96:99]\n\t"
    "v_fma_mixlo_f16 v154, v221, s74, v220\n\t"
    "v_fma_mixlo_f16 v158, -v221, s74, v220\n\t"
    "v_mfma_f32_16x16x32_bf16 v[96:99], a[8:11], a[228:231], v[96:99]\n\t"
    "s_waitcnt lgkmcnt(4)\n\t"
    "ds_read_b64_tr_b16 a[24:25], v188 offset:26624\n\t"
    "v_mfma_f32_16x16x32_bf16 v[96:99], a[4:7], a[224:227], v[96:99]\n\t"
    "ds_read_b64_tr_b16 a[26:27], v188 offset:27136\n\t"
    "v_fma_mixlo_f16 v162, v223, s75, v222\n\t"
    "v_mfma_f32_16x16x32_bf16 v[96:99], a[0:3], a[220:223], v[96:99]\n\t"
    "s_add_u32 m0, s92, 19968\n\t"
    "v_fma_mixlo_f16 v166, -v223, s75, v222\n\t"
    "v_mfma_f32_16x16x32_bf16 v[104:107], a[0:3], a[224:227], v[104:107]\n\t"
    "buffer_load_dwordx4 v186, s[84:87], s66 offen offset:512 lds\n\t"
    "v_mfma_f32_16x16x32_bf16 v[104:107], a[4:7], a[228:231], v[104:107]\n\t"
    "v_fma_mixlo_f16 v170, v216, s74, 0\n\t"
    "v_and_b32 v224, 0xffff0000, v202\n\t"
    "v_mfma_f32_16x16x32_bf16 v[104:107], a[8:11], a[232:235], v[104:107]\n\t"
    "v_and_b32 v225, 0xffff0000, v206\n\t"
    "v_and_b32 v226, 0xffff0000, v210\n\t"
    "v_mfma_f32_16x16x32_bf16 v[104:107], a[12:15], a[236:239], v[104:107]\n\t"
    "ds_read_b64_tr_b16 a[28:29], v188 offset:30720\n\t"
    "ds_read_b64_tr_b16 a[30:31], v188 offset:31232\n\t"
    "v_mfma_f32_16x16x32_bf16 v[112:115], a[12:15], a[240:243], v[112:115]\n\t"
    "v_and_b32 v227, 0xffff0000, v214\n\t"
    "v_add_f32 v228, v227, v225\n\t"
    "v_mfma_f32_16x16x32_bf16 v[112:115], a[8:11], a[236:239], v[112:115]\n\t"
    "v_add_f32_e64 v229, v226, v224\n\t"
    "v_fma_f32 v230, v225, 4.0, v227\n\t"
    "v_mfma_f32_16x16x32_bf16 v[112:115], a[4:7], a[232:235], v[112:115]\n\t"
    "s_add_u32 m0, s92, 24064\n\t"
    "v_fma_f32 v231, v224, 4.0, v226\n\t"
    "v_mfma_f32_16x16x32_bf16 v[112:115], a[0:3], a[228:231], v[112:115]\n\t"
    "buffer_load_dwordx4 v186, s[84:87], s68 offen offset:512 lds\n\t"
    "v_mfma_f32_16x16x32_bf16 v[120:123], a[0:3], a[232:235], v[120:123]\n\t"
    "v_mad_u32_u24 v183, v248, s27, v183\n\t"
    "v_fma_f32 v224, v224, 0.5, v225\n\t"
    "v_mfma_f32_16x16x32_bf16 v[120:123], a[4:7], a[236:239], v[120:123]\n\t"
    "v_mul_f32_e64 v228, s74, v228\n\t"
    "v_fma_f32 v224, v224, 0.5, v226\n\t"
    "v_mfma_f32_16x16x32_bf16 v[120:123], a[8:11], a[240:243], v[120:123]\n\t"
    "v_mul_f32_e64 v230, s74, v230\n\t"
    "v_fma_f32 v224, v224, 0.5, v227\n\t"
    "v_mfma_f32_16x16x32_bf16 v[120:123], a[12:15], a[244:247], v[120:123]\n\t"
    "v_fma_mixhi_f16 v154, v229, s74, v228\n\t"
    "v_fma_mixhi_f16 v158, -v229, s74, v228\n\t"
    "v_mfma_f32_16x16x32_bf16 v[128:131], a[12:15], a[248:251], v[128:131]\n\t"
    "v_fma_mixhi_f16 v162, v231, s75, v230\n\t"
    "v_fma_mixhi_f16 v166, -v231, s75, v230\n\t"
    "v_mfma_f32_16x16x32_bf16 v[128:131], a[8:11], a[244:247], v[128:131]\n\t"
    "v_fma_mixhi_f16 v170, v224, s74, 0\n\t"
    "v_lshlrev_b32_e64 v216, 16, v203\n\t"
    "v_mfma_f32_16x16x32_bf16 v[128:131], a[4:7], a[240:243], v[128:131]\n\t"
    "v_lshlrev_b32 v217, 16, v207\n\t"
    "v_lshlrev_b32 v218, 16, v211\n\t"
    "v_mfma_f32_16x16x32_bf16 v[128:131], a[0:3], a[236:239], v[128:131]\n\t"
    "s_waitcnt vmcnt(22) lgkmcnt(0)\n\t"
    "s_barrier\n\t"
    "v_mfma_f32_16x16x32_bf16 v[76:79], a[16:19], a[208:211], v[76:79]\n\t"
    "ds_read_b128 a[208:211], v181 offset:18432\n\t"
    "v_xor_b32 v186, 0x8000, v186\n\t"
    "v_mfma_f32_16x16x32_bf16 v[76:79], a[20:23], a[212:215], v[76:79]\n\t"
    "ds_read_b64_tr_b16 a[0:1], v188 offset:34816\n\t"
    "ds_read_b64_tr_b16 a[2:3], v188 offset:35328\n\t"
    "v_mfma_f32_16x16x32_bf16 v[76:79], a[24:27], a[216:219], v[76:79]\n\t"
    "s_add_u32 m0, s92, 83968\n\t"
    "v_lshlrev_b32_e64 v219, 16, v215\n\t"
    "v_mfma_f32_16x16x32_bf16 v[76:79], a[28:31], a[220:223], v[76:79]\n\t"
    "buffer_load_dwordx4 v186, s[84:87], s64 offen offset:0 lds\n\t"
    "v_mfma_f32_16x16x32_bf16 v[84:87], a[28:31], a[224:227], v[84:87]\n\t"
    "v_add_f32 v220, v219, v217\n\t"
    "v_add_f32 v221, v218, v216\n\t"
    "v_mfma_f32_16x16x32_bf16 v[84:87], a[24:27], a[220:223], v[84:87]\n\t"
    "s_add_u32 m0, s92, 88064\n\t"
    "v_fma_f32 v222, v217, 4.0, v219\n\t"
    "v_mfma_f32_16x16x32_bf16 v[84:87], a[20:23], a[216:219], v[84:87]\n\t"
    "buffer_load_dwordx4 v186, s[84:87], s66 offen offset:0 lds\n\t"
    "v_mfma_f32_16x16x32_bf16 v[84:87], a[16:19], a[212:215], v[84:87]\n\t"
    "ds_read_b128 a[212:215], v181 offset:18560\n\t"
    "v_fma_f32 v223, v216, 4.0, v218\n\t"
    "v_mfma_f32_16x16x32_bf16 v[92:95], a[16:19], a[216:219], v[92:95]\n\t"
    "ds_read_b128 a[216:219], v181 offset:18688\n\t"
    "v_fma_f32 v216, v216, 0.5, v217\n\t"
    "v_mfma_f32_16x16x32_bf16 v[92:95], a[20:23], a[220:223], v[92:95]\n\t"
    "ds_read_b64_tr_b16 a[4:5], v188 offset:38912\n\t"
    "ds_read_b64_tr_b16 a[6:7], v188 offset:39424\n\t"
    "v_mfma_f32_16x16x32_bf16 v[92:95], a[24:27], a[224:227], v[92:95]\n\t"
    "s_add_u32 m0, s92, 92160\n\t"
    "v_mul_f32_e64 v220, s74, v220\n\t"
    "v_mfma_f32_16x16x32_bf16 v[92:95], a[28:31], a[228:231], v[92:95]\n\t"
    "buffer_load_dwordx4 v186, s[84:87], s68 offen offset:0 lds\n\t"
    "v_mfma_f32_16x16x32_bf16 v[100:103], a[28:31], a[232:235], v[100:103]\n\t"
    "v_fma_f32 v216, v216, 0.5, v218\n\t"
    "v_mul_f32_e64 v222, s74, v222\n\t"
    "v_mfma_f32_16x16x32_bf16 v[100:103], a[24:27], a[228:231], v[100:103]\n\t"
    "ds_read_b64_tr_b16 a[8:9], v187 offset:0\n\t"
    "ds_read_b64_tr_b16 a[10:11], v187 offset:512\n\t"
    "v_mfma_f32_16x16x32_bf16 v[100:103], a[20:23], a[224:227], v[100:103]\n\t"
    "v_fma_f32 v216, v216, 0.5, v219\n\t"
    "v_fma_mixlo_f16 v155, v221, s74, v220\n\t"
    "v_mfma_f32_16x16x32_bf16 v[100:103], a[16:19], a[220:223], v[100:103]\n\t"
    "ds_read_b128 a[220:223], v181 offset:18816\n\t"
    "s_add_u32 m0, s92, 95744\n\t"
    "v_mfma_f32_16x16x32_bf16 v[108:111], a[16:19], a[224:227], v[108:111]\n\t"
    "ds_read_b128 a[224:227], v181 offset:18944\n\t"
    "v_fma_mixlo_f16 v159, -v221, s74, v220\n\t"
    "v_mfma_f32_16x16x32_bf16 v[108:111], a[20:23], a[228:231], v[108:111]\n\t"
    "buffer_load_dwordx4 v186, s[84:87], s64 offen offset:512 lds\n\t"
    "v_mfma_f32_16x16x32_bf16 v[108:111], a[24:27], a[232:235], v[108:111]\n\t"
    "v_fma_mixlo_f16 v163, v223, s75, v222\n\t"
    "v_fma_mixlo_f16 v167, -v223, s75, v222\n\t"
    "v_mfma_f32_16x16x32_bf16 v[108:111], a[28:31], a[236:239], v[108:111]\n\t"
    "ds_read_b64_tr_b16 a[12:13], v187 offset:4096\n\t"
    "ds_read_b64_tr_b16 a[14:15], v187 offset:4608\n\t"
    "v_mfma_f32_16x16x32_bf16 v[116:119], a[28:31], a[240:243], v[116:119]\n\t"
    "s_add_u32 s57, s57, 128\n\t"
    "s_and_b32 s57, s57, 0x1c0\n\t"
    "v_mfma_f32_16x16x32_bf16 v[116:119], a[24:27], a[236:239], v[116:119]\n\t"
    "s_add_u32 m0, s20, 28672\n\t"
    "v_fma_mixlo_f16 v171, v216, s74, 0\n\t"
    "v_mfma_f32_16x16x32_bf16 v[116:119], a[20:23], a[232:235], v[116:119]\n\t"
    "buffer_load_dwordx4 v176, s[8:11], s57 offen offset:0 lds\n\t"
    "v_mfma_f32_16x16x32_bf16 v[116:119], a[16:19], a[228:231], v[116:119]\n\t"
    "ds_read_b128 a[228:231], v181 offset:19072\n\t"
    "v_and_b32 v224, 0xffff0000, v203\n\t"
    "v_mfma_f32_16x16x32_bf16 v[124:127], a[16:19], a[232:235], v[124:127]\n\t"
    "ds_read_b128 a[232:235], v181 offset:19200\n\t"
    "v_and_b32 v225, 0xffff0000, v207\n\t"
    "v_mfma_f32_16x16x32_bf16 v[124:127], a[20:23], a[236:239], v[124:127]\n\t"
    "s_add_u32 m0, s20, 29760\n\t"
    "v_and_b32 v226, 0xffff0000, v211\n\t"
    "v_mfma_f32_16x16x32_bf16 v[124:127], a[24:27], a[240:243], v[124:127]\n\t"
    "buffer_load_dwordx4 v177, s[8:11], s57 offen offset:0 lds\n\t"
    "v_mfma_f32_16x16x32_bf16 v[124:127], a[28:31], a[244:247], v[124:127]\n\t"
    "v_and_b32 v227, 0xffff0000, v215\n\t"
    "v_add_f32_e64 v228, v227, v225\n\t"
    "v_mfma_f32_16x16x32_bf16 v[132:135], a[28:31], a[248:251], v[132:135]\n\t"
    "ds_read_b128 a[248:251], v182 offset:18688\n\t"
    "v_add_f32_e64 v229, v226, v224\n\t"
    "v_mfma_f32_16x16x32_bf16 v[132:135], a[24:27], a[244:247], v[132:135]\n\t"
    "ds_read_b128 a[244:247], v182 offset:18560\n\t"
    "v_fma_f32 v230, v225, 4.0, v227\n\t"
    "v_mfma_f32_16x16x32_bf16 v[132:135], a[20:23], a[240:243], v[132:135]\n\t"
    "ds_read_b128 a[240:243], v183 offset:18432\n\t"
    "v_fma_f32 v231, v224, 4.0, v226\n\t"
    "v_mfma_f32_16x16x32_bf16 v[132:135], a[16:19], a[236:239], v[132:135]\n\t"
    "s_waitcnt vmcnt(24) lgkmcnt(5)\n\t"
    "s_barrier\n\t"
    "v_mfma_f32_16x16x32_bf16 v[72:75], a[0:3], a[208:211], v[72:75]\n\t"
    "ds_read_b128 a[236:239], v181 offset:19328\n\t"
    "s_add_u32 m0, s92, 99840\n\t"
    "v_mfma_f32_16x16x32_bf16 v[72:75], a[4:7], a[212:215], v[72:75]\n\t"
    "ds_read_b64_tr_b16 a[16:17], v187 offset:8192\n\t"
    "ds_read_b64_tr_b16 a[18:19], v187 offset:8704\n\t"
    "v_mfma_f32_16x16x32_bf16 v[72:75], a[8:11], a[216:219], v[72:75]\n\t"
    "buffer_load_dwordx4 v186, s[84:87], s66 offen offset:512 lds\n\t"
    "v_mfma_f32_16x16x32_bf16 v[72:75], a[12:15], a[220:223], v[72:75]\n\t"
    "v_fma_f32 v224, v224, 0.5, v225\n\t"
    "v_mul_f32_e64 v228, s74, v228\n\t"
    "v_mfma_f32_16x16x32_bf16 v[80:83], a[12:15], a[224:227], v[80:83]\n\t"
    "v_fma_f32 v224, v224, 0.5, v226\n\t"
    "v_mul_f32_e64 v230, s74, v230\n\t"
    "v_mfma_f32_16x16x32_bf16 v[80:83], a[8:11], a[220:223], v[80:83]\n\t"
    "s_add_u32 m0, s92, 103936\n\t"
    "v_fma_f32 v224, v224, 0.5, v227\n\t"
    "v_mfma_f32_16x16x32_bf16 v[80:83], a[4:7], a[216:219], v[80:83]\n\t"
    "buffer_load_dwordx4 v186, s[84:87], s68 offen offset:512 lds\n\t"
    "v_mfma_f32_16x16x32_bf16 v[80:83], a[0:3], a[212:215], v[80:83]\n\t"
    "s_waitcnt lgkmcnt(6)\n\t"
    "ds_read_b64_tr_b16 a[20:21], v187 offset:12288\n\t"
    "v_mfma_f32_16x16x32_bf16 v[88:91], a[0:3], a[216:219], v[88:91]\n\t"
    "ds_read_b64_tr_b16 a[22:23], v187 offset:12800\n\t"
    "v_fma_mixhi_f16 v155, v229, s74, v228\n\t"
    "v_mfma_f32_16x16x32_bf16 v[88:91], a[4:7], a[220:223], v[88:91]\n\t"
    "v_fma_mixhi_f16 v159, -v229, s74, v228\n\t"
    "v_fma_mixhi_f16 v163, v231, s75, v230\n\t"
    "v_mfma_f32_16x16x32_bf16 v[88:91], a[8:11], a[224:227], v[88:91]\n\t"
    "v_xor_b32 v186, 0x8000, v186\n\t"
    "v_add_u32 v186, 0x10000, v186\n\t"
    "v_mfma_f32_16x16x32_bf16 v[88:91], a[12:15], a[228:231], v[88:91]\n\t"
    "v_and_b32 v186, 0x3ffff, v186\n\t"
    "s_add_u32 m0, s92, 0\n\t"
    "v_mfma_f32_16x16x32_bf16 v[96:99], a[12:15], a[232:235], v[96:99]\n\t"
    "buffer_load_dwordx4 v186, s[84:87], s64 offen offset:0 lds\n\t"
    "v_mfma_f32_16x16x32_bf16 v[96:99], a[8:11], a[228:231], v[96:99]\n\t"
    "s_waitcnt lgkmcnt(4)\n\t"
    "ds_read_b64_tr_b16 a[24:25], v188 offset:43008\n\t"
    "v_mfma_f32_16x16x32_bf16 v[96:99], a[4:7], a[224:227], v[96:99]\n\t"
    "ds_read_b64_tr_b16 a[26:27], v188 offset:43520\n\t"
    "v_fma_mixhi_f16 v167, -v231, s75, v230\n\t"
    "v_mfma_f32_16x16x32_bf16 v[96:99], a[0:3], a[220:223], v[96:99]\n\t"
    "s_add_u32 m0, s92, 4096\n\t"
    "v_fma_mixhi_f16 v171, v224, s74, 0\n\t"
    "v_mfma_f32_16x16x32_bf16 v[104:107], a[0:3], a[224:227], v[104:107]\n\t"
    "buffer_load_dwordx4 v186, s[84:87], s66 offen offset:0 lds\n\t"
    "v_mfma_f32_16x16x32_bf16 v[104:107], a[4:7], a[228:231], v[104:107]\n\t"
    "v_lshrrev_b32 v191, 16, v185\n\t"
    "v_and_b32 v185, 0xffff, v185\n\t"
    "v_mfma_f32_16x16x32_bf16 v[104:107], a[8:11], a[232:235], v[104:107]\n\t"
    "v_max_u32_e64 v185, v185, v191\n\t"
    "v_mfma_f32_16x16x32_bf16 v[104:107], a[12:15], a[236:239], v[104:107]\n\t"
    "ds_read_b64_tr_b16 a[28:29], v188 offset:47104\n\t"
    "ds_read_b64_tr_b16 a[30:31], v188 offset:47616\n\t"
    "v_mfma_f32_16x16x32_bf16 v[112:115], a[12:15], a[240:243], v[112:115]\n\t"
    "s_add_u32 m0, s20, 30848\n\t"
    "v_max_u32_dpp v185, v185, v185 row_ror:8 row_mask:0xf bank_mask:0xf\n\t"
    "v_mfma_f32_16x16x32_bf16 v[112:115], a[8:11], a[236:239], v[112:115]\n\t"
    "buffer_load_dwordx4 v178, s[8:11], s57 offen offset:0 lds\n\t"
    "v_mfma_f32_16x16x32_bf16 v[112:115], a[4:7], a[232:235], v[112:115]\n\t"
    "v_max_u32_dpp v185, v185, v185 row_ror:4 row_mask:0xf bank_mask:0xf\n\t"
    "v_mfma_f32_16x16x32_bf16 v[112:115], a[0:3], a[228:231], v[112:115]\n\t"
    "v_mfma_f32_16x16x32_bf16 v[120:123], a[0:3], a[232:235], v[120:123]\n\t"
    "v_max_u32_dpp v185, v185, v185 row_ror:2 row_mask:0xf bank_mask:0xf\n\t"
    "v_mfma_f32_16x16x32_bf16 v[120:123], a[4:7], a[236:239], v[120:123]\n\t"
    "s_add_u32 m0, s20, 31936\n\t"
    "v_mfma_f32_16x16x32_bf16 v[120:123], a[8:11], a[240:243], v[120:123]\n\t"
    "buffer_load_dwordx4 v179, s[8:11], s57 offen offset:0 lds\n\t"
    "v_mfma_f32_16x16x32_bf16 v[120:123], a[12:15], a[244:247], v[120:123]\n\t"
    "v_max_u32_dpp v185, v185, v185 row_ror:1 row_mask:0xf bank_mask:0xf\n\t"
    "v_mfma_f32_16x16x32_bf16 v[128:131], a[12:15], a[248:251], v[128:131]\n\t"
    "v_mfma_f32_16x16x32_bf16 v[128:131], a[8:11], a[244:247], v[128:131]\n\t"
    "v_readlane_b32 s36, v185, 0\n\t"
    "v_readlane_b32 s37, v185, 16\n\t"
    "v_mfma_f32_16x16x32_bf16 v[128:131], a[4:7], a[240:243], v[128:131]\n\t"
    "v_readlane_b32 s38, v185, 32\n\t"
    "v_readlane_b32 s39, v185, 48\n\t"
    "v_mfma_f32_16x16x32_bf16 v[128:131], a[0:3], a[236:239], v[128:131]\n\t"
    "s_waitcnt vmcnt(24) lgkmcnt(0)\n\t"
    "s_barrier\n\t"
    "v_mfma_f32_16x16x32_bf16 v[76:79], a[16:19], a[208:211], v[76:79]\n\t"
    "ds_read_b128 a[208:211], v181 offset:18496\n\t"
    "s_max_u32 s40, s36, s37\n\t"
    "v_mfma_f32_16x16x32_bf16 v[76:79], a[20:23], a[212:215], v[76:79]\n\t"
    "ds_read_b64_tr_b16 a[0:1], v188 offset:51200\n\t"
    "ds_read_b64_tr_b16 a[2:3], v188 offset:51712\n\t"
    "v_mfma_f32_16x16x32_bf16 v[76:79], a[24:27], a[216:219], v[76:79]\n\t"
    "global_store_dwordx4 v190, v[152:155], s[18:19] sc1\n\t"
    "s_max_u32 s40, s40, s38\n\t"
    "v_mfma_f32_16x16x32_bf16 v[76:79], a[28:31], a[220:223], v[76:79]\n\t"
    "v_add_u32 v190, 8192, v190\n\t"
    "s_max_u32 s40, s40, s39\n\t"
    "v_mfma_f32_16x16x32_bf16 v[84:87], a[28:31], a[224:227], v[84:87]\n\t"
    "global_store_dwordx4 v190, v[156:159], s[18:19] sc1\n\t"
    "s_lshr_b32 s36, s92, 8\n\t"
    "v_mfma_f32_16x16x32_bf16 v[84:87], a[24:27], a[220:223], v[84:87]\n\t"
    "v_add_u32 v190, 8192, v190\n\t"
    "s_add_u32 s36, s36, 161792\n\t"
    "v_mfma_f32_16x16x32_bf16 v[84:87], a[20:23], a[216:219], v[84:87]\n\t"
    "global_store_dwordx4 v190, v[160:163], s[18:19] sc1\n\t"
    "v_mov_b32_e64 v191, s36\n\t"
    "v_mfma_f32_16x16x32_bf16 v[84:87], a[16:19], a[212:215], v[84:87]\n\t"
    "ds_read_b128 a[212:215], v181 offset:18624\n\t"
    "v_add_u32 v190, 8192, v190\n\t"
    "v_mfma_f32_16x16x32_bf16 v[92:95], a[16:19], a[216:219], v[92:95]\n\t"
    "ds_read_b128 a[216:219], v181 offset:18752\n\t"
    "v_mov_b32_e64 v185, s40\n\t"
    "v_mfma_f32_16x16x32_bf16 v[92:95], a[20:23], a[220:223], v[92:95]\n\t"
    "ds_read_b64_tr_b16 a[4:5], v188 offset:55296\n\t"
    "ds_read_b64_tr_b16 a[6:7], v188 offset:55808\n\t"
    "v_mfma_f32_16x16x32_bf16 v[92:95], a[24:27], a[224:227], v[92:95]\n\t"
    "global_store_dwordx4 v190, v[164:167], s[18:19] sc1\n\t"
    "v_mfma_f32_16x16x32_bf16 v[92:95], a[28:31], a[228:231], v[92:95]\n\t"
    "v_add_u32 v190, 8192, v190\n\t"
    "v_mfma_f32_16x16x32_bf16 v[100:103], a[28:31], a[232:235], v[100:103]\n\t"
    "global_store_dwordx4 v190, v[168:171], s[18:19] sc1\n\t"
    "v_mfma_f32_16x16x32_bf16 v[100:103], a[24:27], a[228:231], v[100:103]\n\t"
    "ds_read_b64_tr_b16 a[8:9], v188 offset:59392\n\t"
    "ds_read_b64_tr_b16 a[10:11], v188 offset:59904\n\t"
    "v_mfma_f32_16x16x32_bf16 v[100:103], a[20:23], a[224:227], v[100:103]\n\t"
    "ds_write_b32 v191, v185\n\t"
    "v_mfma_f32_16x16x32_bf16 v[100:103], a[16:19], a[220:223], v[100:103]\n\t"
    "ds_read_b128 a[220:223], v181 offset:18880\n\t"
    "s_max_u32 s40, s29, s22\n\t"
    "v_mfma_f32_16x16x32_bf16 v[108:111], a[16:19], a[224:227], v[108:111]\n\t"
    "ds_read_b128 a[224:227], v181 offset:19008\n\t"
    "s_lshr_b32 s40, s40, 7\n\t"
    "v_mfma_f32_16x16x32_bf16 v[108:111], a[20:23], a[228:231], v[108:111]\n\t"
    "buffer_load_dwordx4 v[136:139], v243, s[8:11], 0 offen offset:0\n\t"
    "s_sub_i32 s40, s40, 19\n\t"
    "v_mfma_f32_16x16x32_bf16 v[108:111], a[24:27], a[232:235], v[108:111]\n\t"
    "s_sub_i32 s37, 250, s47\n\t"
    "s_nop 0\n\t"
    "v_mfma_f32_16x16x32_bf16 v[108:111], a[28:31], a[236:239], v[108:111]\n\t"
    "ds_read_b64_tr_b16 a[12:13], v188 offset:63488\n\t"
    "ds_read_b64_tr_b16 a[14:15], v188 offset:64000\n\t"
    "v_mfma_f32_16x16x32_bf16 v[116:119], a[28:31], a[240:243], v[116:119]\n\t"
    "buffer_load_dwordx4 v[200:203], v243, s[8:11], 0 offen offset:64\n\t"
    "s_sub_i32 s38, s40, s37\n\t"
    "v_mfma_f32_16x16x32_bf16 v[116:119], a[24:27], a[236:239], v[116:119]\n\t"
    "s_add_i32 s38, s38, 4\n\t"
    "s_cmp_lt_u32 s38, 8\n\ts_cselect_b32 s40, s37, s40\n\t"
    "v_mfma_f32_16x16x32_bf16 v[116:119], a[20:23], a[232:235], v[116:119]\n\t"
    "s_max_i32 s40, s40, 0\n\t"
    "s_nop 0\n\t"
    "v_mfma_f32_16x16x32_bf16 v[116:119], a[16:19], a[228:231], v[116:119]\n\t"
    "ds_read_b128 a[228:231], v181 offset:19136\n\t"
    "s_sub_i32 s36, 140, s47\n\t"
    "v_mfma_f32_16x16x32_bf16 v[124:127], a[16:19], a[232:235], v[124:127]\n\t"
    "ds_read_b128 a[232:235], v181 offset:19264\n\t"
    "v_mfma_f32_16x16x32_bf16 v[124:127], a[20:23], a[236:239], v[124:127]\n\t"
    "s_max_i32 s40, s40, s36\n\t"
    "s_nop 0\n\t"
    "v_mfma_f32_16x16x32_bf16 v[124:127], a[24:27], a[240:243], v[124:127]\n\t"
    "s_add_i32 s42, s40, s47\n\t"
    "s_nop 0\n\t"
    "v_mfma_f32_16x16x32_bf16 v[124:127], a[28:31], a[244:247], v[124:127]\n\t"
    "s_sub_i32 s46, 250, s42\n\t"
    "v_mfma_f32_16x16x32_bf16 v[132:135], a[28:31], a[248:251], v[132:135]\n\t"
    "ds_read_b128 a[248:251], v182 offset:18752\n\t"
    "s_max_i32 s46, s46, -110\n\t"
    "v_mfma_f32_16x16x32_bf16 v[132:135], a[24:27], a[244:247], v[132:135]\n\t"
    "ds_read_b128 a[244:247], v182 offset:18624\n\t"
    "s_lshl_b32 s46, s46, 23\n\t"
    "v_mfma_f32_16x16x32_bf16 v[132:135], a[20:23], a[240:243], v[132:135]\n\t"
    "ds_read_b128 a[240:243], v183 offset:18496\n\t"
    "s_add_i32 s44, s40, 27\n\t"
    "v_mfma_f32_16x16x32_bf16 v[132:135], a[16:19], a[236:239], v[132:135]\n\t"
    "s_waitcnt vmcnt(25) lgkmcnt(5)\n\t"
    "s_barrier\n\t"
    "v_mfma_f32_16x16x32_bf16 v[72:75], a[0:3], a[208:211], v[72:75]\n\t"
    "ds_read_b128 a[236:239], v181 offset:19392\n\t"
    "s_lshl_b32 s44, s44, 7\n\t"
    "v_mfma_f32_16x16x32_bf16 v[72:75], a[4:7], a[212:215], v[72:75]\n\t"
    "ds_read_b64_tr_b16 a[16:17], v189 offset:2048\n\t"
    "ds_read_b64_tr_b16 a[18:19], v189 offset:2560\n\t"
    "v_mfma_f32_16x16x32_bf16 v[72:75], a[8:11], a[216:219], v[72:75]\n\t"
    "buffer_load_dwordx4 v[140:143], v243, s[8:11], 0 offen offset:1024\n\t"
    "s_sub_i32 s44, s44, 1\n\t"
    "v_mfma_f32_16x16x32_bf16 v[72:75], a[12:15], a[220:223], v[72:75]\n\t"
    "s_min_u32 s44, s44, 0x7bff\n\t"
    "s_lshl_b32 s29, s40, 7\n\t"
    "v_mfma_f32_16x16x32_bf16 v[80:83], a[12:15], a[224:227], v[80:83]\n\t"
    "buffer_load_dwordx4 v[204:207], v243, s[8:11], 0 offen offset:1088\n\t"
    "s_min_u32 s29, s29, s44\n\t"
    "v_mfma_f32_16x16x32_bf16 v[80:83], a[8:11], a[220:223], v[80:83]\n\t"
    "s_lshl_b32 s36, s29, 19\n\t"
    "s_nop 0\n\t"
    "v_mfma_f32_16x16x32_bf16 v[80:83], a[4:7], a[216:219], v[80:83]\n\t"
    "s_sub_u32 s29, s29, s36\n\t"
    "s_nop 0\n\t"
    "v_mfma_f32_16x16x32_bf16 v[80:83], a[0:3], a[212:215], v[80:83]\n\t"
    "s_waitcnt lgkmcnt(6)\n\t"
    "ds_read_b64_tr_b16 a[20:21], v189 offset:6144\n\t"
    "v_mfma_f32_16x16x32_bf16 v[88:91], a[0:3], a[216:219], v[88:91]\n\t"
    "ds_read_b64_tr_b16 a[22:23], v189 offset:6656\n\t"
    "s_add_u32 s48, s46, 0x3f800000\n\t"
    "v_mfma_f32_16x16x32_bf16 v[88:91], a[4:7], a[220:223], v[88:91]\n\t"
    "s_mov_b32 s49, 0xbe800000\n\t"
    "s_mov_b32 s50, 0xc0400000\n\t"
    "v_mfma_f32_16x16x32_bf16 v[88:91], a[8:11], a[224:227], v[88:91]\n\t"
    "s_mov_b32 s51, 0x41800000\n\t"
    "v_mfma_f32_16x16x32_bf16 v[88:91], a[12:15], a[228:231], v[88:91]\n\t"
    "s_mov_b32 s52, 0x3d800000\n\t"
    "v_mfma_f32_16x16x32_bf16 v[96:99], a[12:15], a[232:235], v[96:99]\n\t"
    "v_mfma_f32_16x16x32_bf16 v[96:99], a[8:11], a[228:231], v[96:99]\n\t"
    "s_waitcnt lgkmcnt(4)\n\t"
    "ds_read_b64_tr_b16 a[24:25], v189 offset:10240\n\t"
    "v_mfma_f32_16x16x32_bf16 v[96:99], a[4:7], a[224:227], v[96:99]\n\t"
    "ds_read_b64_tr_b16 a[26:27], v189 offset:10752\n\t"
    "s_mov_b32 s53, 0x4b000000\n\t"
    "v_mfma_f32_16x16x32_bf16 v[96:99], a[0:3], a[220:223], v[96:99]\n\t"
    "buffer_load_dwordx4 v[144:147], v243, s[8:11], 0 offen offset:2048\n\t"
    "s_sub_u32 s56, 0x3fb8aa3b, s46\n\t"
    "v_mfma_f32_16x16x32_bf16 v[104:107], a[0:3], a[224:227], v[104:107]\n\t"
    "s_sub_u32 s58, 0x3f38aa3b, s46\n\t"
    "s_sub_u32 s59, 0x3e76384f, s46\n\t"
    "v_mfma_f32_16x16x32_bf16 v[104:107], a[4:7], a[228:231], v[104:107]\n\t"
    "buffer_load_dwordx4 v[208:211], v243, s[8:11], 0 offen offset:2112\n\t"
    "s_sub_u32 s60, 0x3df6384f, s46\n\t"
    "v_mfma_f32_16x16x32_bf16 v[104:107], a[8:11], a[232:235], v[104:107]\n\t"
    "s_sub_u32 s61, 0x3d76384f, s46\n\t"
    "s_nop 0\n\t"
    "v_mfma_f32_16x16x32_bf16 v[104:107], a[12:15], a[236:239], v[104:107]\n\t"
    "ds_read_b64_tr_b16 a[28:29], v189 offset:14336\n\t"
    "ds_read_b64_tr_b16 a[30:31], v189 offset:14848\n\t"
    "v_mfma_f32_16x16x32_bf16 v[112:115], a[12:15], a[240:243], v[112:115]\n\t"
    "s_mov_b32 s25, 128\n\t"
    "s_mov_b32 s26, 44800\n\t"
    "v_mfma_f32_16x16x32_bf16 v[112:115], a[8:11], a[236:239], v[112:115]\n\t"
    "v_mfma_f32_16x16x32_bf16 v[112:115], a[4:7], a[232:235], v[112:115]\n\t"
    "v_mfma_f32_16x16x32_bf16 v[112:115], a[0:3], a[228:231], v[112:115]\n\t"
    "v_mfma_f32_16x16x32_bf16 v[120:123], a[0:3], a[232:235], v[120:123]\n\t"
    "v_mfma_f32_16x16x32_bf16 v[120:123], a[4:7], a[236:239], v[120:123]\n\t"
    "v_mfma_f32_16x16x32_bf16 v[120:123], a[8:11], a[240:243], v[120:123]\n\t"
    "buffer_load_dwordx4 v[148:151], v243, s[8:11], 0 offen offset:3072\n\t"
    "v_mfma_f32_16x16x32_bf16 v[120:123], a[12:15], a[244:247], v[120:123]\n\t"
    "v_mfma_f32_16x16x32_bf16 v[128:131], a[12:15], a[248:251], v[128:131]\n\t"
    "buffer_load_dwordx4 v[212:215], v243, s[8:11], 0 offen offset:3136\n\t"
    "v_mfma_f32_16x16x32_bf16 v[128:131], a[8:11], a[244:247], v[128:131]\n\t"
    "v_mfma_f32_16x16x32_bf16 v[128:131], a[4:7], a[240:243], v[128:131]\n\t"
    "v_mfma_f32_16x16x32_bf16 v[128:131], a[0:3], a[236:239], v[128:131]\n\t"
    "s_waitcnt vmcnt(28) lgkmcnt(0)\n\t"
    "s_barrier\n\t"
    "v_mfma_f32_16x16x32_bf16 v[76:79], a[16:19], a[208:211], v[76:79]\n\t"
    "ds_read_b128 a[208:211], v181 offset:36864\n\t"
    "v_mfma_f32_16x16x32_bf16 v[76:79], a[20:23], a[212:215], v[76:79]\n\t"
    "ds_read_b64_tr_b16 a[0:1], v189 offset:18432\n\t"
    "ds_read_b64_tr_b16 a[2:3], v189 offset:18944\n\t"
    "v_mfma_f32_16x16x32_bf16 v[76:79], a[24:27], a[216:219], v[76:79]\n\t"
    "v_mfma_f32_16x16x32_bf16 v[76:79], a[28:31], a[220:223], v[76:79]\n\t"
    "v_mfma_f32_16x16x32_bf16 v[84:87], a[28:31], a[224:227], v[84:87]\n\t"
    "v_mfma_f32_16x16x32_bf16 v[84:87], a[24:27], a[220:223], v[84:87]\n\t"
    "buffer_load_dwordx4 v[152:155], v244, s[8:11], 0 offen offset:0\n\t"
    "v_mfma_f32_16x16x32_bf16 v[84:87], a[20:23], a[216:219], v[84:87]\n\t"
    "v_mfma_f32_16x16x32_bf16 v[84:87], a[16:19], a[212:215], v[84:87]\n\t"
    "ds_read_b128 a[212:215], v181 offset:36992\n\t"
    "v_mfma_f32_16x16x32_bf16 v[92:95], a[16:19], a[216:219], v[92:95]\n\t"
    "ds_read_b128 a[216:219], v181 offset:37120\n\t"
    "v_mfma_f32_16x16x32_bf16 v[92:95], a[20:23], a[220:223], v[92:95]\n\t"
    "ds_read_b64_tr_b16 a[4:5], v189 offset:22528\n\t"
    "ds_read_b64_tr_b16 a[6:7], v189 offset:23040\n\t"
    "v_mfma_f32_16x16x32_bf16 v[92:95], a[24:27], a[224:227], v[92:95]\n\t"
    "buffer_load_dwordx4 v[216:219], v244, s[8:11], 0 offen offset:64\n\t"
    "v_mfma_f32_16x16x32_bf16 v[92:95], a[28:31], a[228:231], v[92:95]\n\t"
    "v_mfma_f32_16x16x32_bf16 v[100:103], a[28:31], a[232:235], v[100:103]\n\t"
    "v_mfma_f32_16x16x32_bf16 v[100:103], a[24:27], a[228:231], v[100:103]\n\t"
    "v_mfma_f32_16x16x32_bf16 v[100:103], a[20:23], a[224:227], v[100:103]\n\t"
    "v_mfma_f32_16x16x32_bf16 v[100:103], a[16:19], a[220:223], v[100:103]\n\t"
    "ds_read_b128 a[220:223], v181 offset:37248\n\t"
    "v_mfma_f32_16x16x32_bf16 v[108:111], a[16:19], a[224:227], v[108:111]\n\t"
    "ds_read_b128 a[224:227], v181 offset:37376\n\t"
    "v_mfma_f32_16x16x32_bf16 v[108:111], a[20:23], a[228:231], v[108:111]\n\t"
    "ds_read_b64_tr_b16 a[8:9], v189 offset:26624\n\t"
    "ds_read_b64_tr_b16 a[10:11], v189 offset:27136\n\t"
    "v_mfma_f32_16x16x32_bf16 v[108:111], a[24:27], a[232:235], v[108:111]\n\t"
    "v_mfma_f32_16x16x32_bf16 v[108:111], a[28:31], a[236:239], v[108:111]\n\t"
    "buffer_load_dwordx4 v[156:159], v244, s[8:11], 0 offen offset:1024\n\t"
    "v_mfma_f32_16x16x32_bf16 v[116:119], a[28:31], a[240:243], v[116:119]\n\t"
    "v_mfma_f32_16x16x32_bf16 v[116:119], a[24:27], a[236:239], v[116:119]\n\t"
    "buffer_load_dwordx4 v[220:223], v244, s[8:11], 0 offen offset:1088\n\t"
    "v_mfma_f32_16x16x32_bf16 v[116:119], a[20:23], a[232:235], v[116:119]\n\t"
    "v_mfma_f32_16x16x32_bf16 v[116:119], a[16:19], a[228:231], v[116:119]\n\t"
    "ds_read_b128 a[228:231], v181 offset:37504\n\t"
    "v_mfma_f32_16x16x32_bf16 v[124:127], a[16:19], a[232:235], v[124:127]\n\t"
    "ds_read_b128 a[232:235], v181 offset:37632\n\t"
    "v_mfma_f32_16x16x32_bf16 v[124:127], a[20:23], a[236:239], v[124:127]\n\t"
    "v_mfma_f32_16x16x32_bf16 v[124:127], a[24:27], a[240:243], v[124:127]\n\t"
    "v_mfma_f32_16x16x32_bf16 v[124:127], a[28:31], a[244:247], v[124:127]\n\t"
    "v_mfma_f32_16x16x32_bf16 v[132:135], a[28:31], a[248:251], v[132:135]\n\t"
    "v_mfma_f32_16x16x32_bf16 v[132:135], a[24:27], a[244:247], v[132:135]\n\t"
    "ds_read_b128 a[244:247], v182 offset:36992\n\t"
    "v_mfma_f32_16x16x32_bf16 v[132:135], a[20:23], a[240:243], v[132:135]\n\t"
    "ds_read_b128 a[240:243], v183 offset:36864\n\t"
    "v_mfma_f32_16x16x32_bf16 v[132:135], a[16:19], a[236:239], v[132:135]\n\t"
    "s_waitcnt vmcnt(29) lgkmcnt(4)\n\t"
    "s_barrier\n\t"
    "v_mfma_f32_16x16x32_bf16 v[0:3], a[0:3], a[208:211], 0\n\t"
    "ds_read_b128 a[236:239], v181 offset:37760\n\t"
    "v_accvgpr_write_b32 a253, v251\n\t"
    "v_mfma_f32_16x16x32_bf16 v[0:3], a[4:7], a[212:215], v[0:3]\n\t"
    "ds_read_b64_tr_b16 a[16:17], v187 offset:16384\n\t"
    "ds_read_b64_tr_b16 a[18:19], v187 offset:16896\n\t"
    "v_mfma_f32_16x16x32_bf16 v[0:3], a[8:11], a[216:219], v[0:3]\n\t"
    "buffer_load_dwordx4 v[160:163], v244, s[8:11], 0 offen offset:2048\n\t"
    "v_mfma_f32_16x16x32_bf16 v[8:11], a[8:11], a[220:223], 0\n\t"
    "buffer_load_dword v197, v193, s[88:91], 0 offen offset:0 sc1\n\t"
    "v_mfma_f32_16x16x32_bf16 v[8:11], a[4:7], a[216:219], v[8:11]\n\t"
    "buffer_load_dwordx4 v[224:227], v244, s[8:11], 0 offen offset:2112\n\t"
    "v_mfma_f32_16x16x32_bf16 v[8:11], a[0:3], a[212:215], v[8:11]\n\t"
    "s_waitcnt lgkmcnt(5)\n\t"
    "s_nop 0\n\t"
    "v_mfma_f32_16x16x32_bf16 v[16:19], a[0:3], a[216:219], 0\n\t"
    "ds_read_b64_tr_b16 a[20:21], v187 offset:20480\n\t"
    "ds_read_b64_tr_b16 a[22:23], v187 offset:20992\n\t"
    "v_mfma_f32_16x16x32_bf16 v[16:19], a[4:7], a[220:223], v[16:19]\n\t"
    "v_mfma_f32_16x16x32_bf16 v[16:19], a[8:11], a[224:227], v[16:19]\n\t"
    "s_add_u32 m0, s92, 8192\n\t"
    "v_mfma_f32_16x16x32_bf16 v[24:27], a[8:11], a[228:231], 0\n\t"
    "buffer_load_dwordx4 v186, s[84:87], s68 offen offset:0 lds\n\t"
    "v_mfma_f32_16x16x32_bf16 v[24:27], a[4:7], a[224:227], v[24:27]\n\t"
    "v_mfma_f32_16x16x32_bf16 v[24:27], a[0:3], a[220:223], v[24:27]\n\t"
    "s_add_u32 m0, s92, 11776\n\t"
    "v_mfma_f32_16x16x32_bf16 v[32:35], a[0:3], a[224:227], 0\n\t"
    "ds_read_b64_tr_b16 a[24:25], v187 offset:24576\n\t"
    "ds_read_b64_tr_b16 a[26:27], v187 offset:25088\n\t"
    "v_mfma_f32_16x16x32_bf16 v[32:35], a[4:7], a[228:231], v[32:35]\n\t"
    "s_waitcnt lgkmcnt(6)\n\t"
    "s_nop 0\n\t"
    "v_mfma_f32_16x16x32_bf16 v[32:35], a[8:11], a[232:235], v[32:35]\n\t"
    "buffer_load_dwordx4 v[164:167], v244, s[8:11], 0 offen offset:3072\n\t"
    "v_mfma_f32_16x16x32_bf16 v[40:43], a[8:11], a[236:239], 0\n\t"
    "buffer_load_dwordx4 v186, s[84:87], s64 offen offset:512 lds\n\t"
    "v_mfma_f32_16x16x32_bf16 v[40:43], a[4:7], a[232:235], v[40:43]\n\t"
    "buffer_load_dwordx4 v[228:231], v244, s[8:11], 0 offen offset:3136\n\t"
    "v_mfma_f32_16x16x32_bf16 v[40:43], a[0:3], a[228:231], v[40:43]\n\t"
    "s_add_u32 m0, s92, 108032\n\t"
    "v_mfma_f32_16x16x32_bf16 v[48:51], a[0:3], a[232:235], 0\n\t"
    "buffer_load_dwordx4 v186, s[84:87], s66 offen offset:512 lds\n\t"
    "v_mfma_f32_16x16x32_bf16 v[48:51], a[4:7], a[236:239], v[48:51]\n\t"
    "v_mfma_f32_16x16x32_bf16 v[48:51], a[8:11], a[240:243], v[48:51]\n\t"
    "s_add_u32 m0, s92, 112128\n\t"
    "v_mfma_f32_16x16x32_bf16 v[56:59], a[8:11], a[244:247], 0\n\t"
    "buffer_load_dwordx4 v186, s[84:87], s68 offen offset:512 lds\n\t"
    "v_mfma_f32_16x16x32_bf16 v[56:59], a[4:7], a[240:243], v[56:59]\n\t"
    "v_mfma_f32_16x16x32_bf16 v[56:59], a[0:3], a[236:239], v[56:59]\n\t"
    "s_waitcnt vmcnt(35) lgkmcnt(0)\n\t"
    "s_barrier\n\t"
    "v_mfma_f32_16x16x32_bf16 v[4:7], a[16:19], a[208:211], 0\n\t"
    "ds_read_b128 a[208:211], v181 offset:36928\n\t"
    "v_xor_b32 v186, 0x8000, v186\n\t"
    "v_mfma_f32_16x16x32_bf16 v[4:7], a[20:23], a[212:215], v[4:7]\n\t"
    "ds_read_b64_tr_b16 a[0:1], v188 offset:18432\n\t"
    "ds_read_b64_tr_b16 a[2:3], v188 offset:18944\n\t"
    "v_mfma_f32_16x16x32_bf16 v[4:7], a[24:27], a[216:219], v[4:7]\n\t"
    "buffer_load_dword v168, v180, s[8:11], 0 offen offset:0\n\t"
    "s_add_u32 m0, s92, 116736\n\t"
    "v_mfma_f32_16x16x32_bf16 v[12:15], a[24:27], a[220:223], 0\n\t"
    "buffer_load_dwordx4 v186, s[84:87], s64 offen offset:0 lds\n\t"
    "v_mfma_f32_16x16x32_bf16 v[12:15], a[20:23], a[216:219], v[12:15]\n\t"
    "v_mov_b32_dpp v64, v128 row_shr:1 row_mask:0xf bank_mask:0xf bound_ctrl:1\n\t"
    "v_mov_b32_dpp v65, v129 row_shr:1 row_mask:0xf bank_mask:0xf bound_ctrl:1\n\t"
    "v_mfma_f32_16x16x32_bf16 v[12:15], a[16:19], a[212:215], v[12:15]\n\t"
    "ds_read_b128 a[212:215], v181 offset:37056\n\t"
    "s_add_u32 m0, s92, 120832\n\t"
    "v_mfma_f32_16x16x32_bf16 v[20:23], a[16:19], a[216:219], 0\n\t"
    "ds_read_b128 a[216:219], v181 offset:37184\n\t"
    "v_mov_b32_dpp v66, v130 row_shr:1 row_mask:0xf bank_mask:0xf bound_ctrl:1\n\t"
    "v_mfma_f32_16x16x32_bf16 v[20:23], a[20:23], a[220:223], v[20:23]\n\t"
    "ds_read_b64_tr_b16 a[4:5], v188 offset:22528\n\t"
    "ds_read_b64_tr_b16 a[6:7], v188 offset:23040\n\t"
    "v_mfma_f32_16x16x32_bf16 v[20:23], a[24:27], a[224:227], v[20:23]\n\t"
    "buffer_load_dwordx4 v186, s[84:87], s66 offen offset:0 lds\n\t"
    "v_mfma_f32_16x16x32_bf16 v[28:31], a[24:27], a[228:231], 0\n\t"
    "v_mov_b32_dpp v67, v131 row_shr:1 row_mask:0xf bank_mask:0xf bound_ctrl:1\n\t"
    "v_mfma_f32_16x16x32_bf16 v[28:31], a[20:23], a[224:227], v[28:31]\n\t"
    "s_add_u32 m0, s92, 124928\n\t"
    "v_mfma_f32_16x16x32_bf16 v[28:31], a[16:19], a[220:223], v[28:31]\n\t"
    "ds_read_b128 a[220:223], v181 offset:37312\n\t"
    "v_mfma_f32_16x16x32_bf16 v[36:39], a[16:19], a[224:227], 0\n\t"
    "ds_read_b128 a[224:227], v181 offset:37440\n\t"
    "v_mov_b32_dpp v68, v132 row_shr:1 row_mask:0xf bank_mask:0xf bound_ctrl:1\n\t"
    "v_mfma_f32_16x16x32_bf16 v[36:39], a[20:23], a[228:231], v[36:39]\n\t"
    "ds_read_b64_tr_b16 a[8:9], v188 offset:26624\n\t"
    "ds_read_b64_tr_b16 a[10:11], v188 offset:27136\n\t"
    "v_mfma_f32_16x16x32_bf16 v[36:39], a[24:27], a[232:235], v[36:39]\n\t"
    "buffer_load_dword v169, v180, s[8:11], 0 offen offset:1024\n\t"
    "v_mfma_f32_16x16x32_bf16 v[44:47], a[24:27], a[236:239], 0\n\t"
    "buffer_load_dwordx4 v186, s[84:87], s68 offen offset:0 lds\n\t"
    "v_mfma_f32_16x16x32_bf16 v[44:47], a[20:23], a[232:235], v[44:47]\n\t"
    "v_mov_b32_dpp v69, v133 row_shr:1 row_mask:0xf bank_mask:0xf bound_ctrl:1\n\t"
    "v_mfma_f32_16x16x32_bf16 v[44:47], a[16:19], a[228:231], v[44:47]\n\t"
    "ds_read_b128 a[228:231], v181 offset:37568\n\t"
    "s_add_u32 m0, s92, 128512\n\t"
    "v_mfma_f32_16x16x32_bf16 v[52:55], a[16:19], a[232:235], 0\n\t"
    "ds_read_b128 a[232:235], v181 offset:37696\n\t"
    "v_mov_b32_dpp v70, v134 row_shr:1 row_mask:0xf bank_mask:0xf bound_ctrl:1\n\t"
    "v_mfma_f32_16x16x32_bf16 v[52:55], a[20:23], a[236:239], v[52:55]\n\t"
    "buffer_load_dwordx4 v186, s[84:87], s64 offen offset:512 lds\n\t"
    "v_mfma_f32_16x16x32_bf16 v[52:55], a[24:27], a[240:243], v[52:55]\n\t"
    "v_mfma_f32_16x16x32_bf16 v[60:63], a[24:27], a[244:247], 0\n\t"
    "ds_read_b128 a[244:247], v182 offset:37056\n\t"
    "s_add_u32 m0, s92, 132608\n\t"
    "v_mfma_f32_16x16x32_bf16 v[60:63], a[20:23], a[240:243], v[60:63]\n\t"
    "ds_read_b128 a[240:243], v183 offset:36928\n\t"
    "v_mov_b32_dpp v71, v135 row_shr:1 row_mask:0xf bank_mask:0xf bound_ctrl:1\n\t"
    "v_mfma_f32_16x16x32_bf16 v[60:63], a[16:19], a[236:239], v[60:63]\n\t"
    "s_waitcnt vmcnt(36) lgkmcnt(4)\n\t"
    "s_barrier\n\t"
    "v_mfma_f32_16x16x32_bf16 v[0:3], a[0:3], a[208:211], v[0:3]\n\t"
    "ds_read_b128 a[236:239], v181 offset:37824\n\t"
    "s_waitcnt vmcnt(20)\n\t"
    "v_mfma_f32_16x16x32_bf16 v[0:3], a[4:7], a[212:215], v[0:3]\n\t"
    "ds_read_b64_tr_b16 a[16:17], v188 offset:30720\n\t"
    "ds_read_b64_tr_b16 a[18:19], v188 offset:31232\n\t"
    "v_mfma_f32_16x16x32_bf16 v[0:3], a[8:11], a[216:219], v[0:3]\n\t"
    "buffer_load_dword v170, v180, s[8:11], 0 offen offset:2048\n\t"
    "v_pk_maximum3_f16 v232, v136, v136, s29 op_sel:[0,0,0] op_sel_hi:[1,1,0] neg_lo:[0,1,0] neg_hi:[0,1,0]\n\t"
    "v_mfma_f32_16x16x32_bf16 v[8:11], a[8:11], a[220:223], v[8:11]\n\t"
    "buffer_load_dwordx4 v186, s[84:87], s66 offen offset:512 lds\n\t"
    "v_mfma_f32_16x16x32_bf16 v[8:11], a[4:7], a[216:219], v[8:11]\n\t"
    "v_pk_maximum3_f16 v233, v140, v140, s29 op_sel:[0,0,0] op_sel_hi:[1,1,0] neg_lo:[0,1,0] neg_hi:[0,1,0]\n\t"
    "v_pk_maximum3_f16 v234, v144, v144, s29 op_sel:[0,0,0] op_sel_hi:[1,1,0] neg_lo:[0,1,0] neg_hi:[0,1,0]\n\t"
    "v_mfma_f32_16x16x32_bf16 v[8:11], a[0:3], a[212:215], v[8:11]\n\t"
    "s_waitcnt lgkmcnt(5)\n\t"
    "s_add_u32 m0, s92, 136704\n\t"
    "v_mfma_f32_16x16x32_bf16 v[16:19], a[0:3], a[216:219], v[16:19]\n\t"
    "ds_read_b64_tr_b16 a[20:21], v188 offset:34816\n\t"
    "ds_read_b64_tr_b16 a[22:23], v188 offset:35328\n\t"
    "v_mfma_f32_16x16x32_bf16 v[16:19], a[4:7], a[220:223], v[16:19]\n\t"
    "buffer_load_dwordx4 v186, s[84:87], s68 offen offset:512 lds\n\t"
    "v_mfma_f32_16x16x32_bf16 v[16:19], a[8:11], a[224:227], v[16:19]\n\t"
    "v_pk_maximum3_f16 v235, v148, v148, s29 op_sel:[0,0,0] op_sel_hi:[1,1,0] neg_lo:[0,1,0] neg_hi:[0,1,0]\n\t"
    "v_pk_maximum3_f16 v249, v232, v233, v249\n\t"
    "v_mfma_f32_16x16x32_bf16 v[24:27], a[8:11], a[228:231], v[24:27]\n\t"
    "v_xor_b32 v186, 0x8000, v186\n\t"
    "v_add_u32 v186, 0x10000, v186\n\t"
    "v_mfma_f32_16x16x32_bf16 v[24:27], a[4:7], a[224:227], v[24:27]\n\t"
    "v_and_b32 v186, 0x3ffff, v186\n\t"
    "s_add_u32 m0, s92, 141312\n\t"
    "v_mfma_f32_16x16x32_bf16 v[24:27], a[0:3], a[220:223], v[24:27]\n\t"
    "buffer_load_dwordx4 v186, s[84:87], s64 offen offset:0 lds\n\t"
    "v_mfma_f32_16x16x32_bf16 v[32:35], a[0:3], a[224:227], v[32:35]\n\t"
    "ds_read_b64_tr_b16 a[24:25], v188 offset:38912\n\t"
    "ds_read_b64_tr_b16 a[26:27], v188 offset:39424\n\t"
    "v_mfma_f32_16x16x32_bf16 v[32:35], a[4:7], a[228:231], v[32:35]\n\t"
    "s_waitcnt lgkmcnt(6)\n\t"
    "s_add_u32 m0, s92, 145408\n\t"
    "v_mfma_f32_16x16x32_bf16 v[32:35], a[8:11], a[232:235], v[32:35]\n\t"
    "buffer_load_dword v171, v180, s[8:11], 0 offen offset:3072\n\t"
    "v_pk_maximum3_f16 v249, v234, v235, v249\n\t"
    "v_mfma_f32_16x16x32_bf16 v[40:43], a[8:11], a[236:239], v[40:43]\n\t"
    "buffer_load_dwordx4 v186, s[84:87], s66 offen offset:0 lds\n\t"
    "v_mfma_f32_16x16x32_bf16 v[40:43], a[4:7], a[232:235], v[40:43]\n\t"
    "v_pk_mad_u16 v232, v232, 8, s29 op_sel:[0,0,1] op_sel_hi:[1,0,1]\n\t"
    "v_pk_mad_u16 v233, v233, 8, s29 op_sel:[0,0,1] op_sel_hi:[1,0,1]\n\t"
    "v_mfma_f32_16x16x32_bf16 v[40:43], a[0:3], a[228:231], v[40:43]\n\t"
    "s_add_u32 s57, s57, 128\n\t"
    "s_and_b32 s57, s57, 0x1c0\n\t"
    "v_mfma_f32_16x16x32_bf16 v[48:51], a[0:3], a[232:235], v[48:51]\n\t"
    "s_add_u32 m0, s20, 47104\n\t"
    "v_pk_mad_u16 v234, v234, 8, s29 op_sel:[0,0,1] op_sel_hi:[1,0,1]\n\t"
    "v_mfma_f32_16x16x32_bf16 v[48:51], a[4:7], a[236:239], v[48:51]\n\t"
    "buffer_load_dwordx4 v176, s[8:11], s57 offen offset:0 lds\n\t"
    "v_mfma_f32_16x16x32_bf16 v[48:51], a[8:11], a[240:243], v[48:51]\n\t"
    "v_pk_mad_u16 v235, v235, 8, s29 op_sel:[0,0,1] op_sel_hi:[1,0,1]\n\t"
    "v_bfi_b32 v136, s24, v232, v136\n\t"
    "v_mfma_f32_16x16x32_bf16 v[56:59], a[8:11], a[244:247], v[56:59]\n\t"
    "s_add_u32 m0, s20, 48192\n\t"
    "v_bfi_b32 v140, s24, v233, v140\n\t"
    "v_mfma_f32_16x16x32_bf16 v[56:59], a[4:7], a[240:243], v[56:59]\n\t"
    "buffer_load_dwordx4 v177, s[8:11], s57 offen offset:0 lds\n\t"
    "v_mfma_f32_16x16x32_bf16 v[56:59], a[0:3], a[236:239], v[56:59]\n\t"
    "s_waitcnt vmcnt(19) lgkmcnt(0)\n\t"
    "s_barrier\n\t"
    "v_mfma_f32_16x16x32_bf16 v[4:7], a[16:19], a[208:211], v[4:7]\n\t"
    "ds_read_b128 a[208:211], v181 offset:0\n\t"
    "s_add_u32 m0, s92, 149504\n\t"
    "v_mfma_f32_16x16x32_bf16 v[4:7], a[20:23], a[212:215], v[4:7]\n\t"
    "ds_read_b64_tr_b16 a[0:1], v187 offset:0\n\t"
    "ds_read_b64_tr_b16 a[2:3], v187 offset:512\n\t"
    "v_mfma_f32_16x16x32_bf16 v[4:7], a[24:27], a[216:219], v[4:7]\n\t"
    "buffer_load_dwordx4 v186, s[84:87], s68 offen offset:0 lds\n\t"
    "v_mfma_f32_16x16x32_bf16 v[12:15], a[24:27], a[220:223], v[12:15]\n\t"
    "v_bfi_b32 v144, s24, v234, v144\n\t"
    "v_bfi_b32 v148, s24, v235, v148\n\t"
    "v_mfma_f32_16x16x32_bf16 v[12:15], a[20:23], a[216:219], v[12:15]\n\t"
    "s_add_u32 m0, s92, 153088\n\t"
    "v_pk_maximum3_f16 v232, v137, v137, s29 op_sel:[0,0,0] op_sel_hi:[1,1,0] neg_lo:[0,1,0] neg_hi:[0,1,0]\n\t"
    "v_mfma_f32_16x16x32_bf16 v[12:15], a[16:19], a[212:215], v[12:15]\n\t"
    "ds_read_b128 a[212:215], v181 offset:128\n\t"
    "v_pk_maximum3_f16 v233, v141, v141, s29 op_sel:[0,0,0] op_sel_hi:[1,1,0] neg_lo:[0,1,0] neg_hi:[0,1,0]\n\t"
    "v_mfma_f32_16x16x32_bf16 v[20:23], a[16:19], a[216:219], v[20:23]\n\t"
    "ds_read_b128 a[216:219], v181 offset:256\n\t"
    "v_pk_maximum3_f16 v234, v145, v145, s29 op_sel:[0,0,0] op_sel_hi:[1,1,0] neg_lo:[0,1,0] neg_hi:[0,1,0]\n\t"
    "v_mfma_f32_16x16x32_bf16 v[20:23], a[20:23], a[220:223], v[20:23]\n\t"
    "ds_read_b64_tr_b16 a[4:5], v187 offset:4096\n\t"
    "ds_read_b64_tr_b16 a[6:7], v187 offset:4608\n\t"
    "v_mfma_f32_16x16x32_bf16 v[20:23], a[24:27], a[224:227], v[20:23]\n\t"
    "buffer_load_dwordx4 v186, s[84:87], s64 offen offset:512 lds\n\t"
    "v_mfma_f32_16x16x32_bf16 v[28:31], a[24:27], a[228:231], v[28:31]\n\t"
    "v_pk_maximum3_f16 v235, v149, v149, s29 op_sel:[0,0,0] op_sel_hi:[1,1,0] neg_lo:[0,1,0] neg_hi:[0,1,0]\n\t"
    "v_pk_maximum3_f16 v249, v232, v233, v249\n\t"
    "v_mfma_f32_16x16x32_bf16 v[28:31], a[20:23], a[224:227], v[28:31]\n\t"
    "s_add_u32 m0, s92, 157184\n\t"
    "v_pk_maximum3_f16 v249, v234, v235, v249\n\t"
    "v_mfma_f32_16x16x32_bf16 v[28:31], a[16:19], a[220:223], v[28:31]\n\t"
    "ds_read_b128 a[220:223], v181 offset:384\n\t"
    "v_pk_mad_u16 v232, v232, 8, s29 op_sel:[0,0,1] op_sel_hi:[1,0,1]\n\t"
    "v_mfma_f32_16x16x32_bf16 v[36:39], a[16:19], a[224:227], v[36:39]\n\t"
    "ds_read_b128 a[224:227], v181 offset:512\n\t"
    "v_pk_mad_u16 v233, v233, 8, s29 op_sel:[0,0,1] op_sel_hi:[1,0,1]\n\t"
    "v_mfma_f32_16x16x32_bf16 v[36:39], a[20:23], a[228:231], v[36:39]\n\t"
    "ds_read_b64_tr_b16 a[8:9], v187 offset:8192\n\t"
    "ds_read_b64_tr_b16 a[10:11], v187 offset:8704\n\t"
    "v_mfma_f32_16x16x32_bf16 v[36:39], a[24:27], a[232:235], v[36:39]\n\t"
    "buffer_load_dwordx4 v186, s[84:87], s66 offen offset:512 lds\n\t"
    "v_mfma_f32_16x16x32_bf16 v[44:47], a[24:27], a[236:239], v[44:47]\n\t"
    "v_pk_mad_u16 v234, v234, 8, s29 op_sel:[0,0,1] op_sel_hi:[1,0,1]\n\t"
    "v_pk_mad_u16 v235, v235, 8, s29 op_sel:[0,0,1] op_sel_hi:[1,0,1]\n\t"
    "v_mfma_f32_16x16x32_bf16 v[44:47], a[20:23], a[232:235], v[44:47]\n\t"
    "s_add_u32 m0, s20, 49280\n\t"
    "v_bfi_b32 v137, s24, v232, v137\n\t"
    "v_mfma_f32_16x16x32_bf16 v[44:47], a[16:19], a[228:231], v[44:47]\n\t"
    "ds_read_b128 a[228:231], v181 offset:640\n\t"
    "v_bfi_b32 v141, s24, v233, v141\n\t"
    "v_mfma_f32_16x16x32_bf16 v[52:55], a[16:19], a[232:235], v[52:55]\n\t"
    "ds_read_b128 a[232:235], v181 offset:768\n\t"
    "v_bfi_b32 v145, s24, v234, v145\n\t"
    "v_mfma_f32_16x16x32_bf16 v[52:55], a[20:23], a[236:239], v[52:55]\n\t"
    "buffer_load_dwordx4 v178, s[8:11], s57 offen offset:0 lds\n\t"
    "v_mfma_f32_16x16x32_bf16 v[52:55], a[24:27], a[240:243], v[52:55]\n\t"
    "v_bfi_b32 v149, s24, v235, v149\n\t"
    "v_pk_maximum3_f16 v232, v138, v138, s29 op_sel:[0,0,0] op_sel_hi:[1,1,0] neg_lo:[0,1,0] neg_hi:[0,1,0]\n\t"
    "v_mfma_f32_16x16x32_bf16 v[60:63], a[24:27], a[244:247], v[60:63]\n\t"
    "ds_read_b128 a[244:247], v182 offset:128\n\t"
    "s_add_u32 m0, s20, 50368\n\t"
    "v_mfma_f32_16x16x32_bf16 v[60:63], a[20:23], a[240:243], v[60:63]\n\t"
    "ds_read_b128 a[240:243], v183 offset:0\n\t"
    "v_pk_maximum3_f16 v233, v142, v142, s29 op_sel:[0,0,0] op_sel_hi:[1,1,0] neg_lo:[0,1,0] neg_hi:[0,1,0]\n\t"
    "v_mfma_f32_16x16x32_bf16 v[60:63], a[16:19], a[236:239], v[60:63]\n\t"
    "s_waitcnt vmcnt(18) lgkmcnt(4)\n\t"
    "s_barrier\n\t"
    "v_mfma_f32_16x16x32_bf16 v[0:3], a[0:3], a[208:211], v[0:3]\n\t"
    "ds_read_b128 a[236:239], v181 offset:896\n\t"
    "v_mov_b32 v250, 161792\n\t"
    "v_mfma_f32_16x16x32_bf16 v[0:3], a[4:7], a[212:215], v[0:3]\n\t"
    "ds_read_b64_tr_b16 a[16:17], v187 offset:12288\n\t"
    "ds_read_b64_tr_b16 a[18:19], v187 offset:12800\n\t"
    "v_mfma_f32_16x16x32_bf16 v[0:3], a[8:11], a[216:219], v[0:3]\n\t"
    "buffer_load_dwordx4 v179, s[8:11], s57 offen offset:0 lds\n\t"
    "v_mfma_f32_16x16x32_bf16 v[8:11], a[8:11], a[220:223], v[8:11]\n\t"
    "ds_read_b128 v[172:175], v250\n\t"
    "v_mov_b32 v198, 0x1ff\n\t"
    "v_mfma_f32_16x16x32_bf16 v[8:11], a[4:7], a[216:219], v[8:11]\n\t"
    "s_add_u32 m0, s92, 83456\n\t"
    "v_pk_maximum3_f16 v234, v146, v146, s29 op_sel:[0,0,0] op_sel_hi:[1,1,0] neg_lo:[0,1,0] neg_hi:[0,1,0]\n\t"
    "v_mfma_f32_16x16x32_bf16 v[8:11], a[0:3], a[212:215], v[8:11]\n\t"
    "s_waitcnt lgkmcnt(6)\n\t"
    "v_pk_maximum3_f16 v235, v150, v150, s29 op_sel:[0,0,0] op_sel_hi:[1,1,0] neg_lo:[0,1,0] neg_hi:[0,1,0]\n\t"
    "v_mfma_f32_16x16x32_bf16 v[16:19], a[0:3], a[216:219], v[16:19]\n\t"
    "ds_read_b64_tr_b16 a[20:21], v188 offset:43008\n\t"
    "ds_read_b64_tr_b16 a[22:23], v188 offset:43520\n\t"
    "v_mfma_f32_16x16x32_bf16 v[16:19], a[4:7], a[220:223], v[16:19]\n\t"
    "buffer_load_dwordx4 v186, s[84:87], s68 offen offset:512 lds\n\t"
    "v_mfma_f32_16x16x32_bf16 v[16:19], a[8:11], a[224:227], v[16:19]\n\t"
    "s_waitcnt vmcnt(27) lgkmcnt(2)\n\t"
    "v_max3_u32 v172, v172, v173, v174\n\t"
    "v_mfma_f32_16x16x32_bf16 v[24:27], a[8:11], a[228:231], v[24:27]\n\t"
    "v_xor_b32 v186, 0x8000, v186\n\t"
    "s_add_u32 m0, s92, 88064\n\t"
    "v_mfma_f32_16x16x32_bf16 v[24:27], a[4:7], a[224:227], v[24:27]\n\t"
    "buffer_load_dwordx4 v186, s[84:87], s64 offen offset:0 lds\n\t"
    "v_mfma_f32_16x16x32_bf16 v[24:27], a[0:3], a[220:223], v[24:27]\n\t"
    "v_max_u32 v172, v172, v175\n\t"
    "v_pk_maximum3_f16 v249, v232, v233, v249\n\t"
    "v_mfma_f32_16x16x32_bf16 v[32:35], a[0:3], a[224:227], v[32:35]\n\t"
    "ds_read_b64_tr_b16 a[24:25], v188 offset:47104\n\t"
    "ds_read_b64_tr_b16 a[26:27], v188 offset:47616\n\t"
    "v_mfma_f32_16x16x32_bf16 v[32:35], a[4:7], a[228:231], v[32:35]\n\t"
    "s_waitcnt lgkmcnt(7)\n\t"
    "s_add_u32 m0, s92, 92160\n\t"
    "v_mfma_f32_16x16x32_bf16 v[32:35], a[8:11], a[232:235], v[32:35]\n\t"
    "buffer_load_dwordx4 v186, s[84:87], s66 offen offset:0 lds\n\t"
    "v_mfma_f32_16x16x32_bf16 v[40:43], a[8:11], a[236:239], v[40:43]\n\t"
    "v_readfirstlane_b32 s70, v172\n\t"
    "v_pk_maximum3_f16 v249, v234, v235, v249\n\t"
    "v_mfma_f32_16x16x32_bf16 v[40:43], a[4:7], a[232:235], v[40:43]\n\t"
    "s_add_u32 m0, s92, 96256\n\t"
    "v_pk_mad_u16 v232, v232, 8, s29 op_sel:[0,0,1] op_sel_hi:[1,0,1]\n\t"
    "v_mfma_f32_16x16x32_bf16 v[40:43], a[0:3], a[228:231], v[40:43]\n\t"
    "buffer_load_dwordx4 v186, s[84:87], s68 offen offset:0 lds\n\t"
    "v_mfma_f32_16x16x32_bf16 v[48:51], a[0:3], a[232:235], v[48:51]\n\t"
    "s_lshr_b32 s70, s70, 7\n\t"
    "s_min_u32 s70, s70, 255\n\t"
    "v_mfma_f32_16x16x32_bf16 v[48:51], a[4:7], a[236:239], v[48:51]\n\t"
    "s_add_u32 m0, s92, 99840\n\t"
    "s_and_b32 s71, s94, 256\n\t"
    "v_mfma_f32_16x16x32_bf16 v[48:51], a[8:11], a[240:243], v[48:51]\n\t"
    "buffer_load_dwordx4 v186, s[84:87], s64 offen offset:512 lds\n\t"
    "v_mfma_f32_16x16x32_bf16 v[56:59], a[8:11], a[244:247], v[56:59]\n\t"
    "s_or_b32 s70, s70, s71\n\t"
    "s_or_b32 s31, s31, s71\n\t"
    "v_mfma_f32_16x16x32_bf16 v[56:59], a[4:7], a[240:243], v[56:59]\n\t"
    "s_add_u32 m0, s92, 103936\n\t"
    "v_bfi_b32 v197, v198, s70, v197\n\t"
    "v_mfma_f32_16x16x32_bf16 v[56:59], a[0:3], a[236:239], v[56:59]\n\t"
    "s_waitcnt vmcnt(19) lgkmcnt(0)\n\t"
    "s_barrier\n\t"
    "v_mfma_f32_16x16x32_bf16 v[4:7], a[16:19], a[208:211], v[4:7]\n\t"
    "ds_read_b128 a[208:211], v181 offset:64\n\t"
    "v_pk_mad_u16 v233, v233, 8, s29 op_sel:[0,0,1] op_sel_hi:[1,0,1]\n\t"
    "v_mfma_f32_16x16x32_bf16 v[4:7], a[20:23], a[212:215], v[4:7]\n\t"
    "ds_read_b64_tr_b16 a[0:1], v188 offset:51200\n\t"
    "ds_read_b64_tr_b16 a[2:3], v188 offset:51712\n\t"
    "v_mfma_f32_16x16x32_bf16 v[4:7], a[24:27], a[216:219], v[4:7]\n\t"
    "buffer_load_dwordx4 v186, s[84:87], s66 offen offset:512 lds\n\t"
    "v_mfma_f32_16x16x32_bf16 v[12:15], a[24:27], a[220:223], v[12:15]\n\t"
    "buffer_store_dword v197, v196, s[88:91], 0 offen sc1\n\t"
    "v_pk_mad_u16 v234, v234, 8, s29 op_sel:[0,0,1] op_sel_hi:[1,0,1]\n\t"
    "v_mfma_f32_16x16x32_bf16 v[12:15], a[20:23], a[216:219], v[12:15]\n\t"
    "s_add_u32 s57, s57, 128\n\t"
    "s_and_b32 s57, s57, 0x1c0\n\t"
    "v_mfma_f32_16x16x32_bf16 v[12:15], a[16:19], a[212:215], v[12:15]\n\t"
    "ds_read_b128 a[212:215], v181 offset:192\n\t"
    "s_add_u32 m0, s20, 65536\n\t"
    "v_mfma_f32_16x16x32_bf16 v[20:23], a[16:19], a[216:219], v[20:23]\n\t"
    "ds_read_b128 a[216:219], v181 offset:320\n\t"
    "v_pk_mad_u16 v235, v235, 8, s29 op_sel:[0,0,1] op_sel_hi:[1,0,1]\n\t"
    "v_mfma_f32_16x16x32_bf16 v[20:23], a[20:23], a[220:223], v[20:23]\n\t"
    "ds_read_b64_tr_b16 a[4:5], v188 offset:55296\n\t"
    "ds_read_b64_tr_b16 a[6:7], v188 offset:55808\n\t"
    "v_mfma_f32_16x16x32_bf16 v[20:23], a[24:27], a[224:227], v[20:23]\n\t"
    "buffer_load_dwordx4 v176, s[8:11], s57 offen offset:0 lds\n\t"
    "v_mfma_f32_16x16x32_bf16 v[28:31], a[24:27], a[228:231], v[28:31]\n\t"
    "v_bfi_b32 v138, s24, v232, v138\n\t"
    "v_bfi_b32 v142, s24, v233, v142\n\t"
    "v_mfma_f32_16x16x32_bf16 v[28:31], a[20:23], a[224:227], v[28:31]\n\t"
    "s_add_u32 m0, s20, 66624\n\t"
    "v_bfi_b32 v146, s24, v234, v146\n\t"
    "v_mfma_f32_16x16x32_bf16 v[28:31], a[16:19], a[220:223], v[28:31]\n\t"
    "ds_read_b128 a[220:223], v181 offset:448\n\t"
    "v_bfi_b32 v150, s24, v235, v150\n\t"
    "v_mfma_f32_16x16x32_bf16 v[36:39], a[16:19], a[224:227], v[36:39]\n\t"
    "ds_read_b128 a[224:227], v181 offset:576\n\t"
    "v_pk_maximum3_f16 v232, v139, v139, s29 op_sel:[0,0,0] op_sel_hi:[1,1,0] neg_lo:[0,1,0] neg_hi:[0,1,0]\n\t"
    "v_mfma_f32_16x16x32_bf16 v[36:39], a[20:23], a[228:231], v[36:39]\n\t"
    "ds_read_b64_tr_b16 a[8:9], v188 offset:59392\n\t"
    "ds_read_b64_tr_b16 a[10:11], v188 offset:59904\n\t"
    "v_mfma_f32_16x16x32_bf16 v[36:39], a[24:27], a[232:235], v[36:39]\n\t"
    "buffer_load_dwordx4 v177, s[8:11], s57 offen offset:0 lds\n\t"
    "v_mfma_f32_16x16x32_bf16 v[44:47], a[24:27], a[236:239], v[44:47]\n\t"
    "v_pk_maximum3_f16 v233, v143, v143, s29 op_sel:[0,0,0] op_sel_hi:[1,1,0] neg_lo:[0,1,0] neg_hi:[0,1,0]\n\t"
    "v_pk_maximum3_f16 v234, v147, v147, s29 op_sel:[0,0,0] op_sel_hi:[1,1,0] neg_lo:[0,1,0] neg_hi:[0,1,0]\n\t"
    "v_mfma_f32_16x16x32_bf16 v[44:47], a[20:23], a[232:235], v[44:47]\n\t"
    "s_add_u32 m0, s20, 67712\n\t"
    "v_pk_maximum3_f16 v235, v151, v151, s29 op_sel:[0,0,0] op_sel_hi:[1,1,0] neg_lo:[0,1,0] neg_hi:[0,1,0]\n\t"
    "v_mfma_f32_16x16x32_bf16 v[44:47], a[16:19], a[228:231], v[44:47]\n\t"
    "ds_read_b128 a[228:231], v181 offset:704\n\t"
    "v_pk_maximum3_f16 v249, v232, v233, v249\n\t"
    "v_mfma_f32_16x16x32_bf16 v[52:55], a[16:19], a[232:235], v[52:55]\n\t"
    "ds_read_b128 a[232:235], v181 offset:832\n\t"
    "v_pk_maximum3_f16 v249, v234, v235, v249\n\t"
    "v_mfma_f32_16x16x32_bf16 v[52:55], a[20:23], a[236:239], v[52:55]\n\t"
    "buffer_load_dwordx4 v178, s[8:11], s57 offen offset:0 lds\n\t"
    "v_mfma_f32_16x16x32_bf16 v[52:55], a[24:27], a[240:243], v[52:55]\n\t"
    "v_pk_mad_u16 v232, v232, 8, s29 op_sel:[0,0,1] op_sel_hi:[1,0,1]\n\t"
    "v_pk_mad_u16 v233, v233, 8, s29 op_sel:[0,0,1] op_sel_hi:[1,0,1]\n\t"
    "v_mfma_f32_16x16x32_bf16 v[60:63], a[24:27], a[244:247], v[60:63]\n\t"
    "ds_read_b128 a[244:247], v182 offset:192\n\t"
    "s_add_u32 m0, s20, 68800\n\t"
    "v_mfma_f32_16x16x32_bf16 v[60:63], a[20:23], a[240:243], v[60:63]\n\t"
    "ds_read_b128 a[240:243], v183 offset:64\n\t"
    "v_pk_mad_u16 v234, v234, 8, s29 op_sel:[0,0,1] op_sel_hi:[1,0,1]\n\t"
    "v_mfma_f32_16x16x32_bf16 v[60:63], a[16:19], a[236:239], v[60:63]\n\t"
    "s_waitcnt vmcnt(20) lgkmcnt(4)\n\t"
    "s_barrier\n\t"
    "v_mfma_f32_16x16x32_bf16 v[0:3], a[0:3], a[208:211], v[0:3]\n\t"
    "ds_read_b128 a[236:239], v181 offset:960\n\t"
    "v_pk_mad_u16 v235, v235, 8, s29 op_sel:[0,0,1] op_sel_hi:[1,0,1]\n\t"
    "v_mfma_f32_16x16x32_bf16 v[0:3], a[4:7], a[212:215], v[0:3]\n\t"
    "ds_read_b64_tr_b16 a[16:17], v188 offset:63488\n\t"
    "ds_read_b64_tr_b16 a[18:19], v188 offset:64000\n\t"
    "v_mfma_f32_16x16x32_bf16 v[0:3], a[8:11], a[216:219], v[0:3]\n\t"
    "buffer_load_dwordx4 v179, s[8:11], s57 offen offset:0 lds\n\t"
    "v_mfma_f32_16x16x32_bf16 v[8:11], a[8:11], a[220:223], v[8:11]\n\t"
    "v_bfi_b32 v139, s24, v232, v139\n\t"
    "v_bfi_b32 v143, s24, v233, v143\n\t"
    "v_mfma_f32_16x16x32_bf16 v[8:11], a[4:7], a[216:219], v[8:11]\n\t"
    "s_add_u32 m0, s92, 108032\n\t"
    "v_bfi_b32 v147, s24, v234, v147\n\t"
    "v_mfma_f32_16x16x32_bf16 v[8:11], a[0:3], a[212:215], v[8:11]\n\t"
    "s_waitcnt lgkmcnt(5)\n\t"
    "v_bfi_b32 v151, s24, v235, v151\n\t"
    "v_mfma_f32_16x16x32_bf16 v[16:19], a[0:3], a[216:219], v[16:19]\n\t"
    "ds_read_b64_tr_b16 a[20:21], v189 offset:2048\n\t"
    "ds_read_b64_tr_b16 a[22:23], v189 offset:2560\n\t"
    "v_mfma_f32_16x16x32_bf16 v[16:19], a[4:7], a[220:223], v[16:19]\n\t"
    "buffer_load_dwordx4 v186, s[84:87], s68 offen offset:512 lds\n\t"
    "v_mfma_f32_16x16x32_bf16 v[16:19], a[8:11], a[224:227], v[16:19]\n\t"
    "s_waitcnt vmcnt(35)\n\t"
    "v_pk_maximum3_f16 v232, v152, v152, s29 op_sel:[0,0,0] op_sel_hi:[1,1,0] neg_lo:[0,1,0] neg_hi:[0,1,0]\n\t"
    "v_mfma_f32_16x16x32_bf16 v[24:27], a[8:11], a[228:231], v[24:27]\n\t"
    "v_xor_b32 v186, 0x8000, v186\n\t"
    "v_add_u32 v186, 0x10000, v186\n\t"
    "v_mfma_f32_16x16x32_bf16 v[24:27], a[4:7], a[224:227], v[24:27]\n\t"
    "v_and_b32 v186, 0x3ffff, v186\n\t"
    "s_add_u32 m0, s92, 112640\n\t"
    "v_mfma_f32_16x16x32_bf16 v[24:27], a[0:3], a[220:223], v[24:27]\n\t"
    "buffer_load_dwordx4 v186, s[84:87], s64 offen offset:0 lds\n\t"
    "v_mfma_f32_16x16x32_bf16 v[32:35], a[0:3], a[224:227], v[32:35]\n\t"
    "ds_read_b64_tr_b16 a[24:25], v189 offset:6144\n\t"
    "ds_read_b64_tr_b16 a[26:27], v189 offset:6656\n\t"
    "v_mfma_f32_16x16x32_bf16 v[32:35], a[4:7], a[228:231], v[32:35]\n\t"
    "s_waitcnt lgkmcnt(6)\n\t"
    "s_add_u32 m0, s92, 116736\n\t"
    "v_mfma_f32_16x16x32_bf16 v[32:35], a[8:11], a[232:235], v[32:35]\n\t"
    "buffer_load_dwordx4 v186, s[84:87], s66 offen offset:0 lds\n\t"
    "v_mfma_f32_16x16x32_bf16 v[40:43], a[8:11], a[236:239], v[40:43]\n\t"
    "v_pk_maximum3_f16 v233, v156, v156, s29 op_sel:[0,0,0] op_sel_hi:[1,1,0] neg_lo:[0,1,0] neg_hi:[0,1,0]\n\t"
    "v_pk_maximum3_f16 v234, v160, v160, s29 op_sel:[0,0,0] op_sel_hi:[1,1,0] neg_lo:[0,1,0] neg_hi:[0,1,0]\n\t"
    "v_mfma_f32_16x16x32_bf16 v[40:43], a[4:7], a[232:235], v[40:43]\n\t"
    "v_mad_u32_u24 v183, v248, s27, v183\n\t"
    "s_add_u32 m0, s92, 120832\n\t"
    "v_mfma_f32_16x16x32_bf16 v[40:43], a[0:3], a[228:231], v[40:43]\n\t"
    "buffer_load_dwordx4 v186, s[84:87], s68 offen offset:0 lds\n\t"
    "v_mfma_f32_16x16x32_bf16 v[48:51], a[0:3], a[232:235], v[48:51]\n\t"
    "v_pk_maximum3_f16 v235, v164, v164, s29 op_sel:[0,0,0] op_sel_hi:[1,1,0] neg_lo:[0,1,0] neg_hi:[0,1,0]\n\t"
    "v_pk_maximum3_f16 v249, v232, v233, v249\n\t"
    "v_mfma_f32_16x16x32_bf16 v[48:51], a[4:7], a[236:239], v[48:51]\n\t"
    "s_add_u32 m0, s92, 124416\n\t"
    "v_pk_maximum3_f16 v249, v234, v235, v249\n\t"
    "v_mfma_f32_16x16x32_bf16 v[48:51], a[8:11], a[240:243], v[48:51]\n\t"
    "buffer_load_dwordx4 v186, s[84:87], s64 offen offset:512 lds\n\t"
    "v_mfma_f32_16x16x32_bf16 v[56:59], a[8:11], a[244:247], v[56:59]\n\t"
    "v_pk_mad_u16 v232, v232, 8, s29 op_sel:[0,0,1] op_sel_hi:[1,0,1]\n\t"
    "v_pk_mad_u16 v233, v233, 8, s29 op_sel:[0,0,1] op_sel_hi:[1,0,1]\n\t"
    "v_mfma_f32_16x16x32_bf16 v[56:59], a[4:7], a[240:243], v[56:59]\n\t"
    "v_pk_mad_u16 v234, v234, 8, s29 op_sel:[0,0,1] op_sel_hi:[1,0,1]\n\t"
    "v_pk_mad_u16 v235, v235, 8, s29 op_sel:[0,0,1] op_sel_hi:[1,0,1]\n\t"
    "v_mfma_f32_16x16x32_bf16 v[56:59], a[0:3], a[236:239], v[56:59]\n\t"
    "s_waitcnt vmcnt(16) lgkmcnt(0)\n\t"
    "s_barrier\n\t"
    "v_mfma_f32_16x16x32_bf16 v[4:7], a[16:19], a[208:211], v[4:7]\n\t"
    "ds_read_b128 a[208:211], v181 offset:18432\n\t"
    "s_add_u32 m0, s92, 128512\n\t"
    "v_mfma_f32_16x16x32_bf16 v[4:7], a[20:23], a[212:215], v[4:7]\n\t"
    "ds_read_b64_tr_b16 a[0:1], v189 offset:10240\n\t"
    "ds_read_b64_tr_b16 a[2:3], v189 offset:10752\n\t"
    "v_mfma_f32_16x16x32_bf16 v[4:7], a[24:27], a[216:219], v[4:7]\n\t"
    "buffer_load_dwordx4 v186, s[84:87], s66 offen offset:512 lds\n\t"
    "v_mfma_f32_16x16x32_bf16 v[12:15], a[24:27], a[220:223], v[12:15]\n\t"
    "v_bfi_b32 v152, s24, v232, v152\n\t"
    "v_bfi_b32 v156, s24, v233, v156\n\t"
    "v_mfma_f32_16x16x32_bf16 v[12:15], a[20:23], a[216:219], v[12:15]\n\t"
    "v_bfi_b32 v160, s24, v234, v160\n\t"
    "v_bfi_b32 v164, s24, v235, v164\n\t"
    "v_mfma_f32_16x16x32_bf16 v[12:15], a[16:19], a[212:215], v[12:15]\n\t"
    "ds_read_b128 a[212:215], v181 offset:18560\n\t"
    "v_pk_maximum3_f16 v232, v153, v153, s29 op_sel:[0,0,0] op_sel_hi:[1,1,0] neg_lo:[0,1,0] neg_hi:[0,1,0]\n\t"
    "v_mfma_f32_16x16x32_bf16 v[20:23], a[16:19], a[216:219], v[20:23]\n\t"
    "ds_read_b128 a[216:219], v181 offset:18688\n\t"
    "v_pk_maximum3_f16 v233, v157, v157, s29 op_sel:[0,0,0] op_sel_hi:[1,1,0] neg_lo:[0,1,0] neg_hi:[0,1,0]\n\t"
    "v_mfma_f32_16x16x32_bf16 v[20:23], a[20:23], a[220:223], v[20:23]\n\t"
    "ds_read_b64_tr_b16 a[4:5], v189 offset:14336\n\t"
    "ds_read_b64_tr_b16 a[6:7], v189 offset:14848\n\t"
    "v_mfma_f32_16x16x32_bf16 v[20:23], a[24:27], a[224:227], v[20:23]\n\t"
    "s_add_u32 m0, s92, 132608\n\t"
    "v_pk_maximum3_f16 v234, v161, v161, s29 op_sel:[0,0,0] op_sel_hi:[1,1,0] neg_lo:[0,1,0] neg_hi:[0,1,0]\n\t"
    "v_mfma_f32_16x16x32_bf16 v[28:31], a[24:27], a[228:231], v[28:31]\n\t"
    "buffer_load_dwordx4 v186, s[84:87], s68 offen offset:512 lds\n\t"
    "v_mfma_f32_16x16x32_bf16 v[28:31], a[20:23], a[224:227], v[28:31]\n\t"
    "v_pk_maximum3_f16 v235, v165, v165, s29 op_sel:[0,0,0] op_sel_hi:[1,1,0] neg_lo:[0,1,0] neg_hi:[0,1,0]\n\t"
    "v_pk_maximum3_f16 v249, v232, v233, v249\n\t"
    "v_mfma_f32_16x16x32_bf16 v[28:31], a[16:19], a[220:223], v[28:31]\n\t"
    "ds_read_b128 a[220:223], v181 offset:18816\n\t"
    "v_pk_maximum3_f16 v249, v234, v235, v249\n\t"
    "v_mfma_f32_16x16x32_bf16 v[36:39], a[16:19], a[224:227], v[36:39]\n\t"
    "ds_read_b128 a[224:227], v181 offset:18944\n\t"
    "v_pk_mad_u16 v232, v232, 8, s29 op_sel:[0,0,1] op_sel_hi:[1,0,1]\n\t"
    "v_mfma_f32_16x16x32_bf16 v[36:39], a[20:23], a[228:231], v[36:39]\n\t"
    "ds_read_b64_tr_b16 a[8:9], v189 offset:18432\n\t"
    "ds_read_b64_tr_b16 a[10:11], v189 offset:18944\n\t"
    "v_mfma_f32_16x16x32_bf16 v[36:39], a[24:27], a[232:235], v[36:39]\n\t"
    "v_xor_b32 v186, 0x8000, v186\n\t"
    "s_add_u32 m0, s92, 137216\n\t"
    "v_mfma_f32_16x16x32_bf16 v[44:47], a[24:27], a[236:239], v[44:47]\n\t"
    "buffer_load_dwordx4 v186, s[84:87], s64 offen offset:0 lds\n\t"
    "v_mfma_f32_16x16x32_bf16 v[44:47], a[20:23], a[232:235], v[44:47]\n\t"
    "v_pk_mad_u16 v233, v233, 8, s29 op_sel:[0,0,1] op_sel_hi:[1,0,1]\n\t"
    "v_pk_mad_u16 v234, v234, 8, s29 op_sel:[0,0,1] op_sel_hi:[1,0,1]\n\t"
    "v_mfma_f32_16x16x32_bf16 v[44:47], a[16:19], a[228:231], v[44:47]\n\t"
    "ds_read_b128 a[228:231], v181 offset:19072\n\t"
    "v_pk_mad_u16 v235, v235, 8, s29 op_sel:[0,0,1] op_sel_hi:[1,0,1]\n\t"
    "v_mfma_f32_16x16x32_bf16 v[52:55], a[16:19], a[232:235], v[52:55]\n\t"
    "ds_read_b128 a[232:235], v181 offset:19200\n\t"
    "v_bfi_b32 v153, s24, v232, v153\n\t"
    "v_mfma_f32_16x16x32_bf16 v[52:55], a[20:23], a[236:239], v[52:55]\n\t"
    "v_bfi_b32 v157, s24, v233, v157\n\t"
    "v_bfi_b32 v161, s24, v234, v161\n\t"
    "v_mfma_f32_16x16x32_bf16 v[52:55], a[24:27], a[240:243], v[52:55]\n\t"
    "v_bfi_b32 v165, s24, v235, v165\n\t"
    "v_pk_maximum3_f16 v232, v154, v154, s29 op_sel:[0,0,0] op_sel_hi:[1,1,0] neg_lo:[0,1,0] neg_hi:[0,1,0]\n\t"
    "v_mfma_f32_16x16x32_bf16 v[60:63], a[24:27], a[244:247], v[60:63]\n\t"
    "ds_read_b128 a[244:247], v182 offset:18560\n\t"
    "v_pk_maximum3_f16 v233, v158, v158, s29 op_sel:[0,0,0] op_sel_hi:[1,1,0] neg_lo:[0,1,0] neg_hi:[0,1,0]\n\t"
    "v_mfma_f32_16x16x32_bf16 v[60:63], a[20:23], a[240:243], v[60:63]\n\t"
    "ds_read_b128 a[240:243], v183 offset:18432\n\t"
    "v_pk_maximum3_f16 v234, v162, v162, s29 op_sel:[0,0,0] op_sel_hi:[1,1,0] neg_lo:[0,1,0] neg_hi:[0,1,0]\n\t"
    "v_mfma_f32_16x16x32_bf16 v[60:63], a[16:19], a[236:239], v[60:63]\n\t"
    "s_waitcnt vmcnt(18) lgkmcnt(4)\n\t"
    "s_barrier\n\t"
    "v_mfma_f32_16x16x32_bf16 v[0:3], a[0:3], a[208:211], v[0:3]\n\t"
    "ds_read_b128 a[236:239], v181 offset:19328\n\t"
    "s_add_u32 m0, s92, 141312\n\t"
    "v_mfma_f32_16x16x32_bf16 v[0:3], a[4:7], a[212:215], v[0:3]\n\t"
    "ds_read_b64_tr_b16 a[16:17], v189 offset:22528\n\t"
    "ds_read_b64_tr_b16 a[18:19], v189 offset:23040\n\t"
    "v_mfma_f32_16x16x32_bf16 v[0:3], a[8:11], a[216:219], v[0:3]\n\t"
    "buffer_load_dwordx4 v186, s[84:87], s66 offen offset:0 lds\n\t"
    "v_mfma_f32_16x16x32_bf16 v[8:11], a[8:11], a[220:223], v[8:11]\n\t"
    "v_pk_maximum3_f16 v235, v166, v166, s29 op_sel:[0,0,0] op_sel_hi:[1,1,0] neg_lo:[0,1,0] neg_hi:[0,1,0]\n\t"
    "v_pk_maximum3_f16 v249, v232, v233, v249\n\t"
    "v_mfma_f32_16x16x32_bf16 v[8:11], a[4:7], a[216:219], v[8:11]\n\t"
    "v_pk_maximum3_f16 v249, v234, v235, v249\n\t"
    "v_pk_mad_u16 v232, v232, 8, s29 op_sel:[0,0,1] op_sel_hi:[1,0,1]\n\t"
    "v_mfma_f32_16x16x32_bf16 v[8:11], a[0:3], a[212:215], v[8:11]\n\t"
    "s_waitcnt lgkmcnt(5)\n\t"
    "v_pk_mad_u16 v233, v233, 8, s29 op_sel:[0,0,1] op_sel_hi:[1,0,1]\n\t"
    "v_mfma_f32_16x16x32_bf16 v[16:19], a[0:3], a[216:219], v[16:19]\n\t"
    "ds_read_b64_tr_b16 a[20:21], v189 offset:26624\n\t"
    "ds_read_b64_tr_b16 a[22:23], v189 offset:27136\n\t"
    "v_mfma_f32_16x16x32_bf16 v[16:19], a[4:7], a[220:223], v[16:19]\n\t"
    "s_add_u32 m0, s92, 145408\n\t"
    "v_pk_mad_u16 v234, v234, 8, s29 op_sel:[0,0,1] op_sel_hi:[1,0,1]\n\t"
    "v_mfma_f32_16x16x32_bf16 v[16:19], a[8:11], a[224:227], v[16:19]\n\t"
    "buffer_load_dwordx4 v186, s[84:87], s68 offen offset:0 lds\n\t"
    "v_mfma_f32_16x16x32_bf16 v[24:27], a[8:11], a[228:231], v[24:27]\n\t"
    "v_pk_mad_u16 v235, v235, 8, s29 op_sel:[0,0,1] op_sel_hi:[1,0,1]\n\t"
    "v_bfi_b32 v154, s24, v232, v154\n\t"
    "v_mfma_f32_16x16x32_bf16 v[24:27], a[4:7], a[224:227], v[24:27]\n\t"
    "v_bfi_b32 v158, s24, v233, v158\n\t"
    "v_bfi_b32 v162, s24, v234, v162\n\t"
    "v_mfma_f32_16x16x32_bf16 v[24:27], a[0:3], a[220:223], v[24:27]\n\t"
    "v_bfi_b32 v166, s24, v235, v166\n\t"
    "v_pk_maximum3_f16 v232, v155, v155, s29 op_sel:[0,0,0] op_sel_hi:[1,1,0] neg_lo:[0,1,0] neg_hi:[0,1,0]\n\t"
    "v_mfma_f32_16x16x32_bf16 v[32:35], a[0:3], a[224:227], v[32:35]\n\t"
    "ds_read_b64_tr_b16 a[24:25], v188 offset:18432\n\t"
    "ds_read_b64_tr_b16 a[26:27], v188 offset:18944\n\t"
    "v_mfma_f32_16x16x32_bf16 v[32:35], a[4:7], a[228:231], v[32:35]\n\t"
    "s_waitcnt lgkmcnt(6)\n\t"
    "v_pk_maximum3_f16 v233, v159, v159, s29 op_sel:[0,0,0] op_sel_hi:[1,1,0] neg_lo:[0,1,0] neg_hi:[0,1,0]\n\t"
    "v_mfma_f32_16x16x32_bf16 v[32:35], a[8:11], a[232:235], v[32:35]\n\t"
    "s_add_u32 m0, s92, 148992\n\t"
    "v_pk_maximum3_f16 v234, v163, v163, s29 op_sel:[0,0,0] op_sel_hi:[1,1,0] neg_lo:[0,1,0] neg_hi:[0,1,0]\n\t"
    "v_mfma_f32_16x16x32_bf16 v[40:43], a[8:11], a[236:239], v[40:43]\n\t"
    "buffer_load_dwordx4 v186, s[84:87], s64 offen offset:512 lds\n\t"
    "v_mfma_f32_16x16x32_bf16 v[40:43], a[4:7], a[232:235], v[40:43]\n\t"
    "v_pk_maximum3_f16 v235, v167, v167, s29 op_sel:[0,0,0] op_sel_hi:[1,1,0] neg_lo:[0,1,0] neg_hi:[0,1,0]\n\t"
    "v_pk_maximum3_f16 v249, v232, v233, v249\n\t"
    "v_mfma_f32_16x16x32_bf16 v[40:43], a[0:3], a[228:231], v[40:43]\n\t"
    "v_pk_maximum3_f16 v249, v234, v235, v249\n\t"
    "v_pk_mad_u16 v232, v232, 8, s29 op_sel:[0,0,1] op_sel_hi:[1,0,1]\n\t"
    "v_mfma_f32_16x16x32_bf16 v[48:51], a[0:3], a[232:235], v[48:51]\n\t"
    "v_pk_mad_u16 v233, v233, 8, s29 op_sel:[0,0,1] op_sel_hi:[1,0,1]\n\t"
    "v_pk_mad_u16 v234, v234, 8, s29 op_sel:[0,0,1] op_sel_hi:[1,0,1]\n\t"
    "v_mfma_f32_16x16x32_bf16 v[48:51], a[4:7], a[236:239], v[48:51]\n\t"
    "v_pk_mad_u16 v235, v235, 8, s29 op_sel:[0,0,1] op_sel_hi:[1,0,1]\n\t"
    "v_bfi_b32 v155, s24, v232, v155\n\t"
    "v_mfma_f32_16x16x32_bf16 v[48:51], a[8:11], a[240:243], v[48:51]\n\t"
    "v_bfi_b32 v159, s24, v233, v159\n\t"
    "v_bfi_b32 v163, s24, v234, v163\n\t"
    "v_mfma_f32_16x16x32_bf16 v[56:59], a[8:11], a[244:247], v[56:59]\n\t"
    "v_bfi_b32 v167, s24, v235, v167\n\t"
    "s_waitcnt vmcnt(30)\n\t"
    "v_mfma_f32_16x16x32_bf16 v[56:59], a[4:7], a[240:243], v[56:59]\n\t"
    "v_pk_maximum3_f16 v232, v168, v168, s29 op_sel:[0,0,0] op_sel_hi:[1,1,0] neg_lo:[0,1,0] neg_hi:[0,1,0]\n\t"
    "v_pk_maximum3_f16 v233, v169, v169, s29 op_sel:[0,0,0] op_sel_hi:[1,1,0] neg_lo:[0,1,0] neg_hi:[0,1,0]\n\t"
    "v_mfma_f32_16x16x32_bf16 v[56:59], a[0:3], a[236:239], v[56:59]\n\t"
    "s_waitcnt vmcnt(18) lgkmcnt(0)\n\t"
    "s_barrier\n\t"
    "v_mfma_f32_16x16x32_bf16 v[4:7], a[16:19], a[208:211], v[4:7]\n\t"
    "ds_read_b128 a[208:211], v181 offset:18496\n\t"
    "s_add_u32 m0, s92, 153088\n\t"
    "v_mfma_f32_16x16x32_bf16 v[4:7], a[20:23], a[212:215], v[4:7]\n\t"
    "ds_read_b64_tr_b16 a[0:1], v188 offset:22528\n\t"
    "ds_read_b64_tr_b16 a[2:3], v188 offset:23040\n\t"
    "v_mfma_f32_16x16x32_bf16 v[4:7], a[24:27], a[216:219], v[4:7]\n\t"
    "buffer_load_dwordx4 v186, s[84:87], s66 offen offset:512 lds\n\t"
    "v_mfma_f32_16x16x32_bf16 v[12:15], a[24:27], a[220:223], v[12:15]\n\t"
    "v_pk_maximum3_f16 v234, v170, v170, s29 op_sel:[0,0,0] op_sel_hi:[1,1,0] neg_lo:[0,1,0] neg_hi:[0,1,0]\n\t"
    "v_pk_maximum3_f16 v235, v171, v171, s29 op_sel:[0,0,0] op_sel_hi:[1,1,0] neg_lo:[0,1,0] neg_hi:[0,1,0]\n\t"
    "v_mfma_f32_16x16x32_bf16 v[12:15], a[20:23], a[216:219], v[12:15]\n\t"
    "v_pk_maximum3_f16 v249, v232, v233, v249\n\t"
    "v_pk_maximum3_f16 v249, v234, v235, v249\n\t"
    "v_mfma_f32_16x16x32_bf16 v[12:15], a[16:19], a[212:215], v[12:15]\n\t"
    "ds_read_b128 a[212:215], v181 offset:18624\n\t"
    "v_pk_mad_u16 v232, v232, 8, s29 op_sel:[0,0,1] op_sel_hi:[1,0,1]\n\t"
    "v_mfma_f32_16x16x32_bf16 v[20:23], a[16:19], a[216:219], v[20:23]\n\t"
    "ds_read_b128 a[216:219], v181 offset:18752\n\t"
    "v_pk_mad_u16 v233, v233, 8, s29 op_sel:[0,0,1] op_sel_hi:[1,0,1]\n\t"
    "v_mfma_f32_16x16x32_bf16 v[20:23], a[20:23], a[220:223], v[20:23]\n\t"
    "ds_read_b64_tr_b16 a[4:5], v188 offset:26624\n\t"
    "ds_read_b64_tr_b16 a[6:7], v188 offset:27136\n\t"
    "v_mfma_f32_16x16x32_bf16 v[20:23], a[24:27], a[224:227], v[20:23]\n\t"
    "v_pk_mad_u16 v234, v234, 8, s29 op_sel:[0,0,1] op_sel_hi:[1,0,1]\n\t"
    "v_pk_mad_u16 v235, v235, 8, s29 op_sel:[0,0,1] op_sel_hi:[1,0,1]\n\t"
    "v_mfma_f32_16x16x32_bf16 v[28:31], a[24:27], a[228:231], v[28:31]\n\t"
    "v_bfi_b32 v168, s24, v232, v168\n\t"
    "v_bfi_b32 v169, s24, v233, v169\n\t"
    "v_mfma_f32_16x16x32_bf16 v[28:31], a[20:23], a[224:227], v[28:31]\n\t"
    "v_bfi_b32 v170, s24, v234, v170\n\t"
    "v_bfi_b32 v171, s24, v235, v171\n\t"
    "v_mfma_f32_16x16x32_bf16 v[28:31], a[16:19], a[220:223], v[28:31]\n\t"
    "ds_read_b128 a[220:223], v181 offset:18880\n\t"
    "s_add_u32 m0, s92, 157184\n\t"
    "v_mfma_f32_16x16x32_bf16 v[36:39], a[16:19], a[224:227], v[36:39]\n\t"
    "ds_read_b128 a[224:227], v181 offset:19008\n\t"
    "s_mov_b32 s27, 8448\n\t"
    "v_mfma_f32_16x16x32_bf16 v[36:39], a[20:23], a[228:231], v[36:39]\n\t"
    "ds_read_b64_tr_b16 a[8:9], v188 offset:30720\n\t"
    "ds_read_b64_tr_b16 a[10:11], v188 offset:31232\n\t"
    "v_mfma_f32_16x16x32_bf16 v[36:39], a[24:27], a[232:235], v[36:39]\n\t"
    "buffer_load_dwordx4 v186, s[84:87], s68 offen offset:512 lds\n\t"
    "v_mfma_f32_16x16x32_bf16 v[44:47], a[24:27], a[236:239], v[44:47]\n\t"
    "v_mad_i32_i24 v247, v248, s27, v246\n\t"
    "s_add_u32 s30, s21, 44800\n\t"
    "v_mfma_f32_16x16x32_bf16 v[44:47], a[20:23], a[232:235], v[44:47]\n\t"
    "s_mov_b32 m0, s21\n\t"
    "v_pk_add_f16 v236, v136, v144\n\t"
    "v_mfma_f32_16x16x32_bf16 v[44:47], a[16:19], a[228:231], v[44:47]\n\t"
    "ds_read_b128 a[228:231], v181 offset:19136\n\t"
    "v_pk_add_f16 v237, v140, v148\n\t"
    "v_mfma_f32_16x16x32_bf16 v[52:55], a[16:19], a[232:235], v[52:55]\n\t"
    "ds_read_b128 a[232:235], v181 offset:19264\n\t"
    "v_pk_fma_f16 v240, v148, 0.5, v144 op_sel_hi:[1,0,1]\n\t"
    "v_mfma_f32_16x16x32_bf16 v[52:55], a[20:23], a[236:239], v[52:55]\n\t"
    "v_pk_add_f16 v238, v236, v237\n\t"
    "v_pk_add_f16 v239, v236, v237 neg_lo:[0,1] neg_hi:[0,1]\n\t"
    "v_mfma_f32_16x16x32_bf16 v[52:55], a[24:27], a[240:243], v[52:55]\n\t"
    "ds_write_addtid_b32 v238 offset:0\n\t"
    "v_mfma_f32_16x16x32_bf16 v[60:63], a[24:27], a[244:247], v[60:63]\n\t"
    "ds_read_b128 a[244:247], v182 offset:18624\n\t"
    "v_pk_fma_f16 v236, v144, 4.0, v136 op_sel_hi:[1,0,1]\n\t"
    "v_mfma_f32_16x16x32_bf16 v[60:63], a[20:23], a[240:243], v[60:63]\n\t"
    "ds_read_b128 a[240:243], v183 offset:18496\n\t"
    "ds_write_addtid_b32 v239 offset:8960\n\t"
    "v_mfma_f32_16x16x32_bf16 v[60:63], a[16:19], a[236:239], v[60:63]\n\t"
    "s_waitcnt vmcnt(12) lgkmcnt(6)\n\t"
    "s_barrier\n\t"
    "v_mfma_f32_16x16x32_bf16 v[0:3], a[0:3], a[208:211], v[0:3]\n\t"
    "ds_read_b128 a[236:239], v181 offset:19392\n\t"
    "v_pk_fma_f16 v237, v148, 4.0, v140 op_sel_hi:[1,0,1]\n\t"
    "v_mfma_f32_16x16x32_bf16 v[0:3], a[4:7], a[212:215], v[0:3]\n\t"
    "ds_read_b64_tr_b16 a[16:17], v188 offset:34816\n\t"
    "ds_read_b64_tr_b16 a[18:19], v188 offset:35328\n\t"
    "v_mfma_f32_16x16x32_bf16 v[0:3], a[8:11], a[216:219], v[0:3]\n\t"
    "buffer_load_dword v250, v194, s[88:91], 0 offen offset:0 sc1\n\t"
    "v_pk_fma_f16 v240, v240, 0.5, v140 op_sel_hi:[1,0,1]\n\t"
    "v_mfma_f32_16x16x32_bf16 v[8:11], a[8:11], a[220:223], v[8:11]\n\t"
    "v_pk_fma_f16 v238, v237, 2.0, v236 op_sel_hi:[1,0,1]\n\t"
    "v_pk_fma_f16 v239, v237, -2.0, v236 op_sel_hi:[1,0,1]\n\t"
    "v_mfma_f32_16x16x32_bf16 v[8:11], a[4:7], a[216:219], v[8:11]\n\t"
    "ds_write_addtid_b32 v238 offset:17920\n\t"
    "v_pk_fma_f16 v240, v240, 0.5, v136 op_sel_hi:[1,0,1]\n\t"
    "v_mfma_f32_16x16x32_bf16 v[8:11], a[0:3], a[212:215], v[8:11]\n\t"
    "s_waitcnt lgkmcnt(8)\n\t"
    "ds_write_addtid_b32 v239 offset:26880\n\t"
    "v_mfma_f32_16x16x32_bf16 v[16:19], a[0:3], a[216:219], v[16:19]\n\t"
    "ds_read_b64_tr_b16 a[20:21], v188 offset:38912\n\t"
    "ds_read_b64_tr_b16 a[22:23], v188 offset:39424\n\t"
    "v_mfma_f32_16x16x32_bf16 v[16:19], a[4:7], a[220:223], v[16:19]\n\t"
    "ds_write_addtid_b32 v240 offset:35840\n\t"
    "v_pk_add_f16 v236, v137, v145\n\t"
    "v_mfma_f32_16x16x32_bf16 v[16:19], a[8:11], a[224:227], v[16:19]\n\t"
    "v_pk_add_f16 v237, v141, v149\n\t"
    "v_pk_fma_f16 v240, v149, 0.5, v145 op_sel_hi:[1,0,1]\n\t"
    "v_mfma_f32_16x16x32_bf16 v[24:27], a[8:11], a[228:231], v[24:27]\n\t"
    "v_pk_add_f16 v238, v236, v237\n\t"
    "v_pk_add_f16 v239, v236, v237 neg_lo:[0,1] neg_hi:[0,1]\n\t"
    "v_mfma_f32_16x16x32_bf16 v[24:27], a[4:7], a[224:227], v[24:27]\n\t"
    "ds_write_addtid_b32 v238 offset:256\n\t"
    "v_pk_fma_f16 v236, v145, 4.0, v137 op_sel_hi:[1,0,1]\n\t"
    "v_mfma_f32_16x16x32_bf16 v[24:27], a[0:3], a[220:223], v[24:27]\n\t"
    "ds_write_addtid_b32 v239 offset:9216\n\t"
    "v_pk_fma_f16 v237, v149, 4.0, v141 op_sel_hi:[1,0,1]\n\t"
    "v_mfma_f32_16x16x32_bf16 v[32:35], a[0:3], a[224:227], v[32:35]\n\t"
    "ds_read_b64_tr_b16 a[24:25], v188 offset:43008\n\t"
    "ds_read_b64_tr_b16 a[26:27], v188 offset:43520\n\t"
    "v_mfma_f32_16x16x32_bf16 v[32:35], a[4:7], a[228:231], v[32:35]\n\t"
    "s_waitcnt lgkmcnt(11)\n\t"
    "v_pk_fma_f16 v240, v240, 0.5, v141 op_sel_hi:[1,0,1]\n\t"
    "v_mfma_f32_16x16x32_bf16 v[32:35], a[8:11], a[232:235], v[32:35]\n\t"
    "v_pk_fma_f16 v238, v237, 2.0, v236 op_sel_hi:[1,0,1]\n\t"
    "v_pk_fma_f16 v239, v237, -2.0, v236 op_sel_hi:[1,0,1]\n\t"
    "v_mfma_f32_16x16x32_bf16 v[40:43], a[8:11], a[236:239], v[40:43]\n\t"
    "ds_write_addtid_b32 v238 offset:18176\n\t"
    "v_pk_fma_f16 v240, v240, 0.5, v137 op_sel_hi:[1,0,1]\n\t"
    "v_mfma_f32_16x16x32_bf16 v[40:43], a[4:7], a[232:235], v[40:43]\n\t"
    "ds_write_addtid_b32 v239 offset:27136\n\t"
    "v_mfma_f32_16x16x32_bf16 v[40:43], a[0:3], a[228:231], v[40:43]\n\t"
    "ds_write_addtid_b32 v240 offset:36096\n\t"
    "v_pk_add_f16 v236, v138, v146\n\t"
    "v_mfma_f32_16x16x32_bf16 v[48:51], a[0:3], a[232:235], v[48:51]\n\t"
    "v_pk_add_f16 v237, v142, v150\n\t"
    "v_pk_fma_f16 v240, v150, 0.5, v146 op_sel_hi:[1,0,1]\n\t"
    "v_mfma_f32_16x16x32_bf16 v[48:51], a[4:7], a[236:239], v[48:51]\n\t"
    "v_pk_add_f16 v238, v236, v237\n\t"
    "v_pk_add_f16 v239, v236, v237 neg_lo:[0,1] neg_hi:[0,1]\n\t"
    "v_mfma_f32_16x16x32_bf16 v[48:51], a[8:11], a[240:243], v[48:51]\n\t"
    "ds_write_addtid_b32 v238 offset:512\n\t"
    "v_mfma_f32_16x16x32_bf16 v[56:59], a[8:11], a[244:247], v[56:59]\n\t"
    "v_pk_fma_f16 v236, v146, 4.0, v138 op_sel_hi:[1,0,1]\n\t"
    "v_mfma_f32_16x16x32_bf16 v[56:59], a[4:7], a[240:243], v[56:59]\n\t"
    "ds_write_addtid_b32 v239 offset:9472\n\t"
    "v_pk_fma_f16 v237, v150, 4.0, v142 op_sel_hi:[1,0,1]\n\t"
    "v_mfma_f32_16x16x32_bf16 v[56:59], a[0:3], a[236:239], v[56:59]\n\t"
    "s_waitcnt vmcnt(10) lgkmcnt(5)\n\t"
    "s_barrier\n\t"
    "v_mfma_f32_16x16x32_bf16 v[4:7], a[16:19], a[208:211], v[4:7]\n\t"
    "ds_read_b128 a[208:211], v181 offset:36864\n\t"
    "v_pk_fma_f16 v240, v240, 0.5, v142 op_sel_hi:[1,0,1]\n\t"
    "v_mfma_f32_16x16x32_bf16 v[4:7], a[20:23], a[212:215], v[4:7]\n\t"
    "ds_read_b64_tr_b16 a[0:1], v188 offset:47104\n\t"
    "ds_read_b64_tr_b16 a[2:3], v188 offset:47616\n\t"
    "v_mfma_f32_16x16x32_bf16 v[4:7], a[24:27], a[216:219], v[4:7]\n\t"
    "v_pk_fma_f16 v238, v237, 2.0, v236 op_sel_hi:[1,0,1]\n\t"
    "v_pk_fma_f16 v239, v237, -2.0, v236 op_sel_hi:[1,0,1]\n\t"
    "v_mfma_f32_16x16x32_bf16 v[12:15], a[24:27], a[220:223], v[12:15]\n\t"
    "ds_write_addtid_b32 v238 offset:18432\n\t"
    "v_pk_fma_f16 v240, v240, 0.5, v138 op_sel_hi:[1,0,1]\n\t"
    "v_mfma_f32_16x16x32_bf16 v[12:15], a[20:23], a[216:219], v[12:15]\n\t"
    "ds_write_addtid_b32 v239 offset:27392\n\t"
    "v_mfma_f32_16x16x32_bf16 v[12:15], a[16:19], a[212:215], v[12:15]\n\t"
    "ds_read_b128 a[212:215], v181 offset:36992\n\t"
    "ds_write_addtid_b32 v240 offset:36352\n\t"
    "v_mfma_f32_16x16x32_bf16 v[20:23], a[16:19], a[216:219], v[20:23]\n\t"
    "ds_read_b128 a[216:219], v181 offset:37120\n\t"
    "v_pk_add_f16 v236, v139, v147\n\t"
    "v_mfma_f32_16x16x32_bf16 v[20:23], a[20:23], a[220:223], v[20:23]\n\t"
    "ds_read_b64_tr_b16 a[4:5], v188 offset:51200\n\t"
    "ds_read_b64_tr_b16 a[6:7], v188 offset:51712\n\t"
    "v_mfma_f32_16x16x32_bf16 v[20:23], a[24:27], a[224:227], v[20:23]\n\t"
    "v_pk_add_f16 v237, v143, v151\n\t"
    "v_pk_fma_f16 v240, v151, 0.5, v147 op_sel_hi:[1,0,1]\n\t"
    "v_mfma_f32_16x16x32_bf16 v[28:31], a[24:27], a[228:231], v[28:31]\n\t"
    "v_pk_add_f16 v238, v236, v237\n\t"
    "v_pk_add_f16 v239, v236, v237 neg_lo:[0,1] neg_hi:[0,1]\n\t"
    "v_mfma_f32_16x16x32_bf16 v[28:31], a[20:23], a[224:227], v[28:31]\n\t"
    "ds_write_addtid_b32 v238 offset:768\n\t"
    "v_pk_fma_f16 v236, v147, 4.0, v139 op_sel_hi:[1,0,1]\n\t"
    "v_mfma_f32_16x16x32_bf16 v[28:31], a[16:19], a[220:223], v[28:31]\n\t"
    "ds_read_b128 a[220:223], v181 offset:37248\n\t"
    "ds_write_addtid_b32 v239 offset:9728\n\t"
    "v_mfma_f32_16x16x32_bf16 v[36:39], a[16:19], a[224:227], v[36:39]\n\t"
    "ds_read_b128 a[224:227], v181 offset:37376\n\t"
    "v_pk_fma_f16 v237, v151, 4.0, v143 op_sel_hi:[1,0,1]\n\t"
    "v_mfma_f32_16x16x32_bf16 v[36:39], a[20:23], a[228:231], v[36:39]\n\t"
    "ds_read_b64_tr_b16 a[8:9], v188 offset:55296\n\t"
    "ds_read_b64_tr_b16 a[10:11], v188 offset:55808\n\t"
    "v_mfma_f32_16x16x32_bf16 v[36:39], a[24:27], a[232:235], v[36:39]\n\t"
    "v_pk_fma_f16 v240, v240, 0.5, v143 op_sel_hi:[1,0,1]\n\t"
    "v_pk_fma_f16 v238, v237, 2.0, v236 op_sel_hi:[1,0,1]\n\t"
    "v_mfma_f32_16x16x32_bf16 v[44:47], a[24:27], a[236:239], v[44:47]\n\t"
    "v_pk_fma_f16 v239, v237, -2.0, v236 op_sel_hi:[1,0,1]\n\t"
    "v_mfma_f32_16x16x32_bf16 v[44:47], a[20:23], a[232:235], v[44:47]\n\t"
    "ds_write_addtid_b32 v238 offset:18688\n\t"
    "v_pk_fma_f16 v240, v240, 0.5, v139 op_sel_hi:[1,0,1]\n\t"
    "v_mfma_f32_16x16x32_bf16 v[44:47], a[16:19], a[228:231], v[44:47]\n\t"
    "ds_read_b128 a[228:231], v181 offset:37504\n\t"
    "ds_write_addtid_b32 v239 offset:27648\n\t"
    "v_mfma_f32_16x16x32_bf16 v[52:55], a[16:19], a[232:235], v[52:55]\n\t"
    "ds_read_b128 a[232:235], v181 offset:37632\n\t"
    "ds_write_addtid_b32 v240 offset:36608\n\t"
    "v_mfma_f32_16x16x32_bf16 v[52:55], a[20:23], a[236:239], v[52:55]\n\t"
    "s_add_u32 m0, s21, 1056\n\t"
    "v_pk_add_f16 v236, v152, v160\n\t"
    "v_mfma_f32_16x16x32_bf16 v[52:55], a[24:27], a[240:243], v[52:55]\n\t"
    "v_pk_add_f16 v237, v156, v164\n\t"
    "v_mfma_f32_16x16x32_bf16 v[60:63], a[24:27], a[244:247], v[60:63]\n\t"
    "ds_read_b128 a[244:247], v182 offset:36992\n\t"
    "v_pk_fma_f16 v240, v164, 0.5, v160 op_sel_hi:[1,0,1]\n\t"
    "v_mfma_f32_16x16x32_bf16 v[60:63], a[20:23], a[240:243], v[60:63]\n\t"
    "ds_read_b128 a[240:243], v183 offset:36864\n\t"
    "v_pk_add_f16 v238, v236, v237\n\t"
    "v_mfma_f32_16x16x32_bf16 v[60:63], a[16:19], a[236:239], v[60:63]\n\t"
    "s_waitcnt vmcnt(7) lgkmcnt(7)\n\t"
    "s_barrier\n\t"
    "v_mfma_f32_16x16x32_bf16 v[0:3], a[0:3], a[208:211], v[0:3]\n\t"
    "ds_read_b128 a[236:239], v181 offset:37760\n\t"
    "v_pk_add_f16 v239, v236, v237 neg_lo:[0,1] neg_hi:[0,1]\n\t"
    "v_mfma_f32_16x16x32_bf16 v[0:3], a[4:7], a[212:215], v[0:3]\n\t"
    "ds_read_b64_tr_b16 a[16:17], v188 offset:59392\n\t"
    "ds_read_b64_tr_b16 a[18:19], v188 offset:59904\n\t"
    "v_mfma_f32_16x16x32_bf16 v[0:3], a[8:11], a[216:219], v[0:3]\n\t"
    "ds_write_addtid_b32 v238 offset:0\n\t"
    "v_pk_fma_f16 v236, v160, 4.0, v152 op_sel_hi:[1,0,1]\n\t"
    "v_mfma_f32_16x16x32_bf16 v[8:11], a[8:11], a[220:223], v[8:11]\n\t"
    "ds_write_addtid_b32 v239 offset:8960\n\t"
    "v_pk_fma_f16 v237, v164, 4.0, v156 op_sel_hi:[1,0,1]\n\t"
    "v_mfma_f32_16x16x32_bf16 v[8:11], a[4:7], a[216:219], v[8:11]\n\t"
    "v_pk_fma_f16 v240, v240, 0.5, v156 op_sel_hi:[1,0,1]\n\t"
    "v_pk_fma_f16 v238, v237, 2.0, v236 op_sel_hi:[1,0,1]\n\t"
    "v_mfma_f32_16x16x32_bf16 v[8:11], a[0:3], a[212:215], v[8:11]\n\t"
    "s_waitcnt lgkmcnt(8)\n\t"
    "v_pk_fma_f16 v239, v237, -2.0, v236 op_sel_hi:[1,0,1]\n\t"
    "v_mfma_f32_16x16x32_bf16 v[16:19], a[0:3], a[216:219], v[16:19]\n\t"
    "ds_read_b64_tr_b16 a[20:21], v188 offset:63488\n\t"
    "ds_read_b64_tr_b16 a[22:23], v188 offset:64000\n\t"
    "v_mfma_f32_16x16x32_bf16 v[16:19], a[4:7], a[220:223], v[16:19]\n\t"
    "ds_write_addtid_b32 v238 offset:17920\n\t"
    "v_pk_fma_f16 v240, v240, 0.5, v152 op_sel_hi:[1,0,1]\n\t"
    "v_mfma_f32_16x16x32_bf16 v[16:19], a[8:11], a[224:227], v[16:19]\n\t"
    "buffer_load_dword v199, v194, s[88:91], 0 offen offset:0 sc1\n\t"
    "ds_write_addtid_b32 v239 offset:26880\n\t"
    "v_mfma_f32_16x16x32_bf16 v[24:27], a[8:11], a[228:231], v[24:27]\n\t"
    "ds_write_addtid_b32 v240 offset:35840\n\t"
    "v_pk_add_f16 v236, v153, v161\n\t"
    "v_mfma_f32_16x16x32_bf16 v[24:27], a[4:7], a[224:227], v[24:27]\n\t"
    "v_pk_add_f16 v237, v157, v165\n\t"
    "v_pk_fma_f16 v240, v165, 0.5, v161 op_sel_hi:[1,0,1]\n\t"
    "v_mfma_f32_16x16x32_bf16 v[24:27], a[0:3], a[220:223], v[24:27]\n\t"
    "v_pk_add_f16 v238, v236, v237\n\t"
    "v_pk_add_f16 v239, v236, v237 neg_lo:[0,1] neg_hi:[0,1]\n\t"
    "v_mfma_f32_16x16x32_bf16 v[32:35], a[0:3], a[224:227], v[32:35]\n\t"
    "ds_read_b64_tr_b16 a[24:25], v189 offset:2048\n\t"
    "ds_read_b64_tr_b16 a[26:27], v189 offset:2560\n\t"
    "v_mfma_f32_16x16x32_bf16 v[32:35], a[4:7], a[228:231], v[32:35]\n\t"
    "s_waitcnt lgkmcnt(11)\n\t"
    "ds_write_addtid_b32 v238 offset:256\n\t"
    "v_mfma_f32_16x16x32_bf16 v[32:35], a[8:11], a[232:235], v[32:35]\n\t"
    "v_pk_fma_f16 v236, v161, 4.0, v153 op_sel_hi:[1,0,1]\n\t"
    "v_mfma_f32_16x16x32_bf16 v[40:43], a[8:11], a[236:239], v[40:43]\n\t"
    "ds_write_addtid_b32 v239 offset:9216\n\t"
    "v_pk_fma_f16 v237, v165, 4.0, v157 op_sel_hi:[1,0,1]\n\t"
    "v_mfma_f32_16x16x32_bf16 v[40:43], a[4:7], a[232:235], v[40:43]\n\t"
    "v_pk_fma_f16 v240, v240, 0.5, v157 op_sel_hi:[1,0,1]\n\t"
    "v_pk_fma_f16 v238, v237, 2.0, v236 op_sel_hi:[1,0,1]\n\t"
    "v_mfma_f32_16x16x32_bf16 v[40:43], a[0:3], a[228:231], v[40:43]\n\t"
    "v_pk_fma_f16 v239, v237, -2.0, v236 op_sel_hi:[1,0,1]\n\t"
    "v_mfma_f32_16x16x32_bf16 v[48:51], a[0:3], a[232:235], v[48:51]\n\t"
    "ds_write_addtid_b32 v238 offset:18176\n\t"
    "v_mfma_f32_16x16x32_bf16 v[48:51], a[4:7], a[236:239], v[48:51]\n\t"
    "v_pk_fma_f16 v240, v240, 0.5, v153 op_sel_hi:[1,0,1]\n\t"
    "v_mfma_f32_16x16x32_bf16 v[48:51], a[8:11], a[240:243], v[48:51]\n\t"
    "ds_write_addtid_b32 v239 offset:27136\n\t"
    "v_mfma_f32_16x16x32_bf16 v[56:59], a[8:11], a[244:247], v[56:59]\n\t"
    "ds_write_addtid_b32 v240 offset:36096\n\t"
    "v_pk_add_f16 v236, v154, v162\n\t"
    "v_mfma_f32_16x16x32_bf16 v[56:59], a[4:7], a[240:243], v[56:59]\n\t"
    "v_pk_add_f16 v237, v158, v166\n\t"
    "v_mfma_f32_16x16x32_bf16 v[56:59], a[0:3], a[236:239], v[56:59]\n\t"
    "s_waitcnt vmcnt(5) lgkmcnt(5)\n\t"
    "s_barrier\n\t"
    "v_mfma_f32_16x16x32_bf16 v[4:7], a[16:19], a[208:211], v[4:7]\n\t"
    "ds_read_b128 a[208:211], v181 offset:36928\n\t"
    "v_pk_fma_f16 v240, v166, 0.5, v162 op_sel_hi:[1,0,1]\n\t"
    "v_mfma_f32_16x16x32_bf16 v[4:7], a[20:23], a[212:215], v[4:7]\n\t"
    "ds_read_b64_tr_b16 a[0:1], v189 offset:6144\n\t"
    "ds_read_b64_tr_b16 a[2:3], v189 offset:6656\n\t"
    "v_mfma_f32_16x16x32_bf16 v[4:7], a[24:27], a[216:219], v[4:7]\n\t"
    "v_pk_add_f16 v238, v236, v237\n\t"
    "v_pk_add_f16 v239, v236, v237 neg_lo:[0,1] neg_hi:[0,1]\n\t"
    "v_mfma_f32_16x16x32_bf16 v[12:15], a[24:27], a[220:223], v[12:15]\n\t"
    "ds_write_addtid_b32 v238 offset:512\n\t"
    "v_pk_fma_f16 v236, v162, 4.0, v154 op_sel_hi:[1,0,1]\n\t"
    "v_mfma_f32_16x16x32_bf16 v[12:15], a[20:23], a[216:219], v[12:15]\n\t"
    "ds_write_addtid_b32 v239 offset:9472\n\t"
    "v_pk_fma_f16 v237, v166, 4.0, v158 op_sel_hi:[1,0,1]\n\t"
    "v_mfma_f32_16x16x32_bf16 v[12:15], a[16:19], a[212:215], v[12:15]\n\t"
    "ds_read_b128 a[212:215], v181 offset:37056\n\t"
    "v_pk_fma_f16 v240, v240, 0.5, v158 op_sel_hi:[1,0,1]\n\t"
    "v_mfma_f32_16x16x32_bf16 v[20:23], a[16:19], a[216:219], v[20:23]\n\t"
    "ds_read_b128 a[216:219], v181 offset:37184\n\t"
    "v_pk_fma_f16 v238, v237, 2.0, v236 op_sel_hi:[1,0,1]\n\t"
    "v_mfma_f32_16x16x32_bf16 v[20:23], a[20:23], a[220:223], v[20:23]\n\t"
    "ds_read_b64_tr_b16 a[4:5], v189 offset:10240\n\t"
    "ds_read_b64_tr_b16 a[6:7], v189 offset:10752\n\t"
    "v_mfma_f32_16x16x32_bf16 v[20:23], a[24:27], a[224:227], v[20:23]\n\t"
    "v_pk_fma_f16 v239, v237, -2.0, v236 op_sel_hi:[1,0,1]\n\t"
    "v_mfma_f32_16x16x32_bf16 v[28:31], a[24:27], a[228:231], v[28:31]\n\t"
    "ds_write_addtid_b32 v238 offset:18432\n\t"
    "v_pk_fma_f16 v240, v240, 0.5, v154 op_sel_hi:[1,0,1]\n\t"
    "v_mfma_f32_16x16x32_bf16 v[28:31], a[20:23], a[224:227], v[28:31]\n\t"
    "ds_write_addtid_b32 v239 offset:27392\n\t"
    "v_mfma_f32_16x16x32_bf16 v[28:31], a[16:19], a[220:223], v[28:31]\n\t"
    "ds_read_b128 a[220:223], v181 offset:37312\n\t"
    "ds_write_addtid_b32 v240 offset:36352\n\t"
    "v_mfma_f32_16x16x32_bf16 v[36:39], a[16:19], a[224:227], v[36:39]\n\t"
    "ds_read_b128 a[224:227], v181 offset:37440\n\t"
    "v_pk_add_f16 v236, v155, v163\n\t"
    "v_mfma_f32_16x16x32_bf16 v[36:39], a[20:23], a[228:231], v[36:39]\n\t"
    "ds_read_b64_tr_b16 a[8:9], v189 offset:14336\n\t"
    "ds_read_b64_tr_b16 a[10:11], v189 offset:14848\n\t"
    "v_mfma_f32_16x16x32_bf16 v[36:39], a[24:27], a[232:235], v[36:39]\n\t"
    "s_waitcnt vmcnt(1)\n\t"
    "v_pk_add_f16 v237, v159, v167\n\t"
    "v_mfma_f32_16x16x32_bf16 v[44:47], a[24:27], a[236:239], v[44:47]\n\t"
    "v_xor_b32 v197, s31, v250\n\t"
    "v_pk_fma_f16 v240, v167, 0.5, v163 op_sel_hi:[1,0,1]\n\t"
    "v_mfma_f32_16x16x32_bf16 v[44:47], a[20:23], a[232:235], v[44:47]\n\t"
    "v_and_b32_e64 v197, s94, v197\n\t"
    "v_pk_add_f16 v238, v236, v237\n\t"
    "v_mfma_f32_16x16x32_bf16 v[44:47], a[16:19], a[228:231], v[44:47]\n\t"
    "ds_read_b128 a[228:231], v181 offset:37568\n\t"
    "v_pk_add_f16 v239, v236, v237 neg_lo:[0,1] neg_hi:[0,1]\n\t"
    "v_mfma_f32_16x16x32_bf16 v[52:55], a[16:19], a[232:235], v[52:55]\n\t"
    "ds_read_b128 a[232:235], v181 offset:37696\n\t"
    "ds_write_addtid_b32 v238 offset:768\n\t"
    "v_mfma_f32_16x16x32_bf16 v[52:55], a[20:23], a[236:239], v[52:55]\n\t"
    "v_cmp_ne_u32 vcc, 0, v197\n\ts_cbranch_vccnz L_slow_%=\n\tL_back_%=:\n\t"
    "v_mfma_f32_16x16x32_bf16 v[52:55], a[24:27], a[240:243], v[52:55]\n\t"
    "buffer_load_dword v197, v193, s[88:91], 0 offen offset:0 sc1\n\t"
    "v_pk_fma_f16 v236, v163, 4.0, v155 op_sel_hi:[1,0,1]\n\t"
    "v_mfma_f32_16x16x32_bf16 v[60:63], a[24:27], a[244:247], v[60:63]\n\t"
    "ds_read_b128 a[244:247], v182 offset:37056\n\t"
    "v_accvgpr_write_b32 a252, v250\n\t"
    "v_mfma_f32_16x16x32_bf16 v[60:63], a[20:23], a[240:243], v[60:63]\n\t"
    "ds_read_b128 a[240:243], v183 offset:36928\n\t"
    "ds_write_addtid_b32 v239 offset:9728\n\t"
    "v_mfma_f32_16x16x32_bf16 v[60:63], a[16:19], a[236:239], v[60:63]\n\t"
    "s_waitcnt vmcnt(3) lgkmcnt(6)\n\t"
    "s_barrier\n\t"
    "v_mfma_f32_16x16x32_bf16 v[0:3], a[0:3], a[208:211], v[0:3]\n\t"
    "ds_read_b128 a[236:239], v181 offset:37824\n\t"
    "v_pk_fma_f16 v237, v167, 4.0, v159 op_sel_hi:[1,0,1]\n\t"
    "v_mfma_f32_16x16x32_bf16 v[0:3], a[4:7], a[212:215], v[0:3]\n\t"
    "ds_read_b64_tr_b16 a[16:17], v189 offset:18432\n\t"
    "ds_read_b64_tr_b16 a[18:19], v189 offset:18944\n\t"
    "v_mfma_f32_16x16x32_bf16 v[0:3], a[8:11], a[216:219], v[0:3]\n\t"
    "global_load_dwordx4 a[128:131], v242, s[16:17] offset:-4096 sc1\n\t"
    "v_pk_fma_f16 v240, v240, 0.5, v159 op_sel_hi:[1,0,1]\n\t"
    "v_mfma_f32_16x16x32_bf16 v[8:11], a[8:11], a[220:223], v[8:11]\n\t"
    "global_load_dwordx4 a[132:135], v242, s[16:17] offset:-3072 sc1\n\t"
    "v_pk_fma_f16 v238, v237, 2.0, v236 op_sel_hi:[1,0,1]\n\t"
    "v_mfma_f32_16x16x32_bf16 v[8:11], a[4:7], a[216:219], v[8:11]\n\t"
    "v_pk_fma_f16 v239, v237, -2.0, v236 op_sel_hi:[1,0,1]\n\t"
    "v_mfma_f32_16x16x32_bf16 v[8:11], a[0:3], a[212:215], v[8:11]\n\t"
    "s_waitcnt lgkmcnt(7)\n\t"
    "ds_write_addtid_b32 v238 offset:18688\n\t"
    "v_mfma_f32_16x16x32_bf16 v[16:19], a[0:3], a[216:219], v[16:19]\n\t"
    "ds_read_b64_tr_b16 a[20:21], v189 offset:22528\n\t"
    "ds_read_b64_tr_b16 a[22:23], v189 offset:23040\n\t"
    "v_mfma_f32_16x16x32_bf16 v[16:19], a[4:7], a[220:223], v[16:19]\n\t"
    "global_load_dwordx4 a[136:139], v242, s[16:17] offset:-2048 sc1\n\t"
    "v_pk_fma_f16 v240, v240, 0.5, v155 op_sel_hi:[1,0,1]\n\t"
    "v_mfma_f32_16x16x32_bf16 v[16:19], a[8:11], a[224:227], v[16:19]\n\t"
    "global_load_dwordx4 a[140:143], v242, s[16:17] offset:-1024 sc1\n\t"
    "ds_write_addtid_b32 v239 offset:27648\n\t"
    "v_mfma_f32_16x16x32_bf16 v[24:27], a[8:11], a[228:231], v[24:27]\n\t"
    "ds_write_addtid_b32 v240 offset:36608\n\t"
    "v_pk_add_f16 v236, v168, v170\n\t"
    "v_mfma_f32_16x16x32_bf16 v[24:27], a[4:7], a[224:227], v[24:27]\n\t"
    "global_load_dwordx4 a[144:147], v242, s[16:17] offset:0 sc1\n\t"
    "v_pk_add_f16 v237, v169, v171\n\t"
    "v_mfma_f32_16x16x32_bf16 v[24:27], a[0:3], a[220:223], v[24:27]\n\t"
    "v_pk_fma_f16 v240, v171, 0.5, v170 op_sel_hi:[1,0,1]\n\t"
    "v_pk_add_f16 v238, v236, v237\n\t"
    "v_mfma_f32_16x16x32_bf16 v[32:35], a[0:3], a[224:227], v[32:35]\n\t"
    "ds_read_b64_tr_b16 a[24:25], v189 offset:26624\n\t"
    "ds_read_b64_tr_b16 a[26:27], v189 offset:27136\n\t"
    "v_mfma_f32_16x16x32_bf16 v[32:35], a[4:7], a[228:231], v[32:35]\n\t"
    "s_waitcnt lgkmcnt(9)\n\t"
    "v_pk_add_f16 v239, v236, v237 neg_lo:[0,1] neg_hi:[0,1]\n\t"
    "v_mfma_f32_16x16x32_bf16 v[32:35], a[8:11], a[232:235], v[32:35]\n\t"
    "global_load_dwordx4 a[148:151], v242, s[16:17] offset:1024 sc1\n\t"
    "v_mfma_f32_16x16x32_bf16 v[40:43], a[8:11], a[236:239], v[40:43]\n\t"
    "global_load_dwordx4 a[152:155], v242, s[16:17] offset:2048 sc1\n\t"
    "v_mfma_f32_16x16x32_bf16 v[40:43], a[4:7], a[232:235], v[40:43]\n\t"
    "ds_write_b32 v184, v238 offset:0\n\t"
    "v_mfma_f32_16x16x32_bf16 v[40:43], a[0:3], a[228:231], v[40:43]\n\t"
    "global_load_dwordx4 a[156:159], v242, s[16:17] offset:3072 sc1\n\t"
    "v_pk_fma_f16 v236, v170, 4.0, v168 op_sel_hi:[1,0,1]\n\t"
    "v_mfma_f32_16x16x32_bf16 v[48:51], a[0:3], a[232:235], v[48:51]\n\t"
    "ds_write_b32 v184, v239 offset:8960\n\t"
    "v_mfma_f32_16x16x32_bf16 v[48:51], a[4:7], a[236:239], v[48:51]\n\t"
    "v_pk_fma_f16 v237, v171, 4.0, v169 op_sel_hi:[1,0,1]\n\t"
    "v_pk_fma_f16 v240, v240, 0.5, v169 op_sel_hi:[1,0,1]\n\t"
    "v_mfma_f32_16x16x32_bf16 v[48:51], a[8:11], a[240:243], v[48:51]\n\t"
    "v_add_u32 v242, 8192, v242\n\t"
    "v_pk_fma_f16 v238, v237, 2.0, v236 op_sel_hi:[1,0,1]\n\t"
    "v_mfma_f32_16x16x32_bf16 v[56:59], a[8:11], a[244:247], v[56:59]\n\t"
    "v_pk_fma_f16 v239, v237, -2.0, v236 op_sel_hi:[1,0,1]\n\t"
    "v_mfma_f32_16x16x32_bf16 v[56:59], a[4:7], a[240:243], v[56:59]\n\t"
    "global_load_dwordx4 a[160:163], v242, s[16:17] offset:-4096 sc1\n\t"
    "v_mfma_f32_16x16x32_bf16 v[56:59], a[0:3], a[236:239], v[56:59]\n\t"
    "s_waitcnt lgkmcnt(2)\n\t"
    "s_barrier\n\t"
    "v_mfma_f32_16x16x32_bf16 v[4:7], a[16:19], a[208:211], v[4:7]\n\t"
    "global_load_dwordx4 a[164:167], v242, s[16:17] offset:-3072 sc1\n\t"
    "v_mfma_f32_16x16x32_bf16 v[4:7], a[20:23], a[212:215], v[4:7]\n\t"
    "ds_write_b32 v184, v238 offset:17920\n\t"
    "v_mfma_f32_16x16x32_bf16 v[4:7], a[24:27], a[216:219], v[4:7]\n\t"
    "v_pk_fma_f16 v240, v240, 0.5, v168 op_sel_hi:[1,0,1]\n\t"
    "v_mfma_f32_16x16x32_bf16 v[12:15], a[24:27], a[220:223], v[12:15]\n\t"
    "global_load_dwordx4 a[168:171], v242, s[16:17] offset:-2048 sc1\n\t"
    "v_mfma_f32_16x16x32_bf16 v[12:15], a[20:23], a[216:219], v[12:15]\n\t"
    "ds_write_b32 v184, v239 offset:26880\n\t"
    "v_mfma_f32_16x16x32_bf16 v[12:15], a[16:19], a[212:215], v[12:15]\n\t"
    "global_load_dwordx4 a[172:175], v242, s[16:17] offset:-1024 sc1\n\t"
    "v_mfma_f32_16x16x32_bf16 v[20:23], a[16:19], a[216:219], v[20:23]\n\t"
    "ds_write_b32 v184, v240 offset:35840\n\t"
    "v_mfma_f32_16x16x32_bf16 v[20:23], a[20:23], a[220:223], v[20:23]\n\t"
    "v_mfma_f32_16x16x32_bf16 v[20:23], a[24:27], a[224:227], v[20:23]\n\t"
    "global_load_dwordx4 a[176:179], v242, s[16:17] offset:0 sc1\n\t"
    "v_mfma_f32_16x16x32_bf16 v[28:31], a[24:27], a[228:231], v[28:31]\n\t"
    "s_waitcnt lgkmcnt(0)\n\t"
    "s_barrier\n\t"
    "v_mfma_f32_16x16x32_bf16 v[28:31], a[20:23], a[224:227], v[28:31]\n\t"
    "global_load_dwordx4 a[180:183], v242, s[16:17] offset:1024 sc1\n\t"
    "ds_read_b128 a[248:251], v246 offset:64\n\t"
    "v_mfma_f32_16x16x32_bf16 v[28:31], a[16:19], a[220:223], v[28:31]\n\t"
    "ds_read_b128 a[208:211], v245 offset:0\n\t"
    "v_mov_b32_dpp v232, v0 row_shl:1 row_mask:0xf bank_mask:0xf bound_ctrl:1\n\t"
    "v_mfma_f32_16x16x32_bf16 v[36:39], a[16:19], a[224:227], v[36:39]\n\t"
    "global_load_dwordx4 a[184:187], v242, s[16:17] offset:2048 sc1\n\t"
    "ds_read_b128 a[212:215], v245 offset:32\n\t"
    "v_mfma_f32_16x16x32_bf16 v[36:39], a[20:23], a[228:231], v[36:39]\n\t"
    "ds_read_b128 a[216:219], v245 offset:64\n\t"
    "v_mov_b32_dpp v233, v1 row_shl:1 row_mask:0xf bank_mask:0xf bound_ctrl:1\n\t"
    "v_mfma_f32_16x16x32_bf16 v[36:39], a[24:27], a[232:235], v[36:39]\n\t"
    "ds_read_b128 a[220:223], v245 offset:96\n\t"
    "v_mov_b32_dpp v234, v2 row_shl:1 row_mask:0xf bank_mask:0xf bound_ctrl:1\n\t"
    "v_mfma_f32_16x16x32_bf16 v[44:47], a[24:27], a[236:239], v[44:47]\n\t"
    "global_load_dwordx4 a[188:191], v242, s[16:17] offset:3072 sc1\n\t"
    "ds_read_b128 a[224:227], v245 offset:128\n\t"
    "v_mfma_f32_16x16x32_bf16 v[44:47], a[20:23], a[232:235], v[44:47]\n\t"
    "v_mov_b32_dpp v235, v3 row_shl:1 row_mask:0xf bank_mask:0xf bound_ctrl:1\n\t"
    "v_mov_b32_dpp v236, v4 row_shl:1 row_mask:0xf bank_mask:0xf bound_ctrl:1\n\t"
    "v_mfma_f32_16x16x32_bf16 v[44:47], a[16:19], a[228:231], v[44:47]\n\t"
    "v_add_u32 v242, 8192, v242\n\t"
    "v_mov_b32_dpp v237, v5 row_shl:1 row_mask:0xf bank_mask:0xf bound_ctrl:1\n\t"
    "v_mfma_f32_16x16x32_bf16 v[52:55], a[16:19], a[232:235], v[52:55]\n\t"
    "ds_read_b128 a[228:231], v245 offset:160\n\t"
    "v_mov_b32_dpp v238, v6 row_shl:1 row_mask:0xf bank_mask:0xf bound_ctrl:1\n\t"
    "v_mfma_f32_16x16x32_bf16 v[52:55], a[20:23], a[236:239], v[52:55]\n\t"
    "ds_read_b128 a[232:235], v245 offset:192\n\t"
    "v_mov_b32_dpp v239, v7 row_shl:1 row_mask:0xf bank_mask:0xf bound_ctrl:1\n\t"
    "v_mfma_f32_16x16x32_bf16 v[52:55], a[24:27], a[240:243], v[52:55]\n\t"
    "v_mfma_f32_16x16x32_bf16 v[60:63], a[24:27], a[244:247], v[60:63]\n\t"
    "ds_read_b128 a[244:247], v246 offset:32\n\t"
    "v_mfma_f32_16x16x32_bf16 v[60:63], a[20:23], a[240:243], v[60:63]\n\t"
    "v_mfma_f32_16x16x32_bf16 v[60:63], a[16:19], a[236:239], v[60:63]\n\t"
    ".p2align 3\n\t"
    "s_cmp_lg_u32 s46, 0\n\t"
    "s_cbranch_scc1 L_sc_%=\n\tL_scb_%=:\n\t"
    "v_pk_fma_f32 v[136:137], v[64:65], s[48:49], v[0:1] op_sel:[0,1,0] op_sel_hi:[1,1,1]\n\t"
    "v_pk_fma_f32 v[138:139], v[66:67], s[48:49], v[2:3] op_sel:[0,1,0] op_sel_hi:[1,1,1]\n\t"
    "v_pk_fma_f32 v[140:141], v[68:69], s[48:49], v[4:5] op_sel:[0,1,0] op_sel_hi:[1,1,1]\n\t"
    "v_pk_fma_f32 v[142:143], v[70:71], s[48:49], v[6:7] op_sel:[0,1,0] op_sel_hi:[1,1,1]\n\t"
    "v_pk_add_f32 v[0:1], v[0:1], v[64:65] neg_lo:[0,1] neg_hi:[0,1]\n\t"
    "v_pk_add_f32 v[2:3], v[2:3], v[66:67] neg_lo:[0,1] neg_hi:[0,1]\n\t"
    "v_pk_add_f32 v[4:5], v[4:5], v[68:69] neg_lo:[0,1] neg_hi:[0,1]\n\t"
    "v_pk_add_f32 v[6:7], v[6:7], v[70:71] neg_lo:[0,1] neg_hi:[0,1]\n\t"
    "v_pk_fma_f32 v[64:65], v[64:65], s[50:51], v[0:1] op_sel:[0,0,0] op_sel_hi:[1,0,1]\n\t"
    "v_pk_fma_f32 v[66:67], v[66:67], s[50:51], v[2:3] op_sel:[0,0,0] op_sel_hi:[1,0,1]\n\t"
    "v_pk_fma_f32 v[68:69], v[68:69], s[50:51], v[4:5] op_sel:[0,0,0] op_sel_hi:[1,0,1]\n\t"
    "v_pk_fma_f32 v[70:71], v[70:71], s[50:51], v[6:7] op_sel:[0,0,0] op_sel_hi:[1,0,1]\n\t"
    "v_pk_fma_f32 v[144:145], v[72:73], s[48:49], v[8:9] op_sel:[0,1,0] op_sel_hi:[1,1,1]\n\t"
    "v_pk_fma_f32 v[146:147], v[74:75], s[48:49], v[10:11] op_sel:[0,1,0] op_sel_hi:[1,1,1]\n\t"
    "v_pk_fma_f32 v[148:149], v[76:77], s[48:49], v[12:13] op_sel:[0,1,0] op_sel_hi:[1,1,1]\n\t"
    "v_pk_fma_f32 v[150:151], v[78:79], s[48:49], v[14:15] op_sel:[0,1,0] op_sel_hi:[1,1,1]\n\t"
    "v_pk_add_f32 v[8:9], v[8:9], v[72:73] neg_lo:[0,1] neg_hi:[0,1]\n\t"
    "v_pk_add_f32 v[10:11], v[10:11], v[74:75] neg_lo:[0,1] neg_hi:[0,1]\n\t"
    "v_pk_add_f32 v[12:13], v[12:13], v[76:77] neg_lo:[0,1] neg_hi:[0,1]\n\t"
    "v_pk_add_f32 v[14:15], v[14:15], v[78:79] neg_lo:[0,1] neg_hi:[0,1]\n\t"
    "v_pk_fma_f32 v[72:73], v[72:73], s[50:51], v[8:9] op_sel:[0,0,0] op_sel_hi:[1,0,1]\n\t"
    "v_pk_fma_f32 v[74:75], v[74:75], s[50:51], v[10:11] op_sel:[0,0,0] op_sel_hi:[1,0,1]\n\t"
    "v_pk_fma_f32 v[76:77], v[76:77], s[50:51], v[12:13] op_sel:[0,0,0] op_sel_hi:[1,0,1]\n\t"
    "v_pk_fma_f32 v[78:79], v[78:79], s[50:51], v[14:15] op_sel:[0,0,0] op_sel_hi:[1,0,1]\n\t"
    "v_pk_fma_f32 v[136:137], v[144:145], s[52:53], v[136:137] op_sel:[0,0,0] op_sel_hi:[1,0,1] neg_lo:[0,0,1] neg_hi:[0,0,1]\n\t"
    "v_pk_fma_f32 v[138:139], v[146:147], s[52:53], v[138:139] op_sel:[0,0,0] op_sel_hi:[1,0,1] neg_lo:[0,0,1] neg_hi:[0,0,1]\n\t"
    "v_pk_fma_f32 v[140:141], v[148:149], s[52:53], v[140:141] op_sel:[0,0,0] op_sel_hi:[1,0,1] neg_lo:[0,0,1] neg_hi:[0,0,1]\n\t"
    "v_pk_fma_f32 v[142:143], v[150:151], s[52:53], v[142:143] op_sel:[0,0,0] op_sel_hi:[1,0,1] neg_lo:[0,0,1] neg_hi:[0,0,1]\n\t"
    "v_pk_fma_f32 v[64:65], v[72:73], s[50:51], v[64:65] op_sel:[0,1,0] op_sel_hi:[1,1,1] neg_lo:[0,0,1] neg_hi:[0,0,1]\n\t"
    "v_pk_fma_f32 v[66:67], v[74:75], s[50:51], v[66:67] op_sel:[0,1,0] op_sel_hi:[1,1,1] neg_lo:[0,0,1] neg_hi:[0,0,1]\n\t"
    "v_pk_fma_f32 v[68:69], v[76:77], s[50:51], v[68:69] op_sel:[0,1,0] op_sel_hi:[1,1,1] neg_lo:[0,0,1] neg_hi:[0,0,1]\n\t"
    "v_pk_fma_f32 v[70:71], v[78:79], s[50:51], v[70:71] op_sel:[0,1,0] op_sel_hi:[1,1,1] neg_lo:[0,0,1] neg_hi:[0,0,1]\n\t"
    "v_pk_add_f32 v[0:1], v[8:9], v[0:1] neg_lo:[0,1] neg_hi:[0,1]\n\t"
    "v_pk_add_f32 v[2:3], v[10:11], v[2:3] neg_lo:[0,1] neg_hi:[0,1]\n\t"
    "v_pk_add_f32 v[4:5], v[12:13], v[4:5] neg_lo:[0,1] neg_hi:[0,1]\n\t"
    "v_pk_add_f32 v[6:7], v[14:15], v[6:7] neg_lo:[0,1] neg_hi:[0,1]\n\t"
    "v_accvgpr_write_b32 a64, v64\n\t"
    "v_accvgpr_write_b32 a65, v65\n\t"
    "v_accvgpr_write_b32 a66, v66\n\t"
    "v_accvgpr_write_b32 a67, v67\n\t"
    "v_accvgpr_write_b32 a68, v68\n\t"
    "v_accvgpr_write_b32 a69, v69\n\t"
    "v_accvgpr_write_b32 a70, v70\n\t"
    "v_accvgpr_write_b32 a71, v71\n\t"
    "v_pk_fma_f32 v[152:153], v[80:81], s[48:49], v[16:17] op_sel:[0,1,0] op_sel_hi:[1,1,1]\n\t"
    "v_pk_fma_f32 v[154:155], v[82:83], s[48:49], v[18:19] op_sel:[0,1,0] op_sel_hi:[1,1,1]\n\t"
    "v_pk_fma_f32 v[156:157], v[84:85], s[48:49], v[20:21] op_sel:[0,1,0] op_sel_hi:[1,1,1]\n\t"
    "v_pk_fma_f32 v[158:159], v[86:87], s[48:49], v[22:23] op_sel:[0,1,0] op_sel_hi:[1,1,1]\n\t"
    "v_pk_add_f32 v[16:17], v[16:17], v[80:81] neg_lo:[0,1] neg_hi:[0,1]\n\t"
    "v_pk_add_f32 v[18:19], v[18:19], v[82:83] neg_lo:[0,1] neg_hi:[0,1]\n\t"
    "v_pk_add_f32 v[20:21], v[20:21], v[84:85] neg_lo:[0,1] neg_hi:[0,1]\n\t"
    "v_pk_add_f32 v[22:23], v[22:23], v[86:87] neg_lo:[0,1] neg_hi:[0,1]\n\t"
    "v_pk_fma_f32 v[80:81], v[80:81], s[50:51], v[16:17] op_sel:[0,0,0] op_sel_hi:[1,0,1]\n\t"
    "v_pk_fma_f32 v[82:83], v[82:83], s[50:51], v[18:19] op_sel:[0,0,0] op_sel_hi:[1,0,1]\n\t"
    "v_pk_fma_f32 v[84:85], v[84:85], s[50:51], v[20:21] op_sel:[0,0,0] op_sel_hi:[1,0,1]\n\t"
    "v_pk_fma_f32 v[86:87], v[86:87], s[50:51], v[22:23] op_sel:[0,0,0] op_sel_hi:[1,0,1]\n\t"
    "v_pk_fma_f32 v[144:145], v[152:153], s[52:53], v[144:145] op_sel:[0,0,0] op_sel_hi:[1,0,1] neg_lo:[0,0,1] neg_hi:[0,0,1]\n\t"
    "v_pk_fma_f32 v[146:147], v[154:155], s[52:53], v[146:147] op_sel:[0,0,0] op_sel_hi:[1,0,1] neg_lo:[0,0,1] neg_hi:[0,0,1]\n\t"
    "v_pk_fma_f32 v[148:149], v[156:157], s[52:53], v[148:149] op_sel:[0,0,0] op_sel_hi:[1,0,1] neg_lo:[0,0,1] neg_hi:[0,0,1]\n\t"
    "v_pk_fma_f32 v[150:151], v[158:159], s[52:53], v[150:151] op_sel:[0,0,0] op_sel_hi:[1,0,1] neg_lo:[0,0,1] neg_hi:[0,0,1]\n\t"
    "v_pk_fma_f32 v[72:73], v[80:81], s[50:51], v[72:73] op_sel:[0,1,0] op_sel_hi:[1,1,1] neg_lo:[0,0,1] neg_hi:[0,0,1]\n\t"
    "v_pk_fma_f32 v[74:75], v[82:83], s[50:51], v[74:75] op_sel:[0,1,0] op_sel_hi:[1,1,1] neg_lo:[0,0,1] neg_hi:[0,0,1]\n\t"
    "v_pk_fma_f32 v[76:77], v[84:85], s[50:51], v[76:77] op_sel:[0,1,0] op_sel_hi:[1,1,1] neg_lo:[0,0,1] neg_hi:[0,0,1]\n\t"
    "v_pk_fma_f32 v[78:79], v[86:87], s[50:51], v[78:79] op_sel:[0,1,0] op_sel_hi:[1,1,1] neg_lo:[0,0,1] neg_hi:[0,0,1]\n\t"
    "v_pk_add_f32 v[8:9], v[16:17], v[8:9] neg_lo:[0,1] neg_hi:[0,1]\n\t"
    "v_pk_add_f32 v[10:11], v[18:19], v[10:11] neg_lo:[0,1] neg_hi:[0,1]\n\t"
    "v_pk_add_f32 v[12:13], v[20:21], v[12:13] neg_lo:[0,1] neg_hi:[0,1]\n\t"
    "v_pk_add_f32 v[14:15], v[22:23], v[14:15] neg_lo:[0,1] neg_hi:[0,1]\n\t"
    "v_accvgpr_write_b32 a72, v72\n\t"
    "v_accvgpr_write_b32 a73, v73\n\t"
    "v_accvgpr_write_b32 a74, v74\n\t"
    "v_accvgpr_write_b32 a75, v75\n\t"
    "v_accvgpr_write_b32 a76, v76\n\t"
    "v_accvgpr_write_b32 a77, v77\n\t"
    "v_accvgpr_write_b32 a78, v78\n\t"
    "v_accvgpr_write_b32 a79, v79\n\t"
    "v_pk_fma_f32 v[160:161], v[88:89], s[48:49], v[24:25] op_sel:[0,1,0] op_sel_hi:[1,1,1]\n\t"
    "v_pk_fma_f32 v[162:163], v[90:91], s[48:49], v[26:27] op_sel:[0,1,0] op_sel_hi:[1,1,1]\n\t"
    "v_pk_fma_f32 v[164:165], v[92:93], s[48:49], v[28:29] op_sel:[0,1,0] op_sel_hi:[1,1,1]\n\t"
    "v_pk_fma_f32 v[166:167], v[94:95], s[48:49], v[30:31] op_sel:[0,1,0] op_sel_hi:[1,1,1]\n\t"
    "v_pk_add_f32 v[24:25], v[24:25], v[88:89] neg_lo:[0,1] neg_hi:[0,1]\n\t"
    "v_pk_add_f32 v[26:27], v[26:27], v[90:91] neg_lo:[0,1] neg_hi:[0,1]\n\t"
    "v_pk_add_f32 v[28:29], v[28:29], v[92:93] neg_lo:[0,1] neg_hi:[0,1]\n\t"
    "v_pk_add_f32 v[30:31], v[30:31], v[94:95] neg_lo:[0,1] neg_hi:[0,1]\n\t"
    "v_pk_fma_f32 v[88:89], v[88:89], s[50:51], v[24:25] op_sel:[0,0,0] op_sel_hi:[1,0,1]\n\t"
    "v_pk_fma_f32 v[90:91], v[90:91], s[50:51], v[26:27] op_sel:[0,0,0] op_sel_hi:[1,0,1]\n\t"
    "v_pk_fma_f32 v[92:93], v[92:93], s[50:51], v[28:29] op_sel:[0,0,0] op_sel_hi:[1,0,1]\n\t"
    "v_pk_fma_f32 v[94:95], v[94:95], s[50:51], v[30:31] op_sel:[0,0,0] op_sel_hi:[1,0,1]\n\t"
    "v_pk_fma_f32 v[152:153], v[160:161], s[52:53], v[152:153] op_sel:[0,0,0] op_sel_hi:[1,0,1] neg_lo:[0,0,1] neg_hi:[0,0,1]\n\t"
    "v_pk_fma_f32 v[154:155], v[162:163], s[52:53], v[154:155] op_sel:[0,0,0] op_sel_hi:[1,0,1] neg_lo:[0,0,1] neg_hi:[0,0,1]\n\t"
    "v_pk_fma_f32 v[156:157], v[164:165], s[52:53], v[156:157] op_sel:[0,0,0] op_sel_hi:[1,0,1] neg_lo:[0,0,1] neg_hi:[0,0,1]\n\t"
    "v_pk_fma_f32 v[158:159], v[166:167], s[52:53], v[158:159] op_sel:[0,0,0] op_sel_hi:[1,0,1] neg_lo:[0,0,1] neg_hi:[0,0,1]\n\t"
    "v_pk_fma_f32 v[80:81], v[88:89], s[50:51], v[80:81] op_sel:[0,1,0] op_sel_hi:[1,1,1] neg_lo:[0,0,1] neg_hi:[0,0,1]\n\t"
    "v_pk_fma_f32 v[82:83], v[90:91], s[50:51], v[82:83] op_sel:[0,1,0] op_sel_hi:[1,1,1] neg_lo:[0,0,1] neg_hi:[0,0,1]\n\t"
    "v_pk_fma_f32 v[84:85], v[92:93], s[50:51], v[84:85] op_sel:[0,1,0] op_sel_hi:[1,1,1] neg_lo:[0,0,1] neg_hi:[0,0,1]\n\t"
    "v_pk_fma_f32 v[86:87], v[94:95], s[50:51], v[86:87] op_sel:[0,1,0] op_sel_hi:[1,1,1] neg_lo:[0,0,1] neg_hi:[0,0,1]\n\t"
    "v_pk_add_f32 v[16:17], v[24:25], v[16:17] neg_lo:[0,1] neg_hi:[0,1]\n\t"
    "v_pk_add_f32 v[18:19], v[26:27], v[18:19] neg_lo:[0,1] neg_hi:[0,1]\n\t"
    "v_pk_add_f32 v[20:21], v[28:29], v[20:21] neg_lo:[0,1] neg_hi:[0,1]\n\t"
    "v_pk_add_f32 v[22:23], v[30:31], v[22:23] neg_lo:[0,1] neg_hi:[0,1]\n\t"
    "v_mfma_f32_32x32x16_bf16 v[64:79], a[192:195], a[192:195], v[0:15]\n\t"
    "v_accvgpr_write_b32 a80, v80\n\t"
    "v_accvgpr_write_b32 a81, v81\n\t"
    "v_accvgpr_write_b32 a82, v82\n\t"
    "v_accvgpr_write_b32 a83, v83\n\t"
    "v_accvgpr_write_b32 a84, v84\n\t"
    "v_accvgpr_write_b32 a85, v85\n\t"
    "v_accvgpr_write_b32 a86, v86\n\t"
    "v_accvgpr_write_b32 a87, v87\n\t"
    "v_pk_fma_f32 v[168:169], v[96:97], s[48:49], v[32:33] op_sel:[0,1,0] op_sel_hi:[1,1,1]\n\t"
    "v_pk_fma_f32 v[170:171], v[98:99], s[48:49], v[34:35] op_sel:[0,1,0] op_sel_hi:[1,1,1]\n\t"
    "v_pk_fma_f32 v[172:173], v[100:101], s[48:49], v[36:37] op_sel:[0,1,0] op_sel_hi:[1,1,1]\n\t"
    "v_pk_fma_f32 v[174:175], v[102:103], s[48:49], v[38:39] op_sel:[0,1,0] op_sel_hi:[1,1,1]\n\t"
    "v_pk_add_f32 v[32:33], v[32:33], v[96:97] neg_lo:[0,1] neg_hi:[0,1]\n\t"
    "v_pk_add_f32 v[34:35], v[34:35], v[98:99] neg_lo:[0,1] neg_hi:[0,1]\n\t"
    "v_pk_add_f32 v[36:37], v[36:37], v[100:101] neg_lo:[0,1] neg_hi:[0,1]\n\t"
    "v_pk_add_f32 v[38:39], v[38:39], v[102:103] neg_lo:[0,1] neg_hi:[0,1]\n\t"
    "v_pk_fma_f32 v[96:97], v[96:97], s[50:51], v[32:33] op_sel:[0,0,0] op_sel_hi:[1,0,1]\n\t"
    "v_pk_fma_f32 v[98:99], v[98:99], s[50:51], v[34:35] op_sel:[0,0,0] op_sel_hi:[1,0,1]\n\t"
    "v_pk_fma_f32 v[100:101], v[100:101], s[50:51], v[36:37] op_sel:[0,0,0] op_sel_hi:[1,0,1]\n\t"
    "v_pk_fma_f32 v[102:103], v[102:103], s[50:51], v[38:39] op_sel:[0,0,0] op_sel_hi:[1,0,1]\n\t"
    "v_pk_fma_f32 v[160:161], v[168:169], s[52:53], v[160:161] op_sel:[0,0,0] op_sel_hi:[1,0,1] neg_lo:[0,0,1] neg_hi:[0,0,1]\n\t"
    "v_pk_fma_f32 v[162:163], v[170:171], s[52:53], v[162:163] op_sel:[0,0,0] op_sel_hi:[1,0,1] neg_lo:[0,0,1] neg_hi:[0,0,1]\n\t"
    "v_pk_fma_f32 v[164:165], v[172:173], s[52:53], v[164:165] op_sel:[0,0,0] op_sel_hi:[1,0,1] neg_lo:[0,0,1] neg_hi:[0,0,1]\n\t"
    "v_pk_fma_f32 v[166:167], v[174:175], s[52:53], v[166:167] op_sel:[0,0,0] op_sel_hi:[1,0,1] neg_lo:[0,0,1] neg_hi:[0,0,1]\n\t"
    "v_pk_fma_f32 v[88:89], v[96:97], s[50:51], v[88:89] op_sel:[0,1,0] op_sel_hi:[1,1,1] neg_lo:[0,0,1] neg_hi:[0,0,1]\n\t"
    "v_pk_fma_f32 v[90:91], v[98:99], s[50:51], v[90:91] op_sel:[0,1,0] op_sel_hi:[1,1,1] neg_lo:[0,0,1] neg_hi:[0,0,1]\n\t"
    "v_pk_fma_f32 v[92:93], v[100:101], s[50:51], v[92:93] op_sel:[0,1,0] op_sel_hi:[1,1,1] neg_lo:[0,0,1] neg_hi:[0,0,1]\n\t"
    "v_pk_fma_f32 v[94:95], v[102:103], s[50:51], v[94:95] op_sel:[0,1,0] op_sel_hi:[1,1,1] neg_lo:[0,0,1] neg_hi:[0,0,1]\n\t"
    "s_waitcnt vmcnt(16)\n\t"
    "s_nop 0\n\t"
    "v_bfi_b32 v197, v198, s70, v197\n\t"
    "buffer_store_dword v197, v196, s[88:91], 0 offen sc1\n\t"
    "v_pk_add_f32 v[24:25], v[32:33], v[24:25] neg_lo:[0,1] neg_hi:[0,1]\n\t"
    "v_pk_add_f32 v[26:27], v[34:35], v[26:27] neg_lo:[0,1] neg_hi:[0,1]\n\t"
    "v_pk_add_f32 v[28:29], v[36:37], v[28:29] neg_lo:[0,1] neg_hi:[0,1]\n\t"
    "v_pk_add_f32 v[30:31], v[38:39], v[30:31] neg_lo:[0,1] neg_hi:[0,1]\n\t"
    "v_mfma_f32_32x32x16_bf16 a[0:15], a[192:195], a[192:195], a[64:79]\n\t"
    "v_accvgpr_write_b32 a88, v88\n\t"
    "v_accvgpr_write_b32 a89, v89\n\t"
    "v_accvgpr_write_b32 a90, v90\n\t"
    "v_accvgpr_write_b32 a91, v91\n\t"
    "v_accvgpr_write_b32 a92, v92\n\t"
    "v_accvgpr_write_b32 a93, v93\n\t"
    "v_accvgpr_write_b32 a94, v94\n\t"
    "v_accvgpr_write_b32 a95, v95\n\t"
    "v_pk_fma_f32 v[176:177], v[104:105], s[48:49], v[40:41] op_sel:[0,1,0] op_sel_hi:[1,1,1]\n\t"
    "v_pk_fma_f32 v[178:179], v[106:107], s[48:49], v[42:43] op_sel:[0,1,0] op_sel_hi:[1,1,1]\n\t"
    "v_pk_fma_f32 v[180:181], v[108:109], s[48:49], v[44:45] op_sel:[0,1,0] op_sel_hi:[1,1,1]\n\t"
    "v_pk_fma_f32 v[182:183], v[110:111], s[48:49], v[46:47] op_sel:[0,1,0] op_sel_hi:[1,1,1]\n\t"
    "v_pk_add_f32 v[40:41], v[40:41], v[104:105] neg_lo:[0,1] neg_hi:[0,1]\n\t"
    "v_pk_add_f32 v[42:43], v[42:43], v[106:107] neg_lo:[0,1] neg_hi:[0,1]\n\t"
    "v_pk_add_f32 v[44:45], v[44:45], v[108:109] neg_lo:[0,1] neg_hi:[0,1]\n\t"
    "v_pk_add_f32 v[46:47], v[46:47], v[110:111] neg_lo:[0,1] neg_hi:[0,1]\n\t"
    "v_pk_fma_f32 v[104:105], v[104:105], s[50:51], v[40:41] op_sel:[0,0,0] op_sel_hi:[1,0,1]\n\t"
    "v_pk_fma_f32 v[106:107], v[106:107], s[50:51], v[42:43] op_sel:[0,0,0] op_sel_hi:[1,0,1]\n\t"
    "v_pk_fma_f32 v[108:109], v[108:109], s[50:51], v[44:45] op_sel:[0,0,0] op_sel_hi:[1,0,1]\n\t"
    "v_pk_fma_f32 v[110:111], v[110:111], s[50:51], v[46:47] op_sel:[0,0,0] op_sel_hi:[1,0,1]\n\t"
    "v_pk_fma_f32 v[168:169], v[176:177], s[52:53], v[168:169] op_sel:[0,0,0] op_sel_hi:[1,0,1] neg_lo:[0,0,1] neg_hi:[0,0,1]\n\t"
    "v_pk_fma_f32 v[170:171], v[178:179], s[52:53], v[170:171] op_sel:[0,0,0] op_sel_hi:[1,0,1] neg_lo:[0,0,1] neg_hi:[0,0,1]\n\t"
    "v_pk_fma_f32 v[172:173], v[180:181], s[52:53], v[172:173] op_sel:[0,0,0] op_sel_hi:[1,0,1] neg_lo:[0,0,1] neg_hi:[0,0,1]\n\t"
    "v_pk_fma_f32 v[174:175], v[182:183], s[52:53], v[174:175] op_sel:[0,0,0] op_sel_hi:[1,0,1] neg_lo:[0,0,1] neg_hi:[0,0,1]\n\t"
    "v_pk_fma_f32 v[96:97], v[104:105], s[50:51], v[96:97] op_sel:[0,1,0] op_sel_hi:[1,1,1] neg_lo:[0,0,1] neg_hi:[0,0,1]\n\t"
    "v_pk_fma_f32 v[98:99], v[106:107], s[50:51], v[98:99] op_sel:[0,1,0] op_sel_hi:[1,1,1] neg_lo:[0,0,1] neg_hi:[0,0,1]\n\t"
    "v_pk_fma_f32 v[100:101], v[108:109], s[50:51], v[100:101] op_sel:[0,1,0] op_sel_hi:[1,1,1] neg_lo:[0,0,1] neg_hi:[0,0,1]\n\t"
    "v_pk_fma_f32 v[102:103], v[110:111], s[50:51], v[102:103] op_sel:[0,1,0] op_sel_hi:[1,1,1] neg_lo:[0,0,1] neg_hi:[0,0,1]\n\t"
    "v_pk_add_f32 v[32:33], v[40:41], v[32:33] neg_lo:[0,1] neg_hi:[0,1]\n\t"
    "v_pk_add_f32 v[34:35], v[42:43], v[34:35] neg_lo:[0,1] neg_hi:[0,1]\n\t"
    "v_pk_add_f32 v[36:37], v[44:45], v[36:37] neg_lo:[0,1] neg_hi:[0,1]\n\t"
    "v_pk_add_f32 v[38:39], v[46:47], v[38:39] neg_lo:[0,1] neg_hi:[0,1]\n\t"
    "v_mfma_f32_32x32x16_bf16 v[80:95], a[192:195], a[192:195], v[16:31]\n\t"
    "v_accvgpr_write_b32 a96, v96\n\t"
    "v_accvgpr_write_b32 a97, v97\n\t"
    "v_accvgpr_write_b32 a98, v98\n\t"
    "v_accvgpr_write_b32 a99, v99\n\t"
    "v_accvgpr_write_b32 a100, v100\n\t"
    "v_accvgpr_write_b32 a101, v101\n\t"
    "v_accvgpr_write_b32 a102, v102\n\t"
    "v_accvgpr_write_b32 a103, v103\n\t"
    "v_pk_fma_f32 v[184:185], v[112:113], s[48:49], v[48:49] op_sel:[0,1,0] op_sel_hi:[1,1,1]\n\t"
    "v_pk_fma_f32 v[186:187], v[114:115], s[48:49], v[50:51] op_sel:[0,1,0] op_sel_hi:[1,1,1]\n\t"
    "v_pk_fma_f32 v[188:189], v[116:117], s[48:49], v[52:53] op_sel:[0,1,0] op_sel_hi:[1,1,1]\n\t"
    "v_pk_fma_f32 v[190:191], v[118:119], s[48:49], v[54:55] op_sel:[0,1,0] op_sel_hi:[1,1,1]\n\t"
    "v_pk_add_f32 v[48:49], v[48:49], v[112:113] neg_lo:[0,1] neg_hi:[0,1]\n\t"
    "v_pk_add_f32 v[50:51], v[50:51], v[114:115] neg_lo:[0,1] neg_hi:[0,1]\n\t"
    "v_pk_add_f32 v[52:53], v[52:53], v[116:117] neg_lo:[0,1] neg_hi:[0,1]\n\t"
    "v_pk_add_f32 v[54:55], v[54:55], v[118:119] neg_lo:[0,1] neg_hi:[0,1]\n\t"
    "v_pk_fma_f32 v[112:113], v[112:113], s[50:51], v[48:49] op_sel:[0,0,0] op_sel_hi:[1,0,1]\n\t"
    "v_pk_fma_f32 v[114:115], v[114:115], s[50:51], v[50:51] op_sel:[0,0,0] op_sel_hi:[1,0,1]\n\t"
    "v_pk_fma_f32 v[116:117], v[116:117], s[50:51], v[52:53] op_sel:[0,0,0] op_sel_hi:[1,0,1]\n\t"
    "v_pk_fma_f32 v[118:119], v[118:119], s[50:51], v[54:55] op_sel:[0,0,0] op_sel_hi:[1,0,1]\n\t"
    "v_pk_fma_f32 v[176:177], v[184:185], s[52:53], v[176:177] op_sel:[0,0,0] op_sel_hi:[1,0,1] neg_lo:[0,0,1] neg_hi:[0,0,1]\n\t"
    "v_pk_fma_f32 v[178:179], v[186:187], s[52:53], v[178:179] op_sel:[0,0,0] op_sel_hi:[1,0,1] neg_lo:[0,0,1] neg_hi:[0,0,1]\n\t"
    "v_pk_fma_f32 v[180:181], v[188:189], s[52:53], v[180:181] op_sel:[0,0,0] op_sel_hi:[1,0,1] neg_lo:[0,0,1] neg_hi:[0,0,1]\n\t"
    "v_pk_fma_f32 v[182:183], v[190:191], s[52:53], v[182:183] op_sel:[0,0,0] op_sel_hi:[1,0,1] neg_lo:[0,0,1] neg_hi:[0,0,1]\n\t"
    "v_pk_fma_f32 v[104:105], v[112:113], s[50:51], v[104:105] op_sel:[0,1,0] op_sel_hi:[1,1,1] neg_lo:[0,0,1] neg_hi:[0,0,1]\n\t"
    "v_pk_fma_f32 v[106:107], v[114:115], s[50:51], v[106:107] op_sel:[0,1,0] op_sel_hi:[1,1,1] neg_lo:[0,0,1] neg_hi:[0,0,1]\n\t"
    "v_pk_fma_f32 v[108:109], v[116:117], s[50:51], v[108:109] op_sel:[0,1,0] op_sel_hi:[1,1,1] neg_lo:[0,0,1] neg_hi:[0,0,1]\n\t"
    "v_pk_fma_f32 v[110:111], v[118:119], s[50:51], v[110:111] op_sel:[0,1,0] op_sel_hi:[1,1,1] neg_lo:[0,0,1] neg_hi:[0,0,1]\n\t"
    "v_pk_add_f32 v[40:41], v[48:49], v[40:41] neg_lo:[0,1] neg_hi:[0,1]\n\t"
    "v_pk_add_f32 v[42:43], v[50:51], v[42:43] neg_lo:[0,1] neg_hi:[0,1]\n\t"
    "v_pk_add_f32 v[44:45], v[52:53], v[44:45] neg_lo:[0,1] neg_hi:[0,1]\n\t"
    "v_pk_add_f32 v[46:47], v[54:55], v[46:47] neg_lo:[0,1] neg_hi:[0,1]\n\t"
    "v_mfma_f32_32x32x16_bf16 a[16:31], a[192:195], a[192:195], a[80:95]\n\t"
    "v_accvgpr_write_b32 a104, v104\n\t"
    "v_accvgpr_write_b32 a105, v105\n\t"
    "v_accvgpr_write_b32 a106, v106\n\t"
    "v_accvgpr_write_b32 a107, v107\n\t"
    "v_accvgpr_write_b32 a108, v108\n\t"
    "v_accvgpr_write_b32 a109, v109\n\t"
    "v_accvgpr_write_b32 a110, v110\n\t"
    "v_accvgpr_write_b32 a111, v111\n\t"
    "v_pk_fma_f32 v[192:193], v[120:121], s[48:49], v[56:57] op_sel:[0,1,0] op_sel_hi:[1,1,1]\n\t"
    "v_pk_fma_f32 v[194:195], v[122:123], s[48:49], v[58:59] op_sel:[0,1,0] op_sel_hi:[1,1,1]\n\t"
    "v_pk_fma_f32 v[196:197], v[124:125], s[48:49], v[60:61] op_sel:[0,1,0] op_sel_hi:[1,1,1]\n\t"
    "v_pk_fma_f32 v[198:199], v[126:127], s[48:49], v[62:63] op_sel:[0,1,0] op_sel_hi:[1,1,1]\n\t"
    "v_pk_add_f32 v[56:57], v[56:57], v[120:121] neg_lo:[0,1] neg_hi:[0,1]\n\t"
    "v_pk_add_f32 v[58:59], v[58:59], v[122:123] neg_lo:[0,1] neg_hi:[0,1]\n\t"
    "v_pk_add_f32 v[60:61], v[60:61], v[124:125] neg_lo:[0,1] neg_hi:[0,1]\n\t"
    "v_pk_add_f32 v[62:63], v[62:63], v[126:127] neg_lo:[0,1] neg_hi:[0,1]\n\t"
    "v_pk_fma_f32 v[120:121], v[120:121], s[50:51], v[56:57] op_sel:[0,0,0] op_sel_hi:[1,0,1]\n\t"
    "v_pk_fma_f32 v[122:123], v[122:123], s[50:51], v[58:59] op_sel:[0,0,0] op_sel_hi:[1,0,1]\n\t"
    "v_pk_fma_f32 v[124:125], v[124:125], s[50:51], v[60:61] op_sel:[0,0,0] op_sel_hi:[1,0,1]\n\t"
    "v_pk_fma_f32 v[126:127], v[126:127], s[50:51], v[62:63] op_sel:[0,0,0] op_sel_hi:[1,0,1]\n\t"
    "v_pk_fma_f32 v[184:185], v[192:193], s[52:53], v[184:185] op_sel:[0,0,0] op_sel_hi:[1,0,1] neg_lo:[0,0,1] neg_hi:[0,0,1]\n\t"
    "v_pk_fma_f32 v[186:187], v[194:195], s[52:53], v[186:187] op_sel:[0,0,0] op_sel_hi:[1,0,1] neg_lo:[0,0,1] neg_hi:[0,0,1]\n\t"
    "v_pk_fma_f32 v[188:189], v[196:197], s[52:53], v[188:189] op_sel:[0,0,0] op_sel_hi:[1,0,1] neg_lo:[0,0,1] neg_hi:[0,0,1]\n\t"
    "v_pk_fma_f32 v[190:191], v[198:199], s[52:53], v[190:191] op_sel:[0,0,0] op_sel_hi:[1,0,1] neg_lo:[0,0,1] neg_hi:[0,0,1]\n\t"
    "v_pk_fma_f32 v[112:113], v[120:121], s[50:51], v[112:113] op_sel:[0,1,0] op_sel_hi:[1,1,1] neg_lo:[0,0,1] neg_hi:[0,0,1]\n\t"
    "v_pk_fma_f32 v[114:115], v[122:123], s[50:51], v[114:115] op_sel:[0,1,0] op_sel_hi:[1,1,1] neg_lo:[0,0,1] neg_hi:[0,0,1]\n\t"
    "v_pk_fma_f32 v[116:117], v[124:125], s[50:51], v[116:117] op_sel:[0,1,0] op_sel_hi:[1,1,1] neg_lo:[0,0,1] neg_hi:[0,0,1]\n\t"
    "v_pk_fma_f32 v[118:119], v[126:127], s[50:51], v[118:119] op_sel:[0,1,0] op_sel_hi:[1,1,1] neg_lo:[0,0,1] neg_hi:[0,0,1]\n\t"
    "v_pk_add_f32 v[48:49], v[56:57], v[48:49] neg_lo:[0,1] neg_hi:[0,1]\n\t"
    "v_pk_add_f32 v[50:51], v[58:59], v[50:51] neg_lo:[0,1] neg_hi:[0,1]\n\t"
    "v_pk_add_f32 v[52:53], v[60:61], v[52:53] neg_lo:[0,1] neg_hi:[0,1]\n\t"
    "v_pk_add_f32 v[54:55], v[62:63], v[54:55] neg_lo:[0,1] neg_hi:[0,1]\n\t"
    "v_mfma_f32_32x32x16_bf16 v[96:111], a[192:195], a[192:195], v[32:47]\n\t"
    "v_accvgpr_write_b32 a112, v112\n\t"
    "v_accvgpr_write_b32 a113, v113\n\t"
    "v_accvgpr_write_b32 a114, v114\n\t"
    "v_accvgpr_write_b32 a115, v115\n\t"
    "v_accvgpr_write_b32 a116, v116\n\t"
    "v_accvgpr_write_b32 a117, v117\n\t"
    "v_accvgpr_write_b32 a118, v118\n\t"
    "v_accvgpr_write_b32 a119, v119\n\t"
    "v_pk_fma_f32 v[240:241], v[128:129], s[48:49], v[232:233] op_sel:[0,1,0] op_sel_hi:[1,1,1]\n\t"
    "v_pk_add_f32 v[232:233], v[232:233], v[128:129] neg_lo:[0,1] neg_hi:[0,1]\n\t"
    "v_pk_fma_f32 v[192:193], v[240:241], s[52:53], v[192:193] op_sel:[0,0,0] op_sel_hi:[1,0,1] neg_lo:[0,0,1] neg_hi:[0,0,1]\n\t"
    "v_pk_fma_f32 v[240:241], v[130:131], s[48:49], v[234:235] op_sel:[0,1,0] op_sel_hi:[1,1,1]\n\t"
    "v_pk_add_f32 v[234:235], v[234:235], v[130:131] neg_lo:[0,1] neg_hi:[0,1]\n\t"
    "v_pk_fma_f32 v[194:195], v[240:241], s[52:53], v[194:195] op_sel:[0,0,0] op_sel_hi:[1,0,1] neg_lo:[0,0,1] neg_hi:[0,0,1]\n\t"
    "v_pk_fma_f32 v[240:241], v[132:133], s[48:49], v[236:237] op_sel:[0,1,0] op_sel_hi:[1,1,1]\n\t"
    "v_pk_add_f32 v[236:237], v[236:237], v[132:133] neg_lo:[0,1] neg_hi:[0,1]\n\t"
    "v_pk_fma_f32 v[196:197], v[240:241], s[52:53], v[196:197] op_sel:[0,0,0] op_sel_hi:[1,0,1] neg_lo:[0,0,1] neg_hi:[0,0,1]\n\t"
    "v_pk_fma_f32 v[240:241], v[134:135], s[48:49], v[238:239] op_sel:[0,1,0] op_sel_hi:[1,1,1]\n\t"
    "v_pk_add_f32 v[238:239], v[238:239], v[134:135] neg_lo:[0,1] neg_hi:[0,1]\n\t"
    "v_pk_fma_f32 v[198:199], v[240:241], s[52:53], v[198:199] op_sel:[0,0,0] op_sel_hi:[1,0,1] neg_lo:[0,0,1] neg_hi:[0,0,1]\n\t"
    "v_pk_fma_f32 v[128:129], v[128:129], s[50:51], v[232:233] op_sel:[0,0,0] op_sel_hi:[1,0,1]\n\t"
    "v_pk_fma_f32 v[130:131], v[130:131], s[50:51], v[234:235] op_sel:[0,0,0] op_sel_hi:[1,0,1]\n\t"
    "v_pk_fma_f32 v[132:133], v[132:133], s[50:51], v[236:237] op_sel:[0,0,0] op_sel_hi:[1,0,1]\n\t"
    "v_pk_fma_f32 v[134:135], v[134:135], s[50:51], v[238:239] op_sel:[0,0,0] op_sel_hi:[1,0,1]\n\t"
    "v_pk_fma_f32 v[120:121], v[128:129], s[50:51], v[120:121] op_sel:[0,1,0] op_sel_hi:[1,1,1] neg_lo:[0,0,1] neg_hi:[0,0,1]\n\t"
    "v_pk_fma_f32 v[122:123], v[130:131], s[50:51], v[122:123] op_sel:[0,1,0] op_sel_hi:[1,1,1] neg_lo:[0,0,1] neg_hi:[0,0,1]\n\t"
    "v_pk_fma_f32 v[124:125], v[132:133], s[50:51], v[124:125] op_sel:[0,1,0] op_sel_hi:[1,1,1] neg_lo:[0,0,1] neg_hi:[0,0,1]\n\t"
    "v_pk_fma_f32 v[126:127], v[134:135], s[50:51], v[126:127] op_sel:[0,1,0] op_sel_hi:[1,1,1] neg_lo:[0,0,1] neg_hi:[0,0,1]\n\t"
    "v_pk_add_f32 v[56:57], v[232:233], v[56:57] neg_lo:[0,1] neg_hi:[0,1]\n\t"
    "v_pk_add_f32 v[58:59], v[234:235], v[58:59] neg_lo:[0,1] neg_hi:[0,1]\n\t"
    "v_pk_add_f32 v[60:61], v[236:237], v[60:61] neg_lo:[0,1] neg_hi:[0,1]\n\t"
    "v_pk_add_f32 v[62:63], v[238:239], v[62:63] neg_lo:[0,1] neg_hi:[0,1]\n\t"
    "v_mfma_f32_32x32x16_bf16 a[32:47], a[192:195], a[192:195], a[96:111]\n\t"
    "v_accvgpr_write_b32 a120, v120\n\t"
    "v_accvgpr_write_b32 a121, v121\n\t"
    "v_accvgpr_write_b32 a122, v122\n\t"
    "v_accvgpr_write_b32 a123, v123\n\t"
    "v_accvgpr_write_b32 a124, v124\n\t"
    "v_accvgpr_write_b32 a125, v125\n\t"
    "v_accvgpr_write_b32 a126, v126\n\t"
    "v_accvgpr_write_b32 a127, v127\n\t"
    "v_mfma_f32_32x32x16_bf16 v[112:127], a[192:195], a[192:195], v[48:63]\n\t"
    "s_nop 0\n\t"
    "s_nop 0\n\t"
    "v_mfma_f32_32x32x16_bf16 a[48:63], a[192:195], a[192:195], a[112:127]\n\t"
    "s_waitcnt vmcnt(13) lgkmcnt(3)\n\t"
    "s_branch L_E_%=\n\t"
    ".p2align 6\n\t"
    "L_T_%=:\n\t"
    ".p2align 3\n\t"
    "v_mfma_f32_16x16x32_f16 v[0:3], a[128:131], a[208:211], v[0:3]\n\t"
    "ds_read_b128 a[240:243], v247 offset:0\n\t"
    "v_mfma_f32_16x16x32_f16 v[0:3], a[132:135], a[212:215], v[0:3]\n\t"
    "ds_read_b128 a[236:239], v245 offset:224\n\t"
    "v_mfma_f32_16x16x32_f16 v[0:3], a[136:139], a[216:219], v[0:3]\n\t"
    "global_load_dwordx4 a[192:195], v242, s[16:17] offset:-4096 sc1\n\t"
    "v_mfma_f32_16x16x32_f16 v[0:3], a[140:143], a[220:223], v[0:3]\n\t"
    "s_mov_b32 m0, s30\n\t"
    "v_pk_add_f16 v240, v128, v232\n\t"
    "v_mfma_f32_16x16x32_f16 v[8:11], a[140:143], a[224:227], v[8:11]\n\t"
    "v_pk_add_f16 v241, v132, v236\n\t"
    "v_pk_fma_f16 v252, v236, 0.5, v232 op_sel_hi:[1,0,1]\n\t"
    "v_mfma_f32_16x16x32_f16 v[8:11], a[136:139], a[220:223], v[8:11]\n\t"
    "v_pk_add_f16 v250, v240, v241\n\t"
    "v_pk_add_f16 v251, v240, v241 neg_lo:[0,1] neg_hi:[0,1]\n\t"
    "v_mfma_f32_16x16x32_f16 v[8:11], a[132:135], a[216:219], v[8:11]\n\t"
    "ds_write_addtid_b32 v250 offset:0\n\t"
    "s_nop 0\n\t"
    "v_mfma_f32_16x16x32_f16 v[8:11], a[128:131], a[212:215], v[8:11]\n\t"
    "s_waitcnt lgkmcnt(5)\n\t"
    "v_pk_fma_f16 v240, v232, 4.0, v128 op_sel_hi:[1,0,1]\n\t"
    "v_mfma_f32_16x16x32_f16 v[16:19], a[128:131], a[216:219], v[16:19]\n\t"
    "ds_write_addtid_b32 v251 offset:8960\n\t"
    "v_pk_fma_f16 v241, v236, 4.0, v132 op_sel_hi:[1,0,1]\n\t"
    "v_mfma_f32_16x16x32_f16 v[16:19], a[132:135], a[220:223], v[16:19]\n\t"
    "global_load_dwordx4 a[196:199], v242, s[16:17] offset:-3072 sc1\n\t"
    "s_nop 0\n\t"
    "v_mfma_f32_16x16x32_f16 v[16:19], a[136:139], a[224:227], v[16:19]\n\t"
    "v_pk_fma_f16 v252, v252, 0.5, v132 op_sel_hi:[1,0,1]\n\t"
    "v_pk_fma_f16 v250, v241, 2.0, v240 op_sel_hi:[1,0,1]\n\t"
    "v_mfma_f32_16x16x32_f16 v[16:19], a[140:143], a[228:231], v[16:19]\n\t"
    "v_pk_fma_f16 v251, v241, -2.0, v240 op_sel_hi:[1,0,1]\n\t"
    "v_mfma_f32_16x16x32_f16 v[24:27], a[140:143], a[232:235], v[24:27]\n\t"
    "ds_write_addtid_b32 v250 offset:17920\n\t"
    "v_pk_fma_f16 v252, v252, 0.5, v128 op_sel_hi:[1,0,1]\n\t"
    "v_mfma_f32_16x16x32_f16 v[24:27], a[136:139], a[228:231], v[24:27]\n\t"
    "s_waitcnt lgkmcnt(3)\n\t"
    "ds_write_addtid_b32 v251 offset:26880\n\t"
    "v_mfma_f32_16x16x32_f16 v[24:27], a[132:135], a[224:227], v[24:27]\n\t"
    "ds_write_addtid_b32 v252 offset:35840\n\t"
    "v_pk_add_f16 v240, v129, v233\n\t"
    "v_mfma_f32_16x16x32_f16 v[24:27], a[128:131], a[220:223], v[24:27]\n\t"
    "v_pk_add_f16 v241, v133, v237\n\t"
    "s_nop 0\n\t"
    "v_mfma_f32_16x16x32_f16 v[32:35], a[128:131], a[224:227], v[32:35]\n\t"
    "global_load_dwordx4 a[200:203], v242, s[16:17] offset:-2048 sc1\n\t"
    "v_mfma_f32_16x16x32_f16 v[32:35], a[132:135], a[228:231], v[32:35]\n\t"
    "v_pk_fma_f16 v252, v237, 0.5, v233 op_sel_hi:[1,0,1]\n\t"
    "v_pk_add_f16 v250, v240, v241\n\t"
    "v_mfma_f32_16x16x32_f16 v[32:35], a[136:139], a[232:235], v[32:35]\n\t"
    "v_pk_add_f16 v251, v240, v241 neg_lo:[0,1] neg_hi:[0,1]\n\t"
    "v_mfma_f32_16x16x32_f16 v[32:35], a[140:143], a[236:239], v[32:35]\n\t"
    "ds_write_addtid_b32 v250 offset:256\n\t"
    "v_pk_fma_f16 v240, v233, 4.0, v129 op_sel_hi:[1,0,1]\n\t"
    "v_mfma_f32_16x16x32_f16 v[40:43], a[140:143], a[240:243], v[40:43]\n\t"
    "ds_write_addtid_b32 v251 offset:9216\n\t"
    "v_pk_fma_f16 v241, v237, 4.0, v133 op_sel_hi:[1,0,1]\n\t"
    "v_mfma_f32_16x16x32_f16 v[40:43], a[136:139], a[236:239], v[40:43]\n\t"
    "v_pk_fma_f16 v252, v252, 0.5, v133 op_sel_hi:[1,0,1]\n\t"
    "v_pk_fma_f16 v250, v241, 2.0, v240 op_sel_hi:[1,0,1]\n\t"
    "v_mfma_f32_16x16x32_f16 v[40:43], a[132:135], a[232:235], v[40:43]\n\t"
    "v_pk_fma_f16 v251, v241, -2.0, v240 op_sel_hi:[1,0,1]\n\t"
    "v_mfma_f32_16x16x32_f16 v[40:43], a[128:131], a[228:231], v[40:43]\n\t"
    "global_load_dwordx4 a[204:207], v242, s[16:17] offset:-1024 sc1\n\t"
    "ds_write_addtid_b32 v250 offset:18176\n\t"
    "v_mfma_f32_16x16x32_f16 v[48:51], a[128:131], a[232:235], v[48:51]\n\t"
    "v_pk_fma_f16 v252, v252, 0.5, v129 op_sel_hi:[1,0,1]\n\t"
    "v_mfma_f32_16x16x32_f16 v[48:51], a[132:135], a[236:239], v[48:51]\n\t"
    "ds_write_addtid_b32 v251 offset:27136\n\t"
    "v_mfma_f32_16x16x32_f16 v[48:51], a[136:139], a[240:243], v[48:51]\n\t"
    "ds_write_addtid_b32 v252 offset:36096\n\t"
    "v_pk_add_f16 v240, v130, v234\n\t"
    "v_mfma_f32_16x16x32_f16 v[48:51], a[140:143], a[244:247], v[48:51]\n\t"
    "v_pk_add_f16 v241, v134, v238\n\t"
    "v_pk_fma_f16 v252, v238, 0.5, v234 op_sel_hi:[1,0,1]\n\t"
    "v_mfma_f32_16x16x32_f16 v[56:59], a[140:143], a[248:251], v[56:59]\n\t"
    "v_pk_add_f16 v250, v240, v241\n\t"
    "v_mfma_f32_16x16x32_f16 v[56:59], a[136:139], a[244:247], v[56:59]\n\t"
    "v_pk_add_f16 v251, v240, v241 neg_lo:[0,1] neg_hi:[0,1]\n\t"
    "v_mfma_f32_16x16x32_f16 v[56:59], a[132:135], a[240:243], v[56:59]\n\t"
    "ds_write_addtid_b32 v250 offset:512\n\t"
    "v_pk_fma_f16 v240, v234, 4.0, v130 op_sel_hi:[1,0,1]\n\t"
    "v_mfma_f32_16x16x32_f16 v[56:59], a[128:131], a[236:239], v[56:59]\n\t"
    "s_waitcnt vmcnt(14)\n\t"
    "ds_write_addtid_b32 v251 offset:9472\n\t"
    "v_mfma_f32_16x16x32_f16 v[4:7], a[144:147], a[208:211], v[4:7]\n\t"
    "ds_read_b128 a[208:211], v245 offset:8960\n\t"
    "v_pk_fma_f16 v241, v238, 4.0, v134 op_sel_hi:[1,0,1]\n\t"
    "v_mfma_f32_16x16x32_f16 v[4:7], a[148:151], a[212:215], v[4:7]\n\t"
    "v_pk_fma_f16 v252, v252, 0.5, v134 op_sel_hi:[1,0,1]\n\t"
    "v_pk_fma_f16 v250, v241, 2.0, v240 op_sel_hi:[1,0,1]\n\t"
    "v_mfma_f32_16x16x32_f16 v[4:7], a[152:155], a[216:219], v[4:7]\n\t"
    "global_load_dwordx4 a[128:131], v242, s[16:17] offset:0 sc1\n\t"
    "s_nop 0\n\t"
    "v_mfma_f32_16x16x32_f16 v[4:7], a[156:159], a[220:223], v[4:7]\n\t"
    "v_pk_fma_f16 v251, v241, -2.0, v240 op_sel_hi:[1,0,1]\n\t"
    "v_mfma_f32_16x16x32_f16 v[12:15], a[156:159], a[224:227], v[12:15]\n\t"
    "ds_write_addtid_b32 v250 offset:18432\n\t"
    "v_pk_fma_f16 v252, v252, 0.5, v130 op_sel_hi:[1,0,1]\n\t"
    "v_mfma_f32_16x16x32_f16 v[12:15], a[152:155], a[220:223], v[12:15]\n\t"
    "ds_write_addtid_b32 v251 offset:27392\n\t"
    "v_mfma_f32_16x16x32_f16 v[12:15], a[148:151], a[216:219], v[12:15]\n\t"
    "ds_write_addtid_b32 v252 offset:36352\n\t"
    "v_pk_add_f16 v240, v131, v235\n\t"
    "v_mfma_f32_16x16x32_f16 v[12:15], a[144:147], a[212:215], v[12:15]\n\t"
    "ds_read_b128 a[212:215], v245 offset:8992\n\t"
    "v_pk_add_f16 v241, v135, v239\n\t"
    "v_mfma_f32_16x16x32_f16 v[20:23], a[144:147], a[216:219], v[20:23]\n\t"
    "ds_read_b128 a[216:219], v245 offset:9024\n\t"
    "v_pk_fma_f16 v252, v239, 0.5, v235 op_sel_hi:[1,0,1]\n\t"
    "v_mfma_f32_16x16x32_f16 v[20:23], a[148:151], a[220:223], v[20:23]\n\t"
    "global_load_dwordx4 a[132:135], v242, s[16:17] offset:1024 sc1\n\t"
    "v_mfma_f32_16x16x32_f16 v[20:23], a[152:155], a[224:227], v[20:23]\n\t"
    "v_pk_add_f16 v250, v240, v241\n\t"
    "v_pk_add_f16 v251, v240, v241 neg_lo:[0,1] neg_hi:[0,1]\n\t"
    "v_mfma_f32_16x16x32_f16 v[20:23], a[156:159], a[228:231], v[20:23]\n\t"
    "ds_write_addtid_b32 v250 offset:768\n\t"
    "v_pk_fma_f16 v240, v235, 4.0, v131 op_sel_hi:[1,0,1]\n\t"
    "v_mfma_f32_16x16x32_f16 v[28:31], a[156:159], a[232:235], v[28:31]\n\t"
    "ds_write_addtid_b32 v251 offset:9728\n\t"
    "v_pk_fma_f16 v241, v239, 4.0, v135 op_sel_hi:[1,0,1]\n\t"
    "v_mfma_f32_16x16x32_f16 v[28:31], a[152:155], a[228:231], v[28:31]\n\t"
    "v_pk_fma_f16 v252, v252, 0.5, v135 op_sel_hi:[1,0,1]\n\t"
    "v_pk_fma_f16 v250, v241, 2.0, v240 op_sel_hi:[1,0,1]\n\t"
    "v_mfma_f32_16x16x32_f16 v[28:31], a[148:151], a[224:227], v[28:31]\n\t"
    "v_pk_fma_f16 v251, v241, -2.0, v240 op_sel_hi:[1,0,1]\n\t"
    "v_mfma_f32_16x16x32_f16 v[28:31], a[144:147], a[220:223], v[28:31]\n\t"
    "ds_read_b128 a[220:223], v245 offset:9056\n\t"
    "ds_write_addtid_b32 v250 offset:18688\n\t"
    "v_mfma_f32_16x16x32_f16 v[36:39], a[144:147], a[224:227], v[36:39]\n\t"
    "ds_read_b128 a[224:227], v245 offset:9088\n\t"
    "v_pk_fma_f16 v252, v252, 0.5, v131 op_sel_hi:[1,0,1]\n\t"
    "v_mfma_f32_16x16x32_f16 v[36:39], a[148:151], a[228:231], v[36:39]\n\t"
    "global_load_dwordx4 a[136:139], v242, s[16:17] offset:2048 sc1\n\t"
    "ds_write_addtid_b32 v251 offset:27648\n\t"
    "v_mfma_f32_16x16x32_f16 v[36:39], a[152:155], a[232:235], v[36:39]\n\t"
    "ds_write_addtid_b32 v252 offset:36608\n\t"
    "v_mfma_f32_16x16x32_f16 v[36:39], a[156:159], a[236:239], v[36:39]\n\t"
    "s_waitcnt vmcnt(21)\n\t"
    "v_pk_maximum3_f16 v240, v224, v224, s29 op_sel:[0,0,0] op_sel_hi:[1,1,0] neg_lo:[0,1,0] neg_hi:[0,1,0]\n\t"
    "v_mfma_f32_16x16x32_f16 v[44:47], a[156:159], a[240:243], v[44:47]\n\t"
    "v_pk_maximum3_f16 v241, v225, v225, s29 op_sel:[0,0,0] op_sel_hi:[1,1,0] neg_lo:[0,1,0] neg_hi:[0,1,0]\n\t"
    "v_pk_maximum3_f16 v250, v226, v226, s29 op_sel:[0,0,0] op_sel_hi:[1,1,0] neg_lo:[0,1,0] neg_hi:[0,1,0]\n\t"
    "v_mfma_f32_16x16x32_f16 v[44:47], a[152:155], a[236:239], v[44:47]\n\t"
    "v_pk_maximum3_f16 v251, v227, v227, s29 op_sel:[0,0,0] op_sel_hi:[1,1,0] neg_lo:[0,1,0] neg_hi:[0,1,0]\n\t"
    "v_pk_maximum3_f16 v249, v240, v241, v249\n\t"
    "v_mfma_f32_16x16x32_f16 v[44:47], a[148:151], a[232:235], v[44:47]\n\t"
    "v_pk_maximum3_f16 v249, v250, v251, v249\n\t"
    "v_pk_mad_u16 v240, v240, 8, s29 op_sel:[0,0,1] op_sel_hi:[1,0,1]\n\t"
    "v_mfma_f32_16x16x32_f16 v[44:47], a[144:147], a[228:231], v[44:47]\n\t"
    "ds_read_b128 a[228:231], v245 offset:9120\n\t"
    "v_pk_mad_u16 v241, v241, 8, s29 op_sel:[0,0,1] op_sel_hi:[1,0,1]\n\t"
    "v_mfma_f32_16x16x32_f16 v[52:55], a[144:147], a[232:235], v[52:55]\n\t"
    "ds_read_b128 a[232:235], v245 offset:9152\n\t"
    "v_pk_mad_u16 v250, v250, 8, s29 op_sel:[0,0,1] op_sel_hi:[1,0,1]\n\t"
    "v_mfma_f32_16x16x32_f16 v[52:55], a[148:151], a[236:239], v[52:55]\n\t"
    "global_load_dwordx4 a[140:143], v242, s[16:17] offset:3072 sc1\n\t"
    "s_nop 0\n\t"
    "v_mfma_f32_16x16x32_f16 v[52:55], a[152:155], a[240:243], v[52:55]\n\t"
    "v_add_u32 v242, 8192, v242\n\t"
    "v_pk_mad_u16 v251, v251, 8, s29 op_sel:[0,0,1] op_sel_hi:[1,0,1]\n\t"
    "v_mfma_f32_16x16x32_f16 v[52:55], a[156:159], a[244:247], v[52:55]\n\t"
    "v_bfi_b32 v128, s24, v240, v224\n\t"
    "v_bfi_b32 v129, s24, v241, v225\n\t"
    "v_mfma_f32_16x16x32_f16 v[60:63], a[156:159], a[248:251], v[60:63]\n\t"
    "ds_read_b128 a[248:251], v246 offset:9024\n\t"
    "v_bfi_b32 v130, s24, v250, v226\n\t"
    "v_mfma_f32_16x16x32_f16 v[60:63], a[152:155], a[244:247], v[60:63]\n\t"
    "ds_read_b128 a[244:247], v246 offset:8992\n\t"
    "v_bfi_b32 v131, s24, v251, v227\n\t"
    "v_mfma_f32_16x16x32_f16 v[60:63], a[148:151], a[240:243], v[60:63]\n\t"
    "ds_read_b128 a[240:243], v247 offset:8960\n\t"
    "s_waitcnt vmcnt(13)\n\t"
    "v_mfma_f32_16x16x32_f16 v[60:63], a[144:147], a[236:239], v[60:63]\n\t"
    "s_waitcnt vmcnt(14) lgkmcnt(7)\n\t"
    "v_pk_maximum3_f16 v240, v228, v228, s29 op_sel:[0,0,0] op_sel_hi:[1,1,0] neg_lo:[0,1,0] neg_hi:[0,1,0]\n\t"
    "v_mfma_f32_16x16x32_f16 v[64:67], a[160:163], a[208:211], v[64:67]\n\t"
    "ds_read_b128 a[236:239], v245 offset:9184\n\t"
    "v_pk_maximum3_f16 v241, v229, v229, s29 op_sel:[0,0,0] op_sel_hi:[1,1,0] neg_lo:[0,1,0] neg_hi:[0,1,0]\n\t"
    "v_mfma_f32_16x16x32_f16 v[64:67], a[164:167], a[212:215], v[64:67]\n\t"
    "v_pk_maximum3_f16 v250, v230, v230, s29 op_sel:[0,0,0] op_sel_hi:[1,1,0] neg_lo:[0,1,0] neg_hi:[0,1,0]\n\t"
    "v_pk_maximum3_f16 v251, v231, v231, s29 op_sel:[0,0,0] op_sel_hi:[1,1,0] neg_lo:[0,1,0] neg_hi:[0,1,0]\n\t"
    "v_mfma_f32_16x16x32_f16 v[64:67], a[168:171], a[216:219], v[64:67]\n\t"
    "global_load_dwordx4 a[144:147], v242, s[16:17] offset:-4096 sc1\n\t"
    "v_mfma_f32_16x16x32_f16 v[64:67], a[172:175], a[220:223], v[64:67]\n\t"
    "v_pk_maximum3_f16 v249, v240, v241, v249\n\t"
    "v_pk_maximum3_f16 v249, v250, v251, v249\n\t"
    "v_mfma_f32_16x16x32_f16 v[72:75], a[172:175], a[224:227], v[72:75]\n\t"
    "v_pk_mad_u16 v240, v240, 8, s29 op_sel:[0,0,1] op_sel_hi:[1,0,1]\n\t"
    "v_pk_mad_u16 v241, v241, 8, s29 op_sel:[0,0,1] op_sel_hi:[1,0,1]\n\t"
    "v_mfma_f32_16x16x32_f16 v[72:75], a[168:171], a[220:223], v[72:75]\n\t"
    "v_pk_mad_u16 v250, v250, 8, s29 op_sel:[0,0,1] op_sel_hi:[1,0,1]\n\t"
    "v_pk_mad_u16 v251, v251, 8, s29 op_sel:[0,0,1] op_sel_hi:[1,0,1]\n\t"
    "v_mfma_f32_16x16x32_f16 v[72:75], a[164:167], a[216:219], v[72:75]\n\t"
    "v_bfi_b32 v132, s24, v240, v228\n\t"
    "v_bfi_b32 v133, s24, v241, v229\n\t"
    "v_mfma_f32_16x16x32_f16 v[72:75], a[160:163], a[212:215], v[72:75]\n\t"
    "s_waitcnt lgkmcnt(4)\n\t"
    "v_bfi_b32 v134, s24, v250, v230\n\t"
    "v_mfma_f32_16x16x32_f16 v[80:83], a[160:163], a[216:219], v[80:83]\n\t"
    "v_bfi_b32 v135, s24, v251, v231\n\t"
    "s_nop 0\n\t"
    "v_mfma_f32_16x16x32_f16 v[80:83], a[164:167], a[220:223], v[80:83]\n\t"
    "global_load_dwordx4 a[148:151], v242, s[16:17] offset:-3072 sc1\n\t"
    "v_mfma_f32_16x16x32_f16 v[80:83], a[168:171], a[224:227], v[80:83]\n\t"
    "buffer_load_dwordx4 v[232:235], v244, s[8:11], s25 offen offset:2048\n\t"
    "v_mfma_f32_16x16x32_f16 v[80:83], a[172:175], a[228:231], v[80:83]\n\t"
    "v_mfma_f32_16x16x32_f16 v[88:91], a[172:175], a[232:235], v[88:91]\n\t"
    "v_mfma_f32_16x16x32_f16 v[88:91], a[168:171], a[228:231], v[88:91]\n\t"
    "s_waitcnt lgkmcnt(0)\n\t"
    "s_nop 0\n\t"
    "v_mfma_f32_16x16x32_f16 v[88:91], a[164:167], a[224:227], v[88:91]\n\t"
    "v_mfma_f32_16x16x32_f16 v[88:91], a[160:163], a[220:223], v[88:91]\n\t"
    "buffer_load_dwordx4 v[224:227], v244, s[8:11], s25 offen offset:2112\n\t"
    "v_mfma_f32_16x16x32_f16 v[96:99], a[160:163], a[224:227], v[96:99]\n\t"
    "global_load_dwordx4 a[152:155], v242, s[16:17] offset:-2048 sc1\n\t"
    "v_mfma_f32_16x16x32_f16 v[96:99], a[164:167], a[228:231], v[96:99]\n\t"
    "v_mfma_f32_16x16x32_f16 v[96:99], a[168:171], a[232:235], v[96:99]\n\t"
    "v_mfma_f32_16x16x32_f16 v[96:99], a[172:175], a[236:239], v[96:99]\n\t"
    "v_mfma_f32_16x16x32_f16 v[104:107], a[172:175], a[240:243], v[104:107]\n\t"
    "v_mfma_f32_16x16x32_f16 v[104:107], a[168:171], a[236:239], v[104:107]\n\t"
    "v_mfma_f32_16x16x32_f16 v[104:107], a[164:167], a[232:235], v[104:107]\n\t"
    "v_mfma_f32_16x16x32_f16 v[104:107], a[160:163], a[228:231], v[104:107]\n\t"
    "global_load_dwordx4 a[156:159], v242, s[16:17] offset:-1024 sc1\n\t"
    "v_mfma_f32_16x16x32_f16 v[112:115], a[160:163], a[232:235], v[112:115]\n\t"
    "v_mfma_f32_16x16x32_f16 v[112:115], a[164:167], a[236:239], v[112:115]\n\t"
    "v_mfma_f32_16x16x32_f16 v[112:115], a[168:171], a[240:243], v[112:115]\n\t"
    "v_mfma_f32_16x16x32_f16 v[112:115], a[172:175], a[244:247], v[112:115]\n\t"
    "v_mfma_f32_16x16x32_f16 v[120:123], a[172:175], a[248:251], v[120:123]\n\t"
    "v_mfma_f32_16x16x32_f16 v[120:123], a[168:171], a[244:247], v[120:123]\n\t"
    "v_mfma_f32_16x16x32_f16 v[120:123], a[164:167], a[240:243], v[120:123]\n\t"
    "v_mfma_f32_16x16x32_f16 v[120:123], a[160:163], a[236:239], v[120:123]\n\t"
    "s_waitcnt vmcnt(14)\n\t"
    "s_nop 0\n\t"
    "v_mfma_f32_16x16x32_f16 v[68:71], a[176:179], a[208:211], v[68:71]\n\t"
    "ds_read_b128 a[208:211], v245 offset:17920\n\t"
    "v_mfma_f32_16x16x32_f16 v[68:71], a[180:183], a[212:215], v[68:71]\n\t"
    "v_mfma_f32_16x16x32_f16 v[68:71], a[184:187], a[216:219], v[68:71]\n\t"
    "global_load_dwordx4 a[160:163], v242, s[16:17] offset:0 sc1\n\t"
    "v_mfma_f32_16x16x32_f16 v[68:71], a[188:191], a[220:223], v[68:71]\n\t"
    "v_mfma_f32_16x16x32_f16 v[76:79], a[188:191], a[224:227], v[76:79]\n\t"
    "v_mfma_f32_16x16x32_f16 v[76:79], a[184:187], a[220:223], v[76:79]\n\t"
    "v_mfma_f32_16x16x32_f16 v[76:79], a[180:183], a[216:219], v[76:79]\n\t"
    "v_mfma_f32_16x16x32_f16 v[76:79], a[176:179], a[212:215], v[76:79]\n\t"
    "ds_read_b128 a[212:215], v245 offset:17952\n\t"
    "v_mfma_f32_16x16x32_f16 v[84:87], a[176:179], a[216:219], v[84:87]\n\t"
    "ds_read_b128 a[216:219], v245 offset:17984\n\t"
    "v_mfma_f32_16x16x32_f16 v[84:87], a[180:183], a[220:223], v[84:87]\n\t"
    "global_load_dwordx4 a[164:167], v242, s[16:17] offset:1024 sc1\n\t"
    "v_mfma_f32_16x16x32_f16 v[84:87], a[184:187], a[224:227], v[84:87]\n\t"
    "v_mfma_f32_16x16x32_f16 v[84:87], a[188:191], a[228:231], v[84:87]\n\t"
    "v_mfma_f32_16x16x32_f16 v[92:95], a[188:191], a[232:235], v[92:95]\n\t"
    "v_mfma_f32_16x16x32_f16 v[92:95], a[184:187], a[228:231], v[92:95]\n\t"
    "v_mfma_f32_16x16x32_f16 v[92:95], a[180:183], a[224:227], v[92:95]\n\t"
    "v_mfma_f32_16x16x32_f16 v[92:95], a[176:179], a[220:223], v[92:95]\n\t"
    "ds_read_b128 a[220:223], v245 offset:18016\n\t"
    "v_mfma_f32_16x16x32_f16 v[100:103], a[176:179], a[224:227], v[100:103]\n\t"
    "ds_read_b128 a[224:227], v245 offset:18048\n\t"
    "v_mfma_f32_16x16x32_f16 v[100:103], a[180:183], a[228:231], v[100:103]\n\t"
    "global_load_dwordx4 a[168:171], v242, s[16:17] offset:2048 sc1\n\t"
    "v_mfma_f32_16x16x32_f16 v[100:103], a[184:187], a[232:235], v[100:103]\n\t"
    "v_mfma_f32_16x16x32_f16 v[100:103], a[188:191], a[236:239], v[100:103]\n\t"
    "v_mfma_f32_16x16x32_f16 v[108:111], a[188:191], a[240:243], v[108:111]\n\t"
    "v_mfma_f32_16x16x32_f16 v[108:111], a[184:187], a[236:239], v[108:111]\n\t"
    "v_mfma_f32_16x16x32_f16 v[108:111], a[180:183], a[232:235], v[108:111]\n\t"
    "v_mfma_f32_16x16x32_f16 v[108:111], a[176:179], a[228:231], v[108:111]\n\t"
    "ds_read_b128 a[228:231], v245 offset:18080\n\t"
    "v_mfma_f32_16x16x32_f16 v[116:119], a[176:179], a[232:235], v[116:119]\n\t"
    "ds_read_b128 a[232:235], v245 offset:18112\n\t"
    "v_mfma_f32_16x16x32_f16 v[116:119], a[180:183], a[236:239], v[116:119]\n\t"
    "global_load_dwordx4 a[172:175], v242, s[16:17] offset:3072 sc1\n\t"
    "v_mfma_f32_16x16x32_f16 v[116:119], a[184:187], a[240:243], v[116:119]\n\t"
    "v_add_u32 v242, 8192, v242\n\t"
    "v_mfma_f32_16x16x32_f16 v[116:119], a[188:191], a[244:247], v[116:119]\n\t"
    "v_mfma_f32_16x16x32_f16 v[124:127], a[188:191], a[248:251], v[124:127]\n\t"
    "ds_read_b128 a[248:251], v246 offset:17984\n\t"
    "v_mfma_f32_16x16x32_f16 v[124:127], a[184:187], a[244:247], v[124:127]\n\t"
    "ds_read_b128 a[244:247], v246 offset:17952\n\t"
    "v_mfma_f32_16x16x32_f16 v[124:127], a[180:183], a[240:243], v[124:127]\n\t"
    "ds_read_b128 a[240:243], v247 offset:17920\n\t"
    "v_mfma_f32_16x16x32_f16 v[124:127], a[176:179], a[236:239], v[124:127]\n\t"
    "s_waitcnt vmcnt(14) lgkmcnt(5)\n\t"
    "s_nop 0\n\t"
    "v_mfma_f32_16x16x32_f16 a[0:3], a[192:195], a[208:211], a[0:3]\n\t"
    "ds_read_b128 a[236:239], v245 offset:18144\n\t"
    "v_mfma_f32_16x16x32_f16 a[0:3], a[196:199], a[212:215], a[0:3]\n\t"
    "buffer_load_dwordx4 v[236:239], v244, s[8:11], s25 offen offset:3072\n\t"
    "v_mfma_f32_16x16x32_f16 a[0:3], a[200:203], a[216:219], a[0:3]\n\t"
    "global_load_dwordx4 a[176:179], v242, s[16:17] offset:-4096 sc1\n\t"
    "v_mfma_f32_16x16x32_f16 a[0:3], a[204:207], a[220:223], a[0:3]\n\t"
    "v_mfma_f32_16x16x32_f16 a[8:11], a[204:207], a[224:227], a[8:11]\n\t"
    "v_mfma_f32_16x16x32_f16 a[8:11], a[200:203], a[220:223], a[8:11]\n\t"
    "v_mfma_f32_16x16x32_f16 a[8:11], a[196:199], a[216:219], a[8:11]\n\t"
    "buffer_load_dwordx4 v[228:231], v244, s[8:11], s25 offen offset:3136\n\t"
    "v_mfma_f32_16x16x32_f16 a[8:11], a[192:195], a[212:215], a[8:11]\n\t"
    "s_waitcnt lgkmcnt(4)\n\t"
    "s_add_u32 s25, s25, 128\n\t"
    "v_mfma_f32_16x16x32_f16 a[16:19], a[192:195], a[216:219], a[16:19]\n\t"
    "s_nop 0\n\t"
    "v_mfma_f32_16x16x32_f16 a[16:19], a[196:199], a[220:223], a[16:19]\n\t"
    "global_load_dwordx4 a[180:183], v242, s[16:17] offset:-3072 sc1\n\t"
    "v_mfma_f32_16x16x32_f16 a[16:19], a[200:203], a[224:227], a[16:19]\n\t"
    "v_mfma_f32_16x16x32_f16 a[16:19], a[204:207], a[228:231], a[16:19]\n\t"
    "v_mfma_f32_16x16x32_f16 a[24:27], a[204:207], a[232:235], a[24:27]\n\t"
    "v_mfma_f32_16x16x32_f16 a[24:27], a[200:203], a[228:231], a[24:27]\n\t"
    "s_waitcnt lgkmcnt(0)\n\t"
    "s_nop 0\n\t"
    "v_mfma_f32_16x16x32_f16 a[24:27], a[196:199], a[224:227], a[24:27]\n\t"
    "v_mfma_f32_16x16x32_f16 a[24:27], a[192:195], a[220:223], a[24:27]\n\t"
    "v_mfma_f32_16x16x32_f16 a[32:35], a[192:195], a[224:227], a[32:35]\n\t"
    "global_load_dwordx4 a[184:187], v242, s[16:17] offset:-2048 sc1\n\t"
    "v_mfma_f32_16x16x32_f16 a[32:35], a[196:199], a[228:231], a[32:35]\n\t"
    "v_mfma_f32_16x16x32_f16 a[32:35], a[200:203], a[232:235], a[32:35]\n\t"
    "v_mfma_f32_16x16x32_f16 a[32:35], a[204:207], a[236:239], a[32:35]\n\t"
    "v_mfma_f32_16x16x32_f16 a[40:43], a[204:207], a[240:243], a[40:43]\n\t"
    "v_mfma_f32_16x16x32_f16 a[40:43], a[200:203], a[236:239], a[40:43]\n\t"
    "v_mfma_f32_16x16x32_f16 a[40:43], a[196:199], a[232:235], a[40:43]\n\t"
    "v_mfma_f32_16x16x32_f16 a[40:43], a[192:195], a[228:231], a[40:43]\n\t"
    "global_load_dwordx4 a[188:191], v242, s[16:17] offset:-1024 sc1\n\t"
    "v_mfma_f32_16x16x32_f16 a[48:51], a[192:195], a[232:235], a[48:51]\n\t"
    "v_mfma_f32_16x16x32_f16 a[48:51], a[196:199], a[236:239], a[48:51]\n\t"
    "v_mfma_f32_16x16x32_f16 a[48:51], a[200:203], a[240:243], a[48:51]\n\t"
    "v_mfma_f32_16x16x32_f16 a[48:51], a[204:207], a[244:247], a[48:51]\n\t"
    "v_mfma_f32_16x16x32_f16 a[56:59], a[204:207], a[248:251], a[56:59]\n\t"
    "v_mfma_f32_16x16x32_f16 a[56:59], a[200:203], a[244:247], a[56:59]\n\t"
    "v_mfma_f32_16x16x32_f16 a[56:59], a[196:199], a[240:243], a[56:59]\n\t"
    "v_mfma_f32_16x16x32_f16 a[56:59], a[192:195], a[236:239], a[56:59]\n\t"
    "s_waitcnt vmcnt(16)\n\t"
    "s_nop 0\n\t"
    "v_mfma_f32_16x16x32_f16 a[4:7], a[128:131], a[208:211], a[4:7]\n\t"
    "ds_read_b128 a[208:211], v245 offset:26880\n\t"
    "v_mfma_f32_16x16x32_f16 a[4:7], a[132:135], a[212:215], a[4:7]\n\t"
    "v_mfma_f32_16x16x32_f16 a[4:7], a[136:139], a[216:219], a[4:7]\n\t"
    "global_load_dwordx4 a[192:195], v242, s[16:17] offset:0 sc1\n\t"
    "v_mfma_f32_16x16x32_f16 a[4:7], a[140:143], a[220:223], a[4:7]\n\t"
    "v_mfma_f32_16x16x32_f16 a[12:15], a[140:143], a[224:227], a[12:15]\n\t"
    "v_mfma_f32_16x16x32_f16 a[12:15], a[136:139], a[220:223], a[12:15]\n\t"
    "v_mfma_f32_16x16x32_f16 a[12:15], a[132:135], a[216:219], a[12:15]\n\t"
    "v_mfma_f32_16x16x32_f16 a[12:15], a[128:131], a[212:215], a[12:15]\n\t"
    "ds_read_b128 a[212:215], v245 offset:26912\n\t"
    "v_mfma_f32_16x16x32_f16 a[20:23], a[128:131], a[216:219], a[20:23]\n\t"
    "ds_read_b128 a[216:219], v245 offset:26944\n\t"
    "v_mfma_f32_16x16x32_f16 a[20:23], a[132:135], a[220:223], a[20:23]\n\t"
    "global_load_dwordx4 a[196:199], v242, s[16:17] offset:1024 sc1\n\t"
    "v_mfma_f32_16x16x32_f16 a[20:23], a[136:139], a[224:227], a[20:23]\n\t"
    "v_mfma_f32_16x16x32_f16 a[20:23], a[140:143], a[228:231], a[20:23]\n\t"
    "v_mfma_f32_16x16x32_f16 a[28:31], a[140:143], a[232:235], a[28:31]\n\t"
    "v_mfma_f32_16x16x32_f16 a[28:31], a[136:139], a[228:231], a[28:31]\n\t"
    "v_mfma_f32_16x16x32_f16 a[28:31], a[132:135], a[224:227], a[28:31]\n\t"
    "v_mfma_f32_16x16x32_f16 a[28:31], a[128:131], a[220:223], a[28:31]\n\t"
    "ds_read_b128 a[220:223], v245 offset:26976\n\t"
    "v_mfma_f32_16x16x32_f16 a[36:39], a[128:131], a[224:227], a[36:39]\n\t"
    "ds_read_b128 a[224:227], v245 offset:27008\n\t"
    "v_mfma_f32_16x16x32_f16 a[36:39], a[132:135], a[228:231], a[36:39]\n\t"
    "global_load_dwordx4 a[200:203], v242, s[16:17] offset:2048 sc1\n\t"
    "v_mfma_f32_16x16x32_f16 a[36:39], a[136:139], a[232:235], a[36:39]\n\t"
    "v_mfma_f32_16x16x32_f16 a[36:39], a[140:143], a[236:239], a[36:39]\n\t"
    "v_mfma_f32_16x16x32_f16 a[44:47], a[140:143], a[240:243], a[44:47]\n\t"
    "v_mfma_f32_16x16x32_f16 a[44:47], a[136:139], a[236:239], a[44:47]\n\t"
    "v_mfma_f32_16x16x32_f16 a[44:47], a[132:135], a[232:235], a[44:47]\n\t"
    "v_mfma_f32_16x16x32_f16 a[44:47], a[128:131], a[228:231], a[44:47]\n\t"
    "ds_read_b128 a[228:231], v245 offset:27040\n\t"
    "v_mfma_f32_16x16x32_f16 a[52:55], a[128:131], a[232:235], a[52:55]\n\t"
    "ds_read_b128 a[232:235], v245 offset:27072\n\t"
    "v_mfma_f32_16x16x32_f16 a[52:55], a[132:135], a[236:239], a[52:55]\n\t"
    "global_load_dwordx4 a[204:207], v242, s[16:17] offset:3072 sc1\n\t"
    "v_mfma_f32_16x16x32_f16 a[52:55], a[136:139], a[240:243], a[52:55]\n\t"
    "v_add_u32 v242, 8192, v242\n\t"
    "v_mfma_f32_16x16x32_f16 a[52:55], a[140:143], a[244:247], a[52:55]\n\t"
    "v_mfma_f32_16x16x32_f16 a[60:63], a[140:143], a[248:251], a[60:63]\n\t"
    "ds_read_b128 a[248:251], v246 offset:26944\n\t"
    "v_mfma_f32_16x16x32_f16 a[60:63], a[136:139], a[244:247], a[60:63]\n\t"
    "ds_read_b128 a[244:247], v246 offset:26912\n\t"
    "v_mfma_f32_16x16x32_f16 a[60:63], a[132:135], a[240:243], a[60:63]\n\t"
    "ds_read_b128 a[240:243], v247 offset:26880\n\t"
    "v_mfma_f32_16x16x32_f16 a[60:63], a[128:131], a[236:239], a[60:63]\n\t"
    "s_waitcnt vmcnt(14) lgkmcnt(5)\n\t"
    "s_nop 0\n\t"
    "v_mfma_f32_16x16x32_f16 a[64:67], a[144:147], a[208:211], a[64:67]\n\t"
    "ds_read_b128 a[236:239], v245 offset:27104\n\t"
    "v_mfma_f32_16x16x32_f16 a[64:67], a[148:151], a[212:215], a[64:67]\n\t"
    "v_mfma_f32_16x16x32_f16 a[64:67], a[152:155], a[216:219], a[64:67]\n\t"
    "global_load_dwordx4 a[128:131], v242, s[16:17] offset:-4096 sc1\n\t"
    "v_mfma_f32_16x16x32_f16 a[64:67], a[156:159], a[220:223], a[64:67]\n\t"
    "v_mfma_f32_16x16x32_f16 a[72:75], a[156:159], a[224:227], a[72:75]\n\t"
    "v_mfma_f32_16x16x32_f16 a[72:75], a[152:155], a[220:223], a[72:75]\n\t"
    "v_mfma_f32_16x16x32_f16 a[72:75], a[148:151], a[216:219], a[72:75]\n\t"
    "v_mfma_f32_16x16x32_f16 a[72:75], a[144:147], a[212:215], a[72:75]\n\t"
    "s_waitcnt lgkmcnt(4)\n\t"
    "s_nop 0\n\t"
    "v_mfma_f32_16x16x32_f16 a[80:83], a[144:147], a[216:219], a[80:83]\n\t"
    "v_mfma_f32_16x16x32_f16 a[80:83], a[148:151], a[220:223], a[80:83]\n\t"
    "global_load_dwordx4 a[132:135], v242, s[16:17] offset:-3072 sc1\n\t"
    "v_mfma_f32_16x16x32_f16 a[80:83], a[152:155], a[224:227], a[80:83]\n\t"
    "v_mfma_f32_16x16x32_f16 a[80:83], a[156:159], a[228:231], a[80:83]\n\t"
    "v_mfma_f32_16x16x32_f16 a[88:91], a[156:159], a[232:235], a[88:91]\n\t"
    "v_mfma_f32_16x16x32_f16 a[88:91], a[152:155], a[228:231], a[88:91]\n\t"
    "s_waitcnt lgkmcnt(0)\n\t"
    "s_nop 0\n\t"
    "v_mfma_f32_16x16x32_f16 a[88:91], a[148:151], a[224:227], a[88:91]\n\t"
    "v_mfma_f32_16x16x32_f16 a[88:91], a[144:147], a[220:223], a[88:91]\n\t"
    "v_mfma_f32_16x16x32_f16 a[96:99], a[144:147], a[224:227], a[96:99]\n\t"
    "global_load_dwordx4 a[136:139], v242, s[16:17] offset:-2048 sc1\n\t"
    "v_mfma_f32_16x16x32_f16 a[96:99], a[148:151], a[228:231], a[96:99]\n\t"
    "v_mfma_f32_16x16x32_f16 a[96:99], a[152:155], a[232:235], a[96:99]\n\t"
    "v_mfma_f32_16x16x32_f16 a[96:99], a[156:159], a[236:239], a[96:99]\n\t"
    "v_mfma_f32_16x16x32_f16 a[104:107], a[156:159], a[240:243], a[104:107]\n\t"
    "v_mfma_f32_16x16x32_f16 a[104:107], a[152:155], a[236:239], a[104:107]\n\t"
    "v_mfma_f32_16x16x32_f16 a[104:107], a[148:151], a[232:235], a[104:107]\n\t"
    "v_mfma_f32_16x16x32_f16 a[104:107], a[144:147], a[228:231], a[104:107]\n\t"
    "global_load_dwordx4 a[140:143], v242, s[16:17] offset:-1024 sc1\n\t"
    "v_mfma_f32_16x16x32_f16 a[112:115], a[144:147], a[232:235], a[112:115]\n\t"
    "s_waitcnt vmcnt(21)\n\t"
    "v_pk_maximum3_f16 v240, v232, v232, s29 op_sel:[0,0,0] op_sel_hi:[1,1,0] neg_lo:[0,1,0] neg_hi:[0,1,0]\n\t"
    "v_mfma_f32_16x16x32_f16 a[112:115], a[148:151], a[236:239], a[112:115]\n\t"
    "v_pk_maximum3_f16 v241, v233, v233, s29 op_sel:[0,0,0] op_sel_hi:[1,1,0] neg_lo:[0,1,0] neg_hi:[0,1,0]\n\t"
    "v_pk_maximum3_f16 v250, v234, v234, s29 op_sel:[0,0,0] op_sel_hi:[1,1,0] neg_lo:[0,1,0] neg_hi:[0,1,0]\n\t"
    "v_mfma_f32_16x16x32_f16 a[112:115], a[152:155], a[240:243], a[112:115]\n\t"
    "v_pk_maximum3_f16 v251, v235, v235, s29 op_sel:[0,0,0] op_sel_hi:[1,1,0] neg_lo:[0,1,0] neg_hi:[0,1,0]\n\t"
    "s_nop 0\n\t"
    "v_mfma_f32_16x16x32_f16 a[112:115], a[156:159], a[244:247], a[112:115]\n\t"
    "v_pk_maximum3_f16 v249, v240, v241, v249\n\t"
    "v_pk_maximum3_f16 v249, v250, v251, v249\n\t"
    "v_mfma_f32_16x16x32_f16 a[120:123], a[156:159], a[248:251], a[120:123]\n\t"
    "v_pk_mad_u16 v240, v240, 8, s29 op_sel:[0,0,1] op_sel_hi:[1,0,1]\n\t"
    "v_pk_mad_u16 v241, v241, 8, s29 op_sel:[0,0,1] op_sel_hi:[1,0,1]\n\t"
    "v_mfma_f32_16x16x32_f16 a[120:123], a[152:155], a[244:247], a[120:123]\n\t"
    "v_pk_mad_u16 v250, v250, 8, s29 op_sel:[0,0,1] op_sel_hi:[1,0,1]\n\t"
    "v_mfma_f32_16x16x32_f16 a[120:123], a[148:151], a[240:243], a[120:123]\n\t"
    "v_pk_mad_u16 v251, v251, 8, s29 op_sel:[0,0,1] op_sel_hi:[1,0,1]\n\t"
    "v_bfi_b32 v232, s24, v240, v232\n\t"
    "v_mfma_f32_16x16x32_f16 a[120:123], a[144:147], a[236:239], a[120:123]\n\t"
    "s_waitcnt vmcnt(14)\n\t"
    "v_bfi_b32 v233, s24, v241, v233\n\t"
    "v_mfma_f32_16x16x32_f16 a[68:71], a[160:163], a[208:211], a[68:71]\n\t"
    "ds_read_b128 a[208:211], v245 offset:35840\n\t"
    "v_bfi_b32 v234, s24, v250, v234\n\t"
    "v_mfma_f32_16x16x32_f16 a[68:71], a[164:167], a[212:215], a[68:71]\n\t"
    "v_bfi_b32 v235, s24, v251, v235\n\t"
    "s_waitcnt vmcnt(13)\n\t"
    "v_mfma_f32_16x16x32_f16 a[68:71], a[168:171], a[216:219], a[68:71]\n\t"
    "global_load_dwordx4 a[144:147], v242, s[16:17] offset:0 sc1\n\t"
    "v_mfma_f32_16x16x32_f16 a[68:71], a[172:175], a[220:223], a[68:71]\n\t"
    "v_pk_maximum3_f16 v240, v236, v236, s29 op_sel:[0,0,0] op_sel_hi:[1,1,0] neg_lo:[0,1,0] neg_hi:[0,1,0]\n\t"
    "v_pk_maximum3_f16 v241, v237, v237, s29 op_sel:[0,0,0] op_sel_hi:[1,1,0] neg_lo:[0,1,0] neg_hi:[0,1,0]\n\t"
    "v_mfma_f32_16x16x32_f16 a[76:79], a[172:175], a[224:227], a[76:79]\n\t"
    "v_pk_maximum3_f16 v250, v238, v238, s29 op_sel:[0,0,0] op_sel_hi:[1,1,0] neg_lo:[0,1,0] neg_hi:[0,1,0]\n\t"
    "v_pk_maximum3_f16 v251, v239, v239, s29 op_sel:[0,0,0] op_sel_hi:[1,1,0] neg_lo:[0,1,0] neg_hi:[0,1,0]\n\t"
    "v_mfma_f32_16x16x32_f16 a[76:79], a[168:171], a[220:223], a[76:79]\n\t"
    "v_pk_maximum3_f16 v249, v240, v241, v249\n\t"
    "v_pk_maximum3_f16 v249, v250, v251, v249\n\t"
    "v_mfma_f32_16x16x32_f16 a[76:79], a[164:167], a[216:219], a[76:79]\n\t"
    "v_pk_mad_u16 v240, v240, 8, s29 op_sel:[0,0,1] op_sel_hi:[1,0,1]\n\t"
    "v_pk_mad_u16 v241, v241, 8, s29 op_sel:[0,0,1] op_sel_hi:[1,0,1]\n\t"
    "v_mfma_f32_16x16x32_f16 a[76:79], a[160:163], a[212:215], a[76:79]\n\t"
    "ds_read_b128 a[212:215], v245 offset:35872\n\t"
    "v_pk_mad_u16 v250, v250, 8, s29 op_sel:[0,0,1] op_sel_hi:[1,0,1]\n\t"
    "v_mfma_f32_16x16x32_f16 a[84:87], a[160:163], a[216:219], a[84:87]\n\t"
    "ds_read_b128 a[216:219], v245 offset:35904\n\t"
    "v_pk_mad_u16 v251, v251, 8, s29 op_sel:[0,0,1] op_sel_hi:[1,0,1]\n\t"
    "v_mfma_f32_16x16x32_f16 a[84:87], a[164:167], a[220:223], a[84:87]\n\t"
    "global_load_dwordx4 a[148:151], v242, s[16:17] offset:1024 sc1\n\t"
    "v_mfma_f32_16x16x32_f16 a[84:87], a[168:171], a[224:227], a[84:87]\n\t"
    "v_bfi_b32 v236, s24, v240, v236\n\t"
    "v_bfi_b32 v237, s24, v241, v237\n\t"
    "v_mfma_f32_16x16x32_f16 a[84:87], a[172:175], a[228:231], a[84:87]\n\t"
    "v_bfi_b32 v238, s24, v250, v238\n\t"
    "v_bfi_b32 v239, s24, v251, v239\n\t"
    "v_mfma_f32_16x16x32_f16 a[92:95], a[172:175], a[232:235], a[92:95]\n\t"
    "s_add_u32 m0, s30, 1056\n\t"
    "v_pk_add_f16 v240, v128, v232\n\t"
    "v_mfma_f32_16x16x32_f16 a[92:95], a[168:171], a[228:231], a[92:95]\n\t"
    "v_pk_add_f16 v241, v132, v236\n\t"
    "v_pk_fma_f16 v252, v236, 0.5, v232 op_sel_hi:[1,0,1]\n\t"
    "v_mfma_f32_16x16x32_f16 a[92:95], a[164:167], a[224:227], a[92:95]\n\t"
    "v_pk_add_f16 v250, v240, v241\n\t"
    "v_pk_add_f16 v251, v240, v241 neg_lo:[0,1] neg_hi:[0,1]\n\t"
    "v_mfma_f32_16x16x32_f16 a[92:95], a[160:163], a[220:223], a[92:95]\n\t"
    "ds_read_b128 a[220:223], v245 offset:35936\n\t"
    "ds_write_addtid_b32 v250 offset:0\n\t"
    "v_mfma_f32_16x16x32_f16 a[100:103], a[160:163], a[224:227], a[100:103]\n\t"
    "ds_read_b128 a[224:227], v245 offset:35968\n\t"
    "v_pk_fma_f16 v240, v232, 4.0, v128 op_sel_hi:[1,0,1]\n\t"
    "v_mfma_f32_16x16x32_f16 a[100:103], a[164:167], a[228:231], a[100:103]\n\t"
    "global_load_dwordx4 a[152:155], v242, s[16:17] offset:2048 sc1\n\t"
    "ds_write_addtid_b32 v251 offset:8960\n\t"
    "v_mfma_f32_16x16x32_f16 a[100:103], a[168:171], a[232:235], a[100:103]\n\t"
    "v_pk_fma_f16 v241, v236, 4.0, v132 op_sel_hi:[1,0,1]\n\t"
    "v_pk_fma_f16 v252, v252, 0.5, v132 op_sel_hi:[1,0,1]\n\t"
    "v_mfma_f32_16x16x32_f16 a[100:103], a[172:175], a[236:239], a[100:103]\n\t"
    "v_pk_fma_f16 v250, v241, 2.0, v240 op_sel_hi:[1,0,1]\n\t"
    "v_pk_fma_f16 v251, v241, -2.0, v240 op_sel_hi:[1,0,1]\n\t"
    "v_mfma_f32_16x16x32_f16 a[108:111], a[172:175], a[240:243], a[108:111]\n\t"
    "ds_write_addtid_b32 v250 offset:17920\n\t"
    "v_pk_fma_f16 v252, v252, 0.5, v128 op_sel_hi:[1,0,1]\n\t"
    "v_mfma_f32_16x16x32_f16 a[108:111], a[168:171], a[236:239], a[108:111]\n\t"
    "ds_write_addtid_b32 v251 offset:26880\n\t"
    "v_mfma_f32_16x16x32_f16 a[108:111], a[164:167], a[232:235], a[108:111]\n\t"
    "ds_write_addtid_b32 v252 offset:35840\n\t"
    "v_pk_add_f16 v240, v129, v233\n\t"
    "v_mfma_f32_16x16x32_f16 a[108:111], a[160:163], a[228:231], a[108:111]\n\t"
    "ds_read_b128 a[228:231], v245 offset:36000\n\t"
    "v_pk_add_f16 v241, v133, v237\n\t"
    "v_mfma_f32_16x16x32_f16 a[116:119], a[160:163], a[232:235], a[116:119]\n\t"
    "ds_read_b128 a[232:235], v245 offset:36032\n\t"
    "v_pk_fma_f16 v252, v237, 0.5, v233 op_sel_hi:[1,0,1]\n\t"
    "v_mfma_f32_16x16x32_f16 a[116:119], a[164:167], a[236:239], a[116:119]\n\t"
    "global_load_dwordx4 a[156:159], v242, s[16:17] offset:3072 sc1\n\t"
    "v_mfma_f32_16x16x32_f16 a[116:119], a[168:171], a[240:243], a[116:119]\n\t"
    "v_add_u32 v242, 8192, v242\n\t"
    "v_pk_add_f16 v250, v240, v241\n\t"
    "v_mfma_f32_16x16x32_f16 a[116:119], a[172:175], a[244:247], a[116:119]\n\t"
    "v_pk_add_f16 v251, v240, v241 neg_lo:[0,1] neg_hi:[0,1]\n\t"
    "v_mfma_f32_16x16x32_f16 a[124:127], a[172:175], a[248:251], a[124:127]\n\t"
    "ds_read_b128 a[248:251], v246 offset:35904\n\t"
    "ds_write_addtid_b32 v250 offset:256\n\t"
    "v_mfma_f32_16x16x32_f16 a[124:127], a[168:171], a[244:247], a[124:127]\n\t"
    "ds_read_b128 a[244:247], v246 offset:35872\n\t"
    "v_pk_fma_f16 v240, v233, 4.0, v129 op_sel_hi:[1,0,1]\n\t"
    "v_mfma_f32_16x16x32_f16 a[124:127], a[164:167], a[240:243], a[124:127]\n\t"
    "ds_read_b128 a[240:243], v247 offset:35840\n\t"
    "ds_write_addtid_b32 v251 offset:9216\n\t"
    "v_mfma_f32_16x16x32_f16 a[124:127], a[160:163], a[236:239], a[124:127]\n\t"
    "s_waitcnt vmcnt(12) lgkmcnt(11)\n\t"
    "v_pk_fma_f16 v241, v237, 4.0, v133 op_sel_hi:[1,0,1]\n\t"
    "v_mfma_f32_16x16x32_f16 v[136:139], a[176:179], a[208:211], v[136:139]\n\t"
    "ds_read_b128 a[236:239], v245 offset:36064\n\t"
    "v_pk_fma_f16 v252, v252, 0.5, v133 op_sel_hi:[1,0,1]\n\t"
    "v_mfma_f32_16x16x32_f16 v[136:139], a[180:183], a[212:215], v[136:139]\n\t"
    "v_pk_fma_f16 v250, v241, 2.0, v240 op_sel_hi:[1,0,1]\n\t"
    "v_pk_fma_f16 v251, v241, -2.0, v240 op_sel_hi:[1,0,1]\n\t"
    "v_mfma_f32_16x16x32_f16 v[136:139], a[184:187], a[216:219], v[136:139]\n\t"
    "global_load_dwordx4 a[160:163], v242, s[16:17] offset:-4096 sc1\n\t"
    "ds_write_addtid_b32 v250 offset:18176\n\t"
    "v_mfma_f32_16x16x32_f16 v[136:139], a[188:191], a[220:223], v[136:139]\n\t"
    "v_pk_fma_f16 v252, v252, 0.5, v129 op_sel_hi:[1,0,1]\n\t"
    "s_nop 0\n\t"
    "v_mfma_f32_16x16x32_f16 v[144:147], a[188:191], a[224:227], v[144:147]\n\t"
    "ds_write_addtid_b32 v251 offset:27136\n\t"
    "v_add_u32_e64 v245, s26, v245\n\t"
    "v_mfma_f32_16x16x32_f16 v[144:147], a[184:187], a[220:223], v[144:147]\n\t"
    "ds_write_addtid_b32 v252 offset:36096\n\t"
    "v_pk_add_f16 v240, v130, v234\n\t"
    "v_mfma_f32_16x16x32_f16 v[144:147], a[180:183], a[216:219], v[144:147]\n\t"
    "v_pk_add_f16 v241, v134, v238\n\t"
    "v_pk_fma_f16 v252, v238, 0.5, v234 op_sel_hi:[1,0,1]\n\t"
    "v_mfma_f32_16x16x32_f16 v[144:147], a[176:179], a[212:215], v[144:147]\n\t"
    "s_waitcnt lgkmcnt(9)\n\t"
    "v_pk_add_f16 v250, v240, v241\n\t"
    "v_mfma_f32_16x16x32_f16 v[152:155], a[176:179], a[216:219], v[152:155]\n\t"
    "v_pk_add_f16 v251, v240, v241 neg_lo:[0,1] neg_hi:[0,1]\n\t"
    "v_add_u32 v246, s26, v246\n\t"
    "v_mfma_f32_16x16x32_f16 v[152:155], a[180:183], a[220:223], v[152:155]\n\t"
    "global_load_dwordx4 a[164:167], v242, s[16:17] offset:-3072 sc1\n\t"
    "ds_write_addtid_b32 v250 offset:512\n\t"
    "v_mfma_f32_16x16x32_f16 v[152:155], a[184:187], a[224:227], v[152:155]\n\t"
    "v_pk_fma_f16 v240, v234, 4.0, v130 op_sel_hi:[1,0,1]\n\t"
    "v_mfma_f32_16x16x32_f16 v[152:155], a[188:191], a[228:231], v[152:155]\n\t"
    "ds_write_addtid_b32 v251 offset:9472\n\t"
    "v_pk_fma_f16 v241, v238, 4.0, v134 op_sel_hi:[1,0,1]\n\t"
    "v_mfma_f32_16x16x32_f16 v[160:163], a[188:191], a[232:235], v[160:163]\n\t"
    "v_pk_fma_f16 v252, v252, 0.5, v134 op_sel_hi:[1,0,1]\n\t"
    "v_pk_fma_f16 v250, v241, 2.0, v240 op_sel_hi:[1,0,1]\n\t"
    "v_mfma_f32_16x16x32_f16 v[160:163], a[184:187], a[228:231], v[160:163]\n\t"
    "s_waitcnt lgkmcnt(5)\n\t"
    "v_pk_fma_f16 v251, v241, -2.0, v240 op_sel_hi:[1,0,1]\n\t"
    "v_mfma_f32_16x16x32_f16 v[160:163], a[180:183], a[224:227], v[160:163]\n\t"
    "ds_write_addtid_b32 v250 offset:18432\n\t"
    "v_pk_fma_f16 v252, v252, 0.5, v130 op_sel_hi:[1,0,1]\n\t"
    "v_mfma_f32_16x16x32_f16 v[160:163], a[176:179], a[220:223], v[160:163]\n\t"
    "ds_write_addtid_b32 v251 offset:27392\n\t"
    "s_add_u32 s27, s27, 64\n\t"
    "v_mfma_f32_16x16x32_f16 v[168:171], a[176:179], a[224:227], v[168:171]\n\t"
    "global_load_dwordx4 a[168:171], v242, s[16:17] offset:-2048 sc1\n\t"
    "ds_write_addtid_b32 v252 offset:36352\n\t"
    "v_mfma_f32_16x16x32_f16 v[168:171], a[180:183], a[228:231], v[168:171]\n\t"
    "v_pk_add_f16 v240, v131, v235\n\t"
    "v_pk_add_f16 v241, v135, v239\n\t"
    "v_mfma_f32_16x16x32_f16 v[168:171], a[184:187], a[232:235], v[168:171]\n\t"
    "v_pk_fma_f16 v252, v239, 0.5, v235 op_sel_hi:[1,0,1]\n\t"
    "v_pk_add_f16 v250, v240, v241\n\t"
    "v_mfma_f32_16x16x32_f16 v[168:171], a[188:191], a[236:239], v[168:171]\n\t"
    "v_pk_add_f16 v251, v240, v241 neg_lo:[0,1] neg_hi:[0,1]\n\t"
    "s_sub_u32 s27, s27, s26\n\t"
    "v_mfma_f32_16x16x32_f16 v[176:179], a[188:191], a[240:243], v[176:179]\n\t"
    "ds_write_addtid_b32 v250 offset:768\n\t"
    "v_pk_fma_f16 v240, v235, 4.0, v131 op_sel_hi:[1,0,1]\n\t"
    "v_mfma_f32_16x16x32_f16 v[176:179], a[184:187], a[236:239], v[176:179]\n\t"
    "ds_write_addtid_b32 v251 offset:9728\n\t"
    "v_pk_fma_f16 v241, v239, 4.0, v135 op_sel_hi:[1,0,1]\n\t"
    "v_mfma_f32_16x16x32_f16 v[176:179], a[180:183], a[232:235], v[176:179]\n\t"
    "v_pk_fma_f16 v252, v252, 0.5, v135 op_sel_hi:[1,0,1]\n\t"
    "v_pk_fma_f16 v250, v241, 2.0, v240 op_sel_hi:[1,0,1]\n\t"
    "v_mfma_f32_16x16x32_f16 v[176:179], a[176:179], a[228:231], v[176:179]\n\t"
    "global_load_dwordx4 a[172:175], v242, s[16:17] offset:-1024 sc1\n\t"
    "s_nop 0\n\t"
    "v_mfma_f32_16x16x32_f16 v[184:187], a[176:179], a[232:235], v[184:187]\n\t"
    "v_pk_fma_f16 v251, v241, -2.0, v240 op_sel_hi:[1,0,1]\n\t"
    "v_mad_i32_i24 v247, v248, s27, v246\n\t"
    "v_mfma_f32_16x16x32_f16 v[184:187], a[180:183], a[236:239], v[184:187]\n\t"
    "ds_write_addtid_b32 v250 offset:18688\n\t"
    "v_pk_fma_f16 v252, v252, 0.5, v131 op_sel_hi:[1,0,1]\n\t"
    "v_mfma_f32_16x16x32_f16 v[184:187], a[184:187], a[240:243], v[184:187]\n\t"
    "ds_write_addtid_b32 v251 offset:27648\n\t"
    "v_mfma_f32_16x16x32_f16 v[184:187], a[188:191], a[244:247], v[184:187]\n\t"
    "ds_write_addtid_b32 v252 offset:36608\n\t"
    "v_mfma_f32_16x16x32_f16 v[192:195], a[188:191], a[248:251], v[192:195]\n\t"
    "v_mfma_f32_16x16x32_f16 v[192:195], a[184:187], a[244:247], v[192:195]\n\t"
    "v_mfma_f32_16x16x32_f16 v[192:195], a[180:183], a[240:243], v[192:195]\n\t"
    "v_mfma_f32_16x16x32_f16 v[192:195], a[176:179], a[236:239], v[192:195]\n\t"
    "s_waitcnt vmcnt(12) lgkmcnt(0)\n\t"
    "s_barrier\n\t"
    "v_mfma_f32_16x16x32_f16 v[140:143], a[192:195], a[208:211], v[140:143]\n\t"
    "ds_read_b128 a[208:211], v245 offset:0\n\t"
    "v_mfma_f32_16x16x32_f16 v[140:143], a[196:199], a[212:215], v[140:143]\n\t"
    "s_sub_u32 s30, s30, s26\n\t"
    "s_nop 0\n\t"
    "v_mfma_f32_16x16x32_f16 v[140:143], a[200:203], a[216:219], v[140:143]\n\t"
    "global_load_dwordx4 a[176:179], v242, s[16:17] offset:0 sc1\n\t"
    "v_mfma_f32_16x16x32_f16 v[140:143], a[204:207], a[220:223], v[140:143]\n\t"
    "v_mfma_f32_16x16x32_f16 v[148:151], a[204:207], a[224:227], v[148:151]\n\t"
    "v_mfma_f32_16x16x32_f16 v[148:151], a[200:203], a[220:223], v[148:151]\n\t"
    "v_mfma_f32_16x16x32_f16 v[148:151], a[196:199], a[216:219], v[148:151]\n\t"
    "v_mfma_f32_16x16x32_f16 v[148:151], a[192:195], a[212:215], v[148:151]\n\t"
    "ds_read_b128 a[212:215], v245 offset:32\n\t"
    "v_mfma_f32_16x16x32_f16 v[156:159], a[192:195], a[216:219], v[156:159]\n\t"
    "ds_read_b128 a[216:219], v245 offset:64\n\t"
    "v_mfma_f32_16x16x32_f16 v[156:159], a[196:199], a[220:223], v[156:159]\n\t"
    "global_load_dwordx4 a[180:183], v242, s[16:17] offset:1024 sc1\n\t"
    "v_mfma_f32_16x16x32_f16 v[156:159], a[200:203], a[224:227], v[156:159]\n\t"
    "v_mfma_f32_16x16x32_f16 v[156:159], a[204:207], a[228:231], v[156:159]\n\t"
    "v_mfma_f32_16x16x32_f16 v[164:167], a[204:207], a[232:235], v[164:167]\n\t"
    "v_mfma_f32_16x16x32_f16 v[164:167], a[200:203], a[228:231], v[164:167]\n\t"
    "v_mfma_f32_16x16x32_f16 v[164:167], a[196:199], a[224:227], v[164:167]\n\t"
    "s_sub_u32 s26, 0, s26\n\t"
    "s_nop 0\n\t"
    "v_mfma_f32_16x16x32_f16 v[164:167], a[192:195], a[220:223], v[164:167]\n\t"
    "ds_read_b128 a[220:223], v245 offset:96\n\t"
    "v_mfma_f32_16x16x32_f16 v[172:175], a[192:195], a[224:227], v[172:175]\n\t"
    "ds_read_b128 a[224:227], v245 offset:128\n\t"
    "v_mfma_f32_16x16x32_f16 v[172:175], a[196:199], a[228:231], v[172:175]\n\t"
    "global_load_dwordx4 a[184:187], v242, s[16:17] offset:2048 sc1\n\t"
    "v_mfma_f32_16x16x32_f16 v[172:175], a[200:203], a[232:235], v[172:175]\n\t"
    "v_mfma_f32_16x16x32_f16 v[172:175], a[204:207], a[236:239], v[172:175]\n\t"
    "v_mfma_f32_16x16x32_f16 v[180:183], a[204:207], a[240:243], v[180:183]\n\t"
    "v_mfma_f32_16x16x32_f16 v[180:183], a[200:203], a[236:239], v[180:183]\n\t"
    "v_mfma_f32_16x16x32_f16 v[180:183], a[196:199], a[232:235], v[180:183]\n\t"
    "v_mfma_f32_16x16x32_f16 v[180:183], a[192:195], a[228:231], v[180:183]\n\t"
    "ds_read_b128 a[228:231], v245 offset:160\n\t"
    "v_mfma_f32_16x16x32_f16 v[188:191], a[192:195], a[232:235], v[188:191]\n\t"
    "ds_read_b128 a[232:235], v245 offset:192\n\t"
    "v_mfma_f32_16x16x32_f16 v[188:191], a[196:199], a[236:239], v[188:191]\n\t"
    "global_load_dwordx4 a[188:191], v242, s[16:17] offset:3072 sc1\n\t"
    "v_mfma_f32_16x16x32_f16 v[188:191], a[200:203], a[240:243], v[188:191]\n\t"
    "v_add_u32 v242, 8192, v242\n\t"
    "v_mfma_f32_16x16x32_f16 v[188:191], a[204:207], a[244:247], v[188:191]\n\t"
    "v_mfma_f32_16x16x32_f16 v[196:199], a[204:207], a[248:251], v[196:199]\n\t"
    "ds_read_b128 a[248:251], v246 offset:64\n\t"
    "v_mfma_f32_16x16x32_f16 v[196:199], a[200:203], a[244:247], v[196:199]\n\t"
    "ds_read_b128 a[244:247], v246 offset:32\n\t"
    "v_mfma_f32_16x16x32_f16 v[196:199], a[196:199], a[240:243], v[196:199]\n\t"
    "s_waitcnt vmcnt(12) lgkmcnt(4)\n\t"
    "s_nop 0\n\t"
    "v_mfma_f32_16x16x32_f16 v[196:199], a[192:195], a[236:239], v[196:199]\n\t"
    ".p2align 3\n\t"
    "L_E_%=:\n\t"
    ".p2align 3\n\t"
    "v_mfma_f32_16x16x32_f16 v[0:3], a[128:131], a[208:211], v[0:3]\n\t"
    "ds_read_b128 a[240:243], v247 offset:0\n\t"
    "s_and_b32 s36, s25, 512\n\t"
    "v_mfma_f32_16x16x32_f16 v[0:3], a[132:135], a[212:215], v[0:3]\n\t"
    "ds_read_b128 a[236:239], v245 offset:224\n\t"
    "v_lshl_or_b32 v243, s36, 22, v243\n\t"
    "v_mfma_f32_16x16x32_f16 v[0:3], a[136:139], a[216:219], v[0:3]\n\t"
    "global_load_dwordx4 a[192:195], v242, s[16:17] offset:-4096 sc1\n\t"
    "v_mfma_f32_16x16x32_f16 v[0:3], a[140:143], a[220:223], v[0:3]\n\t"
    "s_waitcnt vmcnt(62)\n\t"
    "s_mov_b32 m0, s30\n\t"
    "v_mfma_f32_16x16x32_f16 v[8:11], a[140:143], a[224:227], v[8:11]\n\t"
    "buffer_load_dwordx4 v[128:131], v243, s[8:11], s25 offen offset:0\n\t"
    "v_mfma_f32_16x16x32_f16 v[8:11], a[136:139], a[220:223], v[8:11]\n\t"
    "v_pk_maximum3_f16 v240, v200, v200, s29 op_sel:[0,0,0] op_sel_hi:[1,1,0] neg_lo:[0,1,0] neg_hi:[0,1,0]\n\t"
    "v_pk_maximum3_f16 v241, v204, v204, s29 op_sel:[0,0,0] op_sel_hi:[1,1,0] neg_lo:[0,1,0] neg_hi:[0,1,0]\n\t"
    "v_mfma_f32_16x16x32_f16 v[8:11], a[132:135], a[216:219], v[8:11]\n\t"
    "v_pk_maximum3_f16 v250, v208, v208, s29 op_sel:[0,0,0] op_sel_hi:[1,1,0] neg_lo:[0,1,0] neg_hi:[0,1,0]\n\t"
    "v_pk_maximum3_f16 v251, v212, v212, s29 op_sel:[0,0,0] op_sel_hi:[1,1,0] neg_lo:[0,1,0] neg_hi:[0,1,0]\n\t"
    "v_mfma_f32_16x16x32_f16 v[8:11], a[128:131], a[212:215], v[8:11]\n\t"
    "s_waitcnt lgkmcnt(3)\n\t"
    "v_pk_maximum3_f16 v249, v240, v241, v249\n\t"
    "v_mfma_f32_16x16x32_f16 v[16:19], a[128:131], a[216:219], v[16:19]\n\t"
    "v_pk_maximum3_f16 v249, v250, v251, v249\n\t"
    "v_pk_mad_u16 v240, v240, 8, s29 op_sel:[0,0,1] op_sel_hi:[1,0,1]\n\t"
    "v_mfma_f32_16x16x32_f16 v[16:19], a[132:135], a[220:223], v[16:19]\n\t"
    "global_load_dwordx4 a[196:199], v242, s[16:17] offset:-3072 sc1\n\t"
    "s_nop 0\n\t"
    "v_mfma_f32_16x16x32_f16 v[16:19], a[136:139], a[224:227], v[16:19]\n\t"
    "buffer_load_dwordx4 v[232:235], v243, s[8:11], s25 offen offset:64\n\t"
    "v_mfma_f32_16x16x32_f16 v[16:19], a[140:143], a[228:231], v[16:19]\n\t"
    "v_pk_mad_u16 v241, v241, 8, s29 op_sel:[0,0,1] op_sel_hi:[1,0,1]\n\t"
    "v_pk_mad_u16 v250, v250, 8, s29 op_sel:[0,0,1] op_sel_hi:[1,0,1]\n\t"
    "v_mfma_f32_16x16x32_f16 v[24:27], a[140:143], a[232:235], v[24:27]\n\t"
    "v_pk_mad_u16 v251, v251, 8, s29 op_sel:[0,0,1] op_sel_hi:[1,0,1]\n\t"
    "v_bfi_b32 v200, s24, v240, v200\n\t"
    "v_mfma_f32_16x16x32_f16 v[24:27], a[136:139], a[228:231], v[24:27]\n\t"
    "s_waitcnt lgkmcnt(0)\n\t"
    "v_bfi_b32 v204, s24, v241, v204\n\t"
    "v_mfma_f32_16x16x32_f16 v[24:27], a[132:135], a[224:227], v[24:27]\n\t"
    "v_bfi_b32 v208, s24, v250, v208\n\t"
    "v_bfi_b32 v212, s24, v251, v212\n\t"
    "v_mfma_f32_16x16x32_f16 v[24:27], a[128:131], a[220:223], v[24:27]\n\t"
    "v_pk_add_f16 v240, v200, v208\n\t"
    "v_pk_add_f16 v241, v204, v212\n\t"
    "v_mfma_f32_16x16x32_f16 v[32:35], a[128:131], a[224:227], v[32:35]\n\t"
    "global_load_dwordx4 a[200:203], v242, s[16:17] offset:-2048 sc1\n\t"
    "s_nop 0\n\t"
    "v_mfma_f32_16x16x32_f16 v[32:35], a[132:135], a[228:231], v[32:35]\n\t"
    "v_pk_fma_f16 v252, v212, 0.5, v208 op_sel_hi:[1,0,1]\n\t"
    "v_pk_add_f16 v250, v240, v241\n\t"
    "v_mfma_f32_16x16x32_f16 v[32:35], a[136:139], a[232:235], v[32:35]\n\t"
    "v_pk_add_f16 v251, v240, v241 neg_lo:[0,1] neg_hi:[0,1]\n\t"
    "v_mfma_f32_16x16x32_f16 v[32:35], a[140:143], a[236:239], v[32:35]\n\t"
    "ds_write_addtid_b32 v250 offset:0\n\t"
    "v_pk_fma_f16 v240, v208, 4.0, v200 op_sel_hi:[1,0,1]\n\t"
    "v_mfma_f32_16x16x32_f16 v[40:43], a[140:143], a[240:243], v[40:43]\n\t"
    "v_lshl_or_b32 v244, s36, 22, v244\n\t"
    "v_mfma_f32_16x16x32_f16 v[40:43], a[136:139], a[236:239], v[40:43]\n\t"
    "ds_write_addtid_b32 v251 offset:8960\n\t"
    "v_pk_fma_f16 v241, v212, 4.0, v204 op_sel_hi:[1,0,1]\n\t"
    "v_mfma_f32_16x16x32_f16 v[40:43], a[132:135], a[232:235], v[40:43]\n\t"
    "v_pk_fma_f16 v252, v252, 0.5, v204 op_sel_hi:[1,0,1]\n\t"
    "v_pk_fma_f16 v250, v241, 2.0, v240 op_sel_hi:[1,0,1]\n\t"
    "v_mfma_f32_16x16x32_f16 v[40:43], a[128:131], a[228:231], v[40:43]\n\t"
    "global_load_dwordx4 a[204:207], v242, s[16:17] offset:-1024 sc1\n\t"
    "v_mfma_f32_16x16x32_f16 v[48:51], a[128:131], a[232:235], v[48:51]\n\t"
    "v_pk_fma_f16 v251, v241, -2.0, v240 op_sel_hi:[1,0,1]\n\t"
    "v_mfma_f32_16x16x32_f16 v[48:51], a[132:135], a[236:239], v[48:51]\n\t"
    "ds_write_addtid_b32 v250 offset:17920\n\t"
    "v_pk_fma_f16 v252, v252, 0.5, v200 op_sel_hi:[1,0,1]\n\t"
    "v_mfma_f32_16x16x32_f16 v[48:51], a[136:139], a[240:243], v[48:51]\n\t"
    "ds_write_addtid_b32 v251 offset:26880\n\t"
    "v_mfma_f32_16x16x32_f16 v[48:51], a[140:143], a[244:247], v[48:51]\n\t"
    "ds_write_addtid_b32 v252 offset:35840\n\t"
    "v_pk_maximum3_f16 v240, v201, v201, s29 op_sel:[0,0,0] op_sel_hi:[1,1,0] neg_lo:[0,1,0] neg_hi:[0,1,0]\n\t"
    "v_mfma_f32_16x16x32_f16 v[56:59], a[140:143], a[248:251], v[56:59]\n\t"
    "v_pk_maximum3_f16 v241, v205, v205, s29 op_sel:[0,0,0] op_sel_hi:[1,1,0] neg_lo:[0,1,0] neg_hi:[0,1,0]\n\t"
    "v_pk_maximum3_f16 v250, v209, v209, s29 op_sel:[0,0,0] op_sel_hi:[1,1,0] neg_lo:[0,1,0] neg_hi:[0,1,0]\n\t"
    "v_mfma_f32_16x16x32_f16 v[56:59], a[136:139], a[244:247], v[56:59]\n\t"
    "v_pk_maximum3_f16 v251, v213, v213, s29 op_sel:[0,0,0] op_sel_hi:[1,1,0] neg_lo:[0,1,0] neg_hi:[0,1,0]\n\t"
    "v_pk_maximum3_f16 v249, v240, v241, v249\n\t"
    "v_mfma_f32_16x16x32_f16 v[56:59], a[132:135], a[240:243], v[56:59]\n\t"
    "buffer_load_dwordx4 v[132:135], v243, s[8:11], s25 offen offset:1024\n\t"
    "v_mfma_f32_16x16x32_f16 v[56:59], a[128:131], a[236:239], v[56:59]\n\t"
    "s_waitcnt vmcnt(15)\n\t"
    "v_pk_maximum3_f16 v249, v250, v251, v249\n\t"
    "v_mfma_f32_16x16x32_f16 v[4:7], a[144:147], a[208:211], v[4:7]\n\t"
    "ds_read_b128 a[208:211], v245 offset:8960\n\t"
    "v_pk_mad_u16 v240, v240, 8, s29 op_sel:[0,0,1] op_sel_hi:[1,0,1]\n\t"
    "v_mfma_f32_16x16x32_f16 v[4:7], a[148:151], a[212:215], v[4:7]\n\t"
    "v_pk_mad_u16 v241, v241, 8, s29 op_sel:[0,0,1] op_sel_hi:[1,0,1]\n\t"
    "v_pk_mad_u16 v250, v250, 8, s29 op_sel:[0,0,1] op_sel_hi:[1,0,1]\n\t"
    "v_mfma_f32_16x16x32_f16 v[4:7], a[152:155], a[216:219], v[4:7]\n\t"
    "global_load_dwordx4 a[128:131], v242, s[16:17] offset:0 sc1\n\t"
    "s_nop 0\n\t"
    "v_mfma_f32_16x16x32_f16 v[4:7], a[156:159], a[220:223], v[4:7]\n\t"
    "buffer_load_dwordx4 v[236:239], v243, s[8:11], s25 offen offset:1088\n\t"
    "v_mfma_f32_16x16x32_f16 v[12:15], a[156:159], a[224:227], v[12:15]\n\t"
    "v_pk_mad_u16 v251, v251, 8, s29 op_sel:[0,0,1] op_sel_hi:[1,0,1]\n\t"
    "v_bfi_b32 v201, s24, v240, v201\n\t"
    "v_mfma_f32_16x16x32_f16 v[12:15], a[152:155], a[220:223], v[12:15]\n\t"
    "v_bfi_b32 v205, s24, v241, v205\n\t"
    "v_bfi_b32 v209, s24, v250, v209\n\t"
    "v_mfma_f32_16x16x32_f16 v[12:15], a[148:151], a[216:219], v[12:15]\n\t"
    "v_bfi_b32 v213, s24, v251, v213\n\t"
    "v_pk_add_f16 v240, v201, v209\n\t"
    "v_mfma_f32_16x16x32_f16 v[12:15], a[144:147], a[212:215], v[12:15]\n\t"
    "ds_read_b128 a[212:215], v245 offset:8992\n\t"
    "v_pk_add_f16 v241, v205, v213\n\t"
    "v_mfma_f32_16x16x32_f16 v[20:23], a[144:147], a[216:219], v[20:23]\n\t"
    "ds_read_b128 a[216:219], v245 offset:9024\n\t"
    "v_pk_fma_f16 v252, v213, 0.5, v209 op_sel_hi:[1,0,1]\n\t"
    "v_mfma_f32_16x16x32_f16 v[20:23], a[148:151], a[220:223], v[20:23]\n\t"
    "global_load_dwordx4 a[132:135], v242, s[16:17] offset:1024 sc1\n\t"
    "v_mfma_f32_16x16x32_f16 v[20:23], a[152:155], a[224:227], v[20:23]\n\t"
    "v_pk_add_f16 v250, v240, v241\n\t"
    "v_pk_add_f16 v251, v240, v241 neg_lo:[0,1] neg_hi:[0,1]\n\t"
    "v_mfma_f32_16x16x32_f16 v[20:23], a[156:159], a[228:231], v[20:23]\n\t"
    "ds_write_addtid_b32 v250 offset:256\n\t"
    "v_pk_fma_f16 v240, v209, 4.0, v201 op_sel_hi:[1,0,1]\n\t"
    "v_mfma_f32_16x16x32_f16 v[28:31], a[156:159], a[232:235], v[28:31]\n\t"
    "ds_write_addtid_b32 v251 offset:9216\n\t"
    "v_pk_fma_f16 v241, v213, 4.0, v205 op_sel_hi:[1,0,1]\n\t"
    "v_mfma_f32_16x16x32_f16 v[28:31], a[152:155], a[228:231], v[28:31]\n\t"
    "v_pk_fma_f16 v252, v252, 0.5, v205 op_sel_hi:[1,0,1]\n\t"
    "v_pk_fma_f16 v250, v241, 2.0, v240 op_sel_hi:[1,0,1]\n\t"
    "v_mfma_f32_16x16x32_f16 v[28:31], a[148:151], a[224:227], v[28:31]\n\t"
    "v_pk_fma_f16 v251, v241, -2.0, v240 op_sel_hi:[1,0,1]\n\t"
    "v_mfma_f32_16x16x32_f16 v[28:31], a[144:147], a[220:223], v[28:31]\n\t"
    "ds_read_b128 a[220:223], v245 offset:9056\n\t"
    "ds_write_addtid_b32 v250 offset:18176\n\t"
    "v_mfma_f32_16x16x32_f16 v[36:39], a[144:147], a[224:227], v[36:39]\n\t"
    "ds_read_b128 a[224:227], v245 offset:9088\n\t"
    "v_pk_fma_f16 v252, v252, 0.5, v201 op_sel_hi:[1,0,1]\n\t"
    "v_mfma_f32_16x16x32_f16 v[36:39], a[148:151], a[228:231], v[36:39]\n\t"
    "global_load_dwordx4 a[136:139], v242, s[16:17] offset:2048 sc1\n\t"
    "ds_write_addtid_b32 v251 offset:27136\n\t"
    "v_mfma_f32_16x16x32_f16 v[36:39], a[152:155], a[232:235], v[36:39]\n\t"
    "ds_write_addtid_b32 v252 offset:36096\n\t"
    "v_pk_maximum3_f16 v240, v202, v202, s29 op_sel:[0,0,0] op_sel_hi:[1,1,0] neg_lo:[0,1,0] neg_hi:[0,1,0]\n\t"
    "v_mfma_f32_16x16x32_f16 v[36:39], a[156:159], a[236:239], v[36:39]\n\t"
    "v_pk_maximum3_f16 v241, v206, v206, s29 op_sel:[0,0,0] op_sel_hi:[1,1,0] neg_lo:[0,1,0] neg_hi:[0,1,0]\n\t"
    "v_pk_maximum3_f16 v250, v210, v210, s29 op_sel:[0,0,0] op_sel_hi:[1,1,0] neg_lo:[0,1,0] neg_hi:[0,1,0]\n\t"
    "v_mfma_f32_16x16x32_f16 v[44:47], a[156:159], a[240:243], v[44:47]\n\t"
    "v_pk_maximum3_f16 v251, v214, v214, s29 op_sel:[0,0,0] op_sel_hi:[1,1,0] neg_lo:[0,1,0] neg_hi:[0,1,0]\n\t"
    "v_pk_maximum3_f16 v249, v240, v241, v249\n\t"
    "v_mfma_f32_16x16x32_f16 v[44:47], a[152:155], a[236:239], v[44:47]\n\t"
    "v_pk_maximum3_f16 v249, v250, v251, v249\n\t"
    "v_mfma_f32_16x16x32_f16 v[44:47], a[148:151], a[232:235], v[44:47]\n\t"
    "v_pk_mad_u16 v240, v240, 8, s29 op_sel:[0,0,1] op_sel_hi:[1,0,1]\n\t"
    "v_mfma_f32_16x16x32_f16 v[44:47], a[144:147], a[228:231], v[44:47]\n\t"
    "ds_read_b128 a[228:231], v245 offset:9120\n\t"
    "v_pk_mad_u16 v241, v241, 8, s29 op_sel:[0,0,1] op_sel_hi:[1,0,1]\n\t"
    "v_mfma_f32_16x16x32_f16 v[52:55], a[144:147], a[232:235], v[52:55]\n\t"
    "ds_read_b128 a[232:235], v245 offset:9152\n\t"
    "v_pk_mad_u16 v250, v250, 8, s29 op_sel:[0,0,1] op_sel_hi:[1,0,1]\n\t"
    "v_mfma_f32_16x16x32_f16 v[52:55], a[148:151], a[236:239], v[52:55]\n\t"
    "global_load_dwordx4 a[140:143], v242, s[16:17] offset:3072 sc1\n\t"
    "v_mfma_f32_16x16x32_f16 v[52:55], a[152:155], a[240:243], v[52:55]\n\t"
    "v_add_u32 v242, 8192, v242\n\t"
    "v_pk_mad_u16 v251, v251, 8, s29 op_sel:[0,0,1] op_sel_hi:[1,0,1]\n\t"
    "v_mfma_f32_16x16x32_f16 v[52:55], a[156:159], a[244:247], v[52:55]\n\t"
    "v_bfi_b32 v202, s24, v240, v202\n\t"
    "v_bfi_b32 v206, s24, v241, v206\n\t"
    "v_mfma_f32_16x16x32_f16 v[60:63], a[156:159], a[248:251], v[60:63]\n\t"
    "ds_read_b128 a[248:251], v246 offset:9024\n\t"
    "v_bfi_b32 v210, s24, v250, v210\n\t"
    "v_mfma_f32_16x16x32_f16 v[60:63], a[152:155], a[244:247], v[60:63]\n\t"
    "ds_read_b128 a[244:247], v246 offset:8992\n\t"
    "v_bfi_b32 v214, s24, v251, v214\n\t"
    "v_mfma_f32_16x16x32_f16 v[60:63], a[148:151], a[240:243], v[60:63]\n\t"
    "ds_read_b128 a[240:243], v247 offset:8960\n\t"
    "v_pk_add_f16 v240, v202, v210\n\t"
    "v_mfma_f32_16x16x32_f16 v[60:63], a[144:147], a[236:239], v[60:63]\n\t"
    "s_waitcnt vmcnt(16) lgkmcnt(7)\n\t"
    "v_pk_add_f16 v241, v206, v214\n\t"
    "v_mfma_f32_16x16x32_f16 v[64:67], a[160:163], a[208:211], v[64:67]\n\t"
    "ds_read_b128 a[236:239], v245 offset:9184\n\t"
    "v_pk_fma_f16 v252, v214, 0.5, v210 op_sel_hi:[1,0,1]\n\t"
    "v_mfma_f32_16x16x32_f16 v[64:67], a[164:167], a[212:215], v[64:67]\n\t"
    "v_pk_add_f16 v250, v240, v241\n\t"
    "v_pk_add_f16 v251, v240, v241 neg_lo:[0,1] neg_hi:[0,1]\n\t"
    "v_mfma_f32_16x16x32_f16 v[64:67], a[168:171], a[216:219], v[64:67]\n\t"
    "global_load_dwordx4 a[144:147], v242, s[16:17] offset:-4096 sc1\n\t"
    "ds_write_addtid_b32 v250 offset:512\n\t"
    "v_mfma_f32_16x16x32_f16 v[64:67], a[172:175], a[220:223], v[64:67]\n\t"
    "v_pk_fma_f16 v240, v210, 4.0, v202 op_sel_hi:[1,0,1]\n\t"
    "s_nop 0\n\t"
    "v_mfma_f32_16x16x32_f16 v[72:75], a[172:175], a[224:227], v[72:75]\n\t"
    "ds_write_addtid_b32 v251 offset:9472\n\t"
    "v_pk_fma_f16 v241, v214, 4.0, v206 op_sel_hi:[1,0,1]\n\t"
    "v_mfma_f32_16x16x32_f16 v[72:75], a[168:171], a[220:223], v[72:75]\n\t"
    "v_pk_fma_f16 v252, v252, 0.5, v206 op_sel_hi:[1,0,1]\n\t"
    "v_pk_fma_f16 v250, v241, 2.0, v240 op_sel_hi:[1,0,1]\n\t"
    "v_mfma_f32_16x16x32_f16 v[72:75], a[164:167], a[216:219], v[72:75]\n\t"
    "v_pk_fma_f16 v251, v241, -2.0, v240 op_sel_hi:[1,0,1]\n\t"
    "v_mfma_f32_16x16x32_f16 v[72:75], a[160:163], a[212:215], v[72:75]\n\t"
    "s_waitcnt lgkmcnt(6)\n\t"
    "ds_write_addtid_b32 v250 offset:18432\n\t"
    "v_mfma_f32_16x16x32_f16 v[80:83], a[160:163], a[216:219], v[80:83]\n\t"
    "v_pk_fma_f16 v252, v252, 0.5, v202 op_sel_hi:[1,0,1]\n\t"
    "s_nop 0\n\t"
    "v_mfma_f32_16x16x32_f16 v[80:83], a[164:167], a[220:223], v[80:83]\n\t"
    "global_load_dwordx4 a[148:151], v242, s[16:17] offset:-3072 sc1\n\t"
    "ds_write_addtid_b32 v251 offset:27392\n\t"
    "v_mfma_f32_16x16x32_f16 v[80:83], a[168:171], a[224:227], v[80:83]\n\t"
    "ds_write_addtid_b32 v252 offset:36352\n\t"
    "v_pk_maximum3_f16 v240, v203, v203, s29 op_sel:[0,0,0] op_sel_hi:[1,1,0] neg_lo:[0,1,0] neg_hi:[0,1,0]\n\t"
    "v_mfma_f32_16x16x32_f16 v[80:83], a[172:175], a[228:231], v[80:83]\n\t"
    "v_pk_maximum3_f16 v241, v207, v207, s29 op_sel:[0,0,0] op_sel_hi:[1,1,0] neg_lo:[0,1,0] neg_hi:[0,1,0]\n\t"
    "v_pk_maximum3_f16 v250, v211, v211, s29 op_sel:[0,0,0] op_sel_hi:[1,1,0] neg_lo:[0,1,0] neg_hi:[0,1,0]\n\t"
    "v_mfma_f32_16x16x32_f16 v[88:91], a[172:175], a[232:235], v[88:91]\n\t"
    "v_pk_maximum3_f16 v251, v215, v215, s29 op_sel:[0,0,0] op_sel_hi:[1,1,0] neg_lo:[0,1,0] neg_hi:[0,1,0]\n\t"
    "v_pk_maximum3_f16 v249, v240, v241, v249\n\t"
    "v_mfma_f32_16x16x32_f16 v[88:91], a[168:171], a[228:231], v[88:91]\n\t"
    "s_waitcnt lgkmcnt(5)\n\t"
    "v_pk_maximum3_f16 v249, v250, v251, v249\n\t"
    "v_mfma_f32_16x16x32_f16 v[88:91], a[164:167], a[224:227], v[88:91]\n\t"
    "v_pk_mad_u16 v240, v240, 8, s29 op_sel:[0,0,1] op_sel_hi:[1,0,1]\n\t"
    "v_pk_mad_u16 v241, v241, 8, s29 op_sel:[0,0,1] op_sel_hi:[1,0,1]\n\t"
    "v_mfma_f32_16x16x32_f16 v[88:91], a[160:163], a[220:223], v[88:91]\n\t"
    "v_pk_mad_u16 v250, v250, 8, s29 op_sel:[0,0,1] op_sel_hi:[1,0,1]\n\t"
    "v_pk_mad_u16 v251, v251, 8, s29 op_sel:[0,0,1] op_sel_hi:[1,0,1]\n\t"
    "v_mfma_f32_16x16x32_f16 v[96:99], a[160:163], a[224:227], v[96:99]\n\t"
    "global_load_dwordx4 a[152:155], v242, s[16:17] offset:-2048 sc1\n\t"
    "s_nop 0\n\t"
    "v_mfma_f32_16x16x32_f16 v[96:99], a[164:167], a[228:231], v[96:99]\n\t"
    "v_bfi_b32 v203, s24, v240, v203\n\t"
    "v_bfi_b32 v207, s24, v241, v207\n\t"
    "v_mfma_f32_16x16x32_f16 v[96:99], a[168:171], a[232:235], v[96:99]\n\t"
    "v_bfi_b32 v211, s24, v250, v211\n\t"
    "v_bfi_b32 v215, s24, v251, v215\n\t"
    "v_mfma_f32_16x16x32_f16 v[96:99], a[172:175], a[236:239], v[96:99]\n\t"
    "v_pk_add_f16 v240, v203, v211\n\t"
    "v_mfma_f32_16x16x32_f16 v[104:107], a[172:175], a[240:243], v[104:107]\n\t"
    "v_pk_add_f16 v241, v207, v215\n\t"
    "v_pk_fma_f16 v252, v215, 0.5, v211 op_sel_hi:[1,0,1]\n\t"
    "v_mfma_f32_16x16x32_f16 v[104:107], a[168:171], a[236:239], v[104:107]\n\t"
    "v_pk_add_f16 v250, v240, v241\n\t"
    "v_mfma_f32_16x16x32_f16 v[104:107], a[164:167], a[232:235], v[104:107]\n\t"
    "v_pk_add_f16 v251, v240, v241 neg_lo:[0,1] neg_hi:[0,1]\n\t"
    "v_mfma_f32_16x16x32_f16 v[104:107], a[160:163], a[228:231], v[104:107]\n\t"
    "global_load_dwordx4 a[156:159], v242, s[16:17] offset:-1024 sc1\n\t"
    "ds_write_addtid_b32 v250 offset:768\n\t"
    "v_mfma_f32_16x16x32_f16 v[112:115], a[160:163], a[232:235], v[112:115]\n\t"
    "v_pk_fma_f16 v240, v211, 4.0, v203 op_sel_hi:[1,0,1]\n\t"
    "v_mfma_f32_16x16x32_f16 v[112:115], a[164:167], a[236:239], v[112:115]\n\t"
    "ds_write_addtid_b32 v251 offset:9728\n\t"
    "v_pk_fma_f16 v241, v215, 4.0, v207 op_sel_hi:[1,0,1]\n\t"
    "v_mfma_f32_16x16x32_f16 v[112:115], a[168:171], a[240:243], v[112:115]\n\t"
    "v_pk_fma_f16 v252, v252, 0.5, v207 op_sel_hi:[1,0,1]\n\t"
    "v_pk_fma_f16 v250, v241, 2.0, v240 op_sel_hi:[1,0,1]\n\t"
    "v_mfma_f32_16x16x32_f16 v[112:115], a[172:175], a[244:247], v[112:115]\n\t"
    "v_pk_fma_f16 v251, v241, -2.0, v240 op_sel_hi:[1,0,1]\n\t"
    "v_mfma_f32_16x16x32_f16 v[120:123], a[172:175], a[248:251], v[120:123]\n\t"
    "ds_write_addtid_b32 v250 offset:18688\n\t"
    "v_mfma_f32_16x16x32_f16 v[120:123], a[168:171], a[244:247], v[120:123]\n\t"
    "v_pk_fma_f16 v252, v252, 0.5, v203 op_sel_hi:[1,0,1]\n\t"
    "v_mfma_f32_16x16x32_f16 v[120:123], a[164:167], a[240:243], v[120:123]\n\t"
    "ds_write_addtid_b32 v251 offset:27648\n\t"
    "v_mfma_f32_16x16x32_f16 v[120:123], a[160:163], a[236:239], v[120:123]\n\t"
    "s_waitcnt vmcnt(16)\n\t"
    "ds_write_addtid_b32 v252 offset:36608\n\t"
    "v_mfma_f32_16x16x32_f16 v[68:71], a[176:179], a[208:211], v[68:71]\n\t"
    "ds_read_b128 a[208:211], v245 offset:17920\n\t"
    "s_waitcnt vmcnt(39)\n\t"
    "v_mfma_f32_16x16x32_f16 v[68:71], a[180:183], a[212:215], v[68:71]\n\t"
    "s_add_u32 m0, s30, 1056\n\t"
    "v_pk_maximum3_f16 v240, v216, v216, s29 op_sel:[0,0,0] op_sel_hi:[1,1,0] neg_lo:[0,1,0] neg_hi:[0,1,0]\n\t"
    "v_mfma_f32_16x16x32_f16 v[68:71], a[184:187], a[216:219], v[68:71]\n\t"
    "global_load_dwordx4 a[160:163], v242, s[16:17] offset:0 sc1\n\t"
    "v_mfma_f32_16x16x32_f16 v[68:71], a[188:191], a[220:223], v[68:71]\n\t"
    "v_pk_maximum3_f16 v241, v220, v220, s29 op_sel:[0,0,0] op_sel_hi:[1,1,0] neg_lo:[0,1,0] neg_hi:[0,1,0]\n\t"
    "v_pk_maximum3_f16 v250, v224, v224, s29 op_sel:[0,0,0] op_sel_hi:[1,1,0] neg_lo:[0,1,0] neg_hi:[0,1,0]\n\t"
    "v_mfma_f32_16x16x32_f16 v[76:79], a[188:191], a[224:227], v[76:79]\n\t"
    "v_pk_maximum3_f16 v251, v228, v228, s29 op_sel:[0,0,0] op_sel_hi:[1,1,0] neg_lo:[0,1,0] neg_hi:[0,1,0]\n\t"
    "v_pk_maximum3_f16 v249, v240, v241, v249\n\t"
    "v_mfma_f32_16x16x32_f16 v[76:79], a[184:187], a[220:223], v[76:79]\n\t"
    "v_pk_maximum3_f16 v249, v250, v251, v249\n\t"
    "v_pk_mad_u16 v240, v240, 8, s29 op_sel:[0,0,1] op_sel_hi:[1,0,1]\n\t"
    "v_mfma_f32_16x16x32_f16 v[76:79], a[180:183], a[216:219], v[76:79]\n\t"
    "v_pk_mad_u16 v241, v241, 8, s29 op_sel:[0,0,1] op_sel_hi:[1,0,1]\n\t"
    "v_mfma_f32_16x16x32_f16 v[76:79], a[176:179], a[212:215], v[76:79]\n\t"
    "ds_read_b128 a[212:215], v245 offset:17952\n\t"
    "v_pk_mad_u16 v250, v250, 8, s29 op_sel:[0,0,1] op_sel_hi:[1,0,1]\n\t"
    "v_mfma_f32_16x16x32_f16 v[84:87], a[176:179], a[216:219], v[84:87]\n\t"
    "ds_read_b128 a[216:219], v245 offset:17984\n\t"
    "v_pk_mad_u16 v251, v251, 8, s29 op_sel:[0,0,1] op_sel_hi:[1,0,1]\n\t"
    "v_mfma_f32_16x16x32_f16 v[84:87], a[180:183], a[220:223], v[84:87]\n\t"
    "global_load_dwordx4 a[164:167], v242, s[16:17] offset:1024 sc1\n\t"
    "v_mfma_f32_16x16x32_f16 v[84:87], a[184:187], a[224:227], v[84:87]\n\t"
    "v_bfi_b32 v216, s24, v240, v216\n\t"
    "v_bfi_b32 v220, s24, v241, v220\n\t"
    "v_mfma_f32_16x16x32_f16 v[84:87], a[188:191], a[228:231], v[84:87]\n\t"
    "v_bfi_b32 v224, s24, v250, v224\n\t"
    "v_bfi_b32 v228, s24, v251, v228\n\t"
    "v_mfma_f32_16x16x32_f16 v[92:95], a[188:191], a[232:235], v[92:95]\n\t"
    "v_pk_add_f16 v240, v216, v224\n\t"
    "v_pk_add_f16 v241, v220, v228\n\t"
    "v_mfma_f32_16x16x32_f16 v[92:95], a[184:187], a[228:231], v[92:95]\n\t"
    "v_pk_fma_f16 v252, v228, 0.5, v224 op_sel_hi:[1,0,1]\n\t"
    "v_mfma_f32_16x16x32_f16 v[92:95], a[180:183], a[224:227], v[92:95]\n\t"
    "v_pk_add_f16 v250, v240, v241\n\t"
    "v_pk_add_f16 v251, v240, v241 neg_lo:[0,1] neg_hi:[0,1]\n\t"
    "v_mfma_f32_16x16x32_f16 v[92:95], a[176:179], a[220:223], v[92:95]\n\t"
    "ds_read_b128 a[220:223], v245 offset:18016\n\t"
    "ds_write_addtid_b32 v250 offset:0\n\t"
    "v_mfma_f32_16x16x32_f16 v[100:103], a[176:179], a[224:227], v[100:103]\n\t"
    "ds_read_b128 a[224:227], v245 offset:18048\n\t"
    "v_pk_fma_f16 v240, v224, 4.0, v216 op_sel_hi:[1,0,1]\n\t"
    "v_mfma_f32_16x16x32_f16 v[100:103], a[180:183], a[228:231], v[100:103]\n\t"
    "global_load_dwordx4 a[168:171], v242, s[16:17] offset:2048 sc1\n\t"
    "ds_write_addtid_b32 v251 offset:8960\n\t"
    "v_mfma_f32_16x16x32_f16 v[100:103], a[184:187], a[232:235], v[100:103]\n\t"
    "v_pk_fma_f16 v241, v228, 4.0, v220 op_sel_hi:[1,0,1]\n\t"
    "v_pk_fma_f16 v252, v252, 0.5, v220 op_sel_hi:[1,0,1]\n\t"
    "v_mfma_f32_16x16x32_f16 v[100:103], a[188:191], a[236:239], v[100:103]\n\t"
    "v_pk_fma_f16 v250, v241, 2.0, v240 op_sel_hi:[1,0,1]\n\t"
    "v_mfma_f32_16x16x32_f16 v[108:111], a[188:191], a[240:243], v[108:111]\n\t"
    "v_pk_fma_f16 v251, v241, -2.0, v240 op_sel_hi:[1,0,1]\n\t"
    "v_mfma_f32_16x16x32_f16 v[108:111], a[184:187], a[236:239], v[108:111]\n\t"
    "ds_write_addtid_b32 v250 offset:17920\n\t"
    "v_pk_fma_f16 v252, v252, 0.5, v216 op_sel_hi:[1,0,1]\n\t"
    "v_mfma_f32_16x16x32_f16 v[108:111], a[180:183], a[232:235], v[108:111]\n\t"
    "ds_write_addtid_b32 v251 offset:26880\n\t"
    "v_mfma_f32_16x16x32_f16 v[108:111], a[176:179], a[228:231], v[108:111]\n\t"
    "ds_read_b128 a[228:231], v245 offset:18080\n\t"
    "ds_write_addtid_b32 v252 offset:35840\n\t"
    "v_mfma_f32_16x16x32_f16 v[116:119], a[176:179], a[232:235], v[116:119]\n\t"
    "ds_read_b128 a[232:235], v245 offset:18112\n\t"
    "s_waitcnt vmcnt(10)\n\t"
    "v_mfma_f32_16x16x32_f16 v[116:119], a[180:183], a[236:239], v[116:119]\n\t"
    "global_load_dwordx4 a[172:175], v242, s[16:17] offset:3072 sc1\n\t"
    "s_nop 0\n\t"
    "v_mfma_f32_16x16x32_f16 v[116:119], a[184:187], a[240:243], v[116:119]\n\t"
    "v_add_u32 v242, 8192, v242\n\t"
    "v_mov_b32_e64 v200, v232\n\t"
    "v_mfma_f32_16x16x32_f16 v[116:119], a[188:191], a[244:247], v[116:119]\n\t"
    "v_mov_b32 v201, v233\n\t"
    "v_mov_b32 v202, v234\n\t"
    "v_mfma_f32_16x16x32_f16 v[124:127], a[188:191], a[248:251], v[124:127]\n\t"
    "ds_read_b128 a[248:251], v246 offset:17984\n\t"
    "v_mov_b32_e64 v203, v235\n\t"
    "v_mfma_f32_16x16x32_f16 v[124:127], a[184:187], a[244:247], v[124:127]\n\t"
    "ds_read_b128 a[244:247], v246 offset:17952\n\t"
    "v_mov_b32_e64 v204, v236\n\t"
    "v_mfma_f32_16x16x32_f16 v[124:127], a[180:183], a[240:243], v[124:127]\n\t"
    "ds_read_b128 a[240:243], v247 offset:17920\n\t"
    "v_mov_b32_e64 v205, v237\n\t"
    "v_mfma_f32_16x16x32_f16 v[124:127], a[176:179], a[236:239], v[124:127]\n\t"
    "s_waitcnt vmcnt(14) lgkmcnt(9)\n\t"
    "v_mov_b32 v206, v238\n\t"
    "v_mfma_f32_16x16x32_f16 a[0:3], a[192:195], a[208:211], a[0:3]\n\t"
    "ds_read_b128 a[236:239], v245 offset:18144\n\t"
    "v_mov_b32_e64 v207, v239\n\t"
    "v_mfma_f32_16x16x32_f16 a[0:3], a[196:199], a[212:215], a[0:3]\n\t"
    "buffer_load_dwordx4 v[232:235], v243, s[8:11], s25 offen offset:2048\n\t"
    "v_mfma_f32_16x16x32_f16 a[0:3], a[200:203], a[216:219], a[0:3]\n\t"
    "global_load_dwordx4 a[176:179], v242, s[16:17] offset:-4096 sc1\n\t"
    "v_mfma_f32_16x16x32_f16 a[0:3], a[204:207], a[220:223], a[0:3]\n\t"
    "v_pk_maximum3_f16 v240, v217, v217, s29 op_sel:[0,0,0] op_sel_hi:[1,1,0] neg_lo:[0,1,0] neg_hi:[0,1,0]\n\t"
    "v_pk_maximum3_f16 v241, v221, v221, s29 op_sel:[0,0,0] op_sel_hi:[1,1,0] neg_lo:[0,1,0] neg_hi:[0,1,0]\n\t"
    "v_mfma_f32_16x16x32_f16 a[8:11], a[204:207], a[224:227], a[8:11]\n\t"
    "v_pk_maximum3_f16 v250, v225, v225, s29 op_sel:[0,0,0] op_sel_hi:[1,1,0] neg_lo:[0,1,0] neg_hi:[0,1,0]\n\t"
    "v_pk_maximum3_f16 v251, v229, v229, s29 op_sel:[0,0,0] op_sel_hi:[1,1,0] neg_lo:[0,1,0] neg_hi:[0,1,0]\n\t"
    "v_mfma_f32_16x16x32_f16 a[8:11], a[200:203], a[220:223], a[8:11]\n\t"
    "v_pk_maximum3_f16 v249, v240, v241, v249\n\t"
    "v_mfma_f32_16x16x32_f16 a[8:11], a[196:199], a[216:219], a[8:11]\n\t"
    "buffer_load_dwordx4 v[208:211], v243, s[8:11], s25 offen offset:2112\n\t"
    "v_mfma_f32_16x16x32_f16 a[8:11], a[192:195], a[212:215], a[8:11]\n\t"
    "s_waitcnt lgkmcnt(4)\n\t"
    "v_pk_maximum3_f16 v249, v250, v251, v249\n\t"
    "v_mfma_f32_16x16x32_f16 a[16:19], a[192:195], a[216:219], a[16:19]\n\t"
    "v_pk_mad_u16 v240, v240, 8, s29 op_sel:[0,0,1] op_sel_hi:[1,0,1]\n\t"
    "v_pk_mad_u16 v241, v241, 8, s29 op_sel:[0,0,1] op_sel_hi:[1,0,1]\n\t"
    "v_mfma_f32_16x16x32_f16 a[16:19], a[196:199], a[220:223], a[16:19]\n\t"
    "global_load_dwordx4 a[180:183], v242, s[16:17] offset:-3072 sc1\n\t"
    "s_nop 0\n\t"
    "v_mfma_f32_16x16x32_f16 a[16:19], a[200:203], a[224:227], a[16:19]\n\t"
    "v_pk_mad_u16 v250, v250, 8, s29 op_sel:[0,0,1] op_sel_hi:[1,0,1]\n\t"
    "v_pk_mad_u16 v251, v251, 8, s29 op_sel:[0,0,1] op_sel_hi:[1,0,1]\n\t"
    "v_mfma_f32_16x16x32_f16 a[16:19], a[204:207], a[228:231], a[16:19]\n\t"
    "v_bfi_b32 v217, s24, v240, v217\n\t"
    "v_mfma_f32_16x16x32_f16 a[24:27], a[204:207], a[232:235], a[24:27]\n\t"
    "v_bfi_b32 v221, s24, v241, v221\n\t"
    "v_mfma_f32_16x16x32_f16 a[24:27], a[200:203], a[228:231], a[24:27]\n\t"
    "s_waitcnt lgkmcnt(0)\n\t"
    "s_nop 0\n\t"
    "v_mfma_f32_16x16x32_f16 a[24:27], a[196:199], a[224:227], a[24:27]\n\t"
    "v_bfi_b32 v225, s24, v250, v225\n\t"
    "v_mfma_f32_16x16x32_f16 a[24:27], a[192:195], a[220:223], a[24:27]\n\t"
    "v_bfi_b32 v229, s24, v251, v229\n\t"
    "v_mfma_f32_16x16x32_f16 a[32:35], a[192:195], a[224:227], a[32:35]\n\t"
    "global_load_dwordx4 a[184:187], v242, s[16:17] offset:-2048 sc1\n\t"
    "v_mfma_f32_16x16x32_f16 a[32:35], a[196:199], a[228:231], a[32:35]\n\t"
    "v_pk_add_f16 v240, v217, v225\n\t"
    "v_pk_add_f16 v241, v221, v229\n\t"
    "v_mfma_f32_16x16x32_f16 a[32:35], a[200:203], a[232:235], a[32:35]\n\t"
    "v_pk_fma_f16 v252, v229, 0.5, v225 op_sel_hi:[1,0,1]\n\t"
    "v_mfma_f32_16x16x32_f16 a[32:35], a[204:207], a[236:239], a[32:35]\n\t"
    "v_pk_add_f16 v250, v240, v241\n\t"
    "v_mfma_f32_16x16x32_f16 a[40:43], a[204:207], a[240:243], a[40:43]\n\t"
    "v_pk_add_f16 v251, v240, v241 neg_lo:[0,1] neg_hi:[0,1]\n\t"
    "v_mfma_f32_16x16x32_f16 a[40:43], a[200:203], a[236:239], a[40:43]\n\t"
    "ds_write_addtid_b32 v250 offset:256\n\t"
    "v_mfma_f32_16x16x32_f16 a[40:43], a[196:199], a[232:235], a[40:43]\n\t"
    "v_pk_fma_f16 v240, v225, 4.0, v217 op_sel_hi:[1,0,1]\n\t"
    "v_mfma_f32_16x16x32_f16 a[40:43], a[192:195], a[228:231], a[40:43]\n\t"
    "global_load_dwordx4 a[188:191], v242, s[16:17] offset:-1024 sc1\n\t"
    "ds_write_addtid_b32 v251 offset:9216\n\t"
    "v_mfma_f32_16x16x32_f16 a[48:51], a[192:195], a[232:235], a[48:51]\n\t"
    "v_pk_fma_f16 v241, v229, 4.0, v221 op_sel_hi:[1,0,1]\n\t"
    "v_mfma_f32_16x16x32_f16 a[48:51], a[196:199], a[236:239], a[48:51]\n\t"
    "v_mfma_f32_16x16x32_f16 a[48:51], a[200:203], a[240:243], a[48:51]\n\t"
    "v_pk_fma_f16 v252, v252, 0.5, v221 op_sel_hi:[1,0,1]\n\t"
    "v_mfma_f32_16x16x32_f16 a[48:51], a[204:207], a[244:247], a[48:51]\n\t"
    "v_pk_fma_f16 v250, v241, 2.0, v240 op_sel_hi:[1,0,1]\n\t"
    "v_mfma_f32_16x16x32_f16 a[56:59], a[204:207], a[248:251], a[56:59]\n\t"
    "v_pk_fma_f16 v251, v241, -2.0, v240 op_sel_hi:[1,0,1]\n\t"
    "v_mfma_f32_16x16x32_f16 a[56:59], a[200:203], a[244:247], a[56:59]\n\t"
    "ds_write_addtid_b32 v250 offset:18176\n\t"
    "v_mfma_f32_16x16x32_f16 a[56:59], a[196:199], a[240:243], a[56:59]\n\t"
    "v_pk_fma_f16 v252, v252, 0.5, v217 op_sel_hi:[1,0,1]\n\t"
    "v_mfma_f32_16x16x32_f16 a[56:59], a[192:195], a[236:239], a[56:59]\n\t"
    "s_waitcnt vmcnt(14)\n\t"
    "ds_write_addtid_b32 v251 offset:27136\n\t"
    "v_mfma_f32_16x16x32_f16 a[4:7], a[128:131], a[208:211], a[4:7]\n\t"
    "ds_read_b128 a[208:211], v245 offset:26880\n\t"
    "ds_write_addtid_b32 v252 offset:36096\n\t"
    "v_mfma_f32_16x16x32_f16 a[4:7], a[132:135], a[212:215], a[4:7]\n\t"
    "v_pk_maximum3_f16 v240, v218, v218, s29 op_sel:[0,0,0] op_sel_hi:[1,1,0] neg_lo:[0,1,0] neg_hi:[0,1,0]\n\t"
    "s_nop 0\n\t"
    "v_mfma_f32_16x16x32_f16 a[4:7], a[136:139], a[216:219], a[4:7]\n\t"
    "global_load_dwordx4 a[192:195], v242, s[16:17] offset:0 sc1\n\t"
    "v_mfma_f32_16x16x32_f16 a[4:7], a[140:143], a[220:223], a[4:7]\n\t"
    "v_pk_maximum3_f16 v241, v222, v222, s29 op_sel:[0,0,0] op_sel_hi:[1,1,0] neg_lo:[0,1,0] neg_hi:[0,1,0]\n\t"
    "v_pk_maximum3_f16 v250, v226, v226, s29 op_sel:[0,0,0] op_sel_hi:[1,1,0] neg_lo:[0,1,0] neg_hi:[0,1,0]\n\t"
    "v_mfma_f32_16x16x32_f16 a[12:15], a[140:143], a[224:227], a[12:15]\n\t"
    "v_pk_maximum3_f16 v251, v230, v230, s29 op_sel:[0,0,0] op_sel_hi:[1,1,0] neg_lo:[0,1,0] neg_hi:[0,1,0]\n\t"
    "v_mfma_f32_16x16x32_f16 a[12:15], a[136:139], a[220:223], a[12:15]\n\t"
    "v_mfma_f32_16x16x32_f16 a[12:15], a[132:135], a[216:219], a[12:15]\n\t"
    "v_pk_maximum3_f16 v249, v240, v241, v249\n\t"
    "v_mfma_f32_16x16x32_f16 a[12:15], a[128:131], a[212:215], a[12:15]\n\t"
    "ds_read_b128 a[212:215], v245 offset:26912\n\t"
    "v_pk_maximum3_f16 v249, v250, v251, v249\n\t"
    "v_mfma_f32_16x16x32_f16 a[20:23], a[128:131], a[216:219], a[20:23]\n\t"
    "ds_read_b128 a[216:219], v245 offset:26944\n\t"
    "v_pk_mad_u16 v240, v240, 8, s29 op_sel:[0,0,1] op_sel_hi:[1,0,1]\n\t"
    "v_mfma_f32_16x16x32_f16 a[20:23], a[132:135], a[220:223], a[20:23]\n\t"
    "global_load_dwordx4 a[196:199], v242, s[16:17] offset:1024 sc1\n\t"
    "v_mfma_f32_16x16x32_f16 a[20:23], a[136:139], a[224:227], a[20:23]\n\t"
    "v_pk_mad_u16 v241, v241, 8, s29 op_sel:[0,0,1] op_sel_hi:[1,0,1]\n\t"
    "v_pk_mad_u16 v250, v250, 8, s29 op_sel:[0,0,1] op_sel_hi:[1,0,1]\n\t"
    "v_mfma_f32_16x16x32_f16 a[20:23], a[140:143], a[228:231], a[20:23]\n\t"
    "v_pk_mad_u16 v251, v251, 8, s29 op_sel:[0,0,1] op_sel_hi:[1,0,1]\n\t"
    "v_mfma_f32_16x16x32_f16 a[28:31], a[140:143], a[232:235], a[28:31]\n\t"
    "v_bfi_b32 v218, s24, v240, v218\n\t"
    "v_mfma_f32_16x16x32_f16 a[28:31], a[136:139], a[228:231], a[28:31]\n\t"
    "v_bfi_b32 v222, s24, v241, v222\n\t"
    "v_mfma_f32_16x16x32_f16 a[28:31], a[132:135], a[224:227], a[28:31]\n\t"
    "v_bfi_b32 v226, s24, v250, v226\n\t"
    "v_mfma_f32_16x16x32_f16 a[28:31], a[128:131], a[220:223], a[28:31]\n\t"
    "ds_read_b128 a[220:223], v245 offset:26976\n\t"
    "v_bfi_b32 v230, s24, v251, v230\n\t"
    "v_mfma_f32_16x16x32_f16 a[36:39], a[128:131], a[224:227], a[36:39]\n\t"
    "ds_read_b128 a[224:227], v245 offset:27008\n\t"
    "v_pk_add_f16 v240, v218, v226\n\t"
    "v_mfma_f32_16x16x32_f16 a[36:39], a[132:135], a[228:231], a[36:39]\n\t"
    "global_load_dwordx4 a[200:203], v242, s[16:17] offset:2048 sc1\n\t"
    "v_mfma_f32_16x16x32_f16 a[36:39], a[136:139], a[232:235], a[36:39]\n\t"
    "buffer_load_dwordx4 v[236:239], v243, s[8:11], s25 offen offset:3072\n\t"
    "v_mfma_f32_16x16x32_f16 a[36:39], a[140:143], a[236:239], a[36:39]\n\t"
    "v_pk_add_f16 v241, v222, v230\n\t"
    "v_pk_fma_f16 v252, v230, 0.5, v226 op_sel_hi:[1,0,1]\n\t"
    "v_mfma_f32_16x16x32_f16 a[44:47], a[140:143], a[240:243], a[44:47]\n\t"
    "v_pk_add_f16 v250, v240, v241\n\t"
    "v_mfma_f32_16x16x32_f16 a[44:47], a[136:139], a[236:239], a[44:47]\n\t"
    "v_pk_add_f16 v251, v240, v241 neg_lo:[0,1] neg_hi:[0,1]\n\t"
    "v_mfma_f32_16x16x32_f16 a[44:47], a[132:135], a[232:235], a[44:47]\n\t"
    "ds_write_addtid_b32 v250 offset:512\n\t"
    "v_mfma_f32_16x16x32_f16 a[44:47], a[128:131], a[228:231], a[44:47]\n\t"
    "ds_read_b128 a[228:231], v245 offset:27040\n\t"
    "v_pk_fma_f16 v240, v226, 4.0, v218 op_sel_hi:[1,0,1]\n\t"
    "v_mfma_f32_16x16x32_f16 a[52:55], a[128:131], a[232:235], a[52:55]\n\t"
    "ds_read_b128 a[232:235], v245 offset:27072\n\t"
    "ds_write_addtid_b32 v251 offset:9472\n\t"
    "v_mfma_f32_16x16x32_f16 a[52:55], a[132:135], a[236:239], a[52:55]\n\t"
    "global_load_dwordx4 a[204:207], v242, s[16:17] offset:3072 sc1\n\t"
    "v_mfma_f32_16x16x32_f16 a[52:55], a[136:139], a[240:243], a[52:55]\n\t"
    "v_add_u32 v242, 8192, v242\n\t"
    "v_pk_fma_f16 v241, v230, 4.0, v222 op_sel_hi:[1,0,1]\n\t"
    "v_mfma_f32_16x16x32_f16 a[52:55], a[140:143], a[244:247], a[52:55]\n\t"
    "buffer_load_dwordx4 v[212:215], v243, s[8:11], s25 offen offset:3136\n\t"
    "v_mfma_f32_16x16x32_f16 a[60:63], a[140:143], a[248:251], a[60:63]\n\t"
    "ds_read_b128 a[248:251], v246 offset:26944\n\t"
    "v_pk_fma_f16 v252, v252, 0.5, v222 op_sel_hi:[1,0,1]\n\t"
    "v_mfma_f32_16x16x32_f16 a[60:63], a[136:139], a[244:247], a[60:63]\n\t"
    "ds_read_b128 a[244:247], v246 offset:26912\n\t"
    "v_pk_fma_f16 v250, v241, 2.0, v240 op_sel_hi:[1,0,1]\n\t"
    "v_mfma_f32_16x16x32_f16 a[60:63], a[132:135], a[240:243], a[60:63]\n\t"
    "ds_read_b128 a[240:243], v247 offset:26880\n\t"
    "v_pk_fma_f16 v251, v241, -2.0, v240 op_sel_hi:[1,0,1]\n\t"
    "v_mfma_f32_16x16x32_f16 a[60:63], a[128:131], a[236:239], a[60:63]\n\t"
    "s_waitcnt vmcnt(16) lgkmcnt(7)\n\t"
    "ds_write_addtid_b32 v250 offset:18432\n\t"
    "v_mfma_f32_16x16x32_f16 a[64:67], a[144:147], a[208:211], a[64:67]\n\t"
    "ds_read_b128 a[236:239], v245 offset:27104\n\t"
    "v_pk_fma_f16 v252, v252, 0.5, v218 op_sel_hi:[1,0,1]\n\t"
    "v_mfma_f32_16x16x32_f16 a[64:67], a[148:151], a[212:215], a[64:67]\n\t"
    "ds_write_addtid_b32 v251 offset:27392\n\t"
    "s_nop 0\n\t"
    "v_mfma_f32_16x16x32_f16 a[64:67], a[152:155], a[216:219], a[64:67]\n\t"
    "global_load_dwordx4 a[128:131], v242, s[16:17] offset:-4096 sc1\n\t"
    "ds_write_addtid_b32 v252 offset:36352\n\t"
    "v_mfma_f32_16x16x32_f16 a[64:67], a[156:159], a[220:223], a[64:67]\n\t"
    "v_pk_maximum3_f16 v240, v219, v219, s29 op_sel:[0,0,0] op_sel_hi:[1,1,0] neg_lo:[0,1,0] neg_hi:[0,1,0]\n\t"
    "v_pk_maximum3_f16 v241, v223, v223, s29 op_sel:[0,0,0] op_sel_hi:[1,1,0] neg_lo:[0,1,0] neg_hi:[0,1,0]\n\t"
    "v_mfma_f32_16x16x32_f16 a[72:75], a[156:159], a[224:227], a[72:75]\n\t"
    "v_pk_maximum3_f16 v250, v227, v227, s29 op_sel:[0,0,0] op_sel_hi:[1,1,0] neg_lo:[0,1,0] neg_hi:[0,1,0]\n\t"
    "v_mfma_f32_16x16x32_f16 a[72:75], a[152:155], a[220:223], a[72:75]\n\t"
    "v_pk_maximum3_f16 v251, v231, v231, s29 op_sel:[0,0,0] op_sel_hi:[1,1,0] neg_lo:[0,1,0] neg_hi:[0,1,0]\n\t"
    "v_mfma_f32_16x16x32_f16 a[72:75], a[148:151], a[216:219], a[72:75]\n\t"
    "v_pk_maximum3_f16 v249, v240, v241, v249\n\t"
    "v_mfma_f32_16x16x32_f16 a[72:75], a[144:147], a[212:215], a[72:75]\n\t"
    "s_waitcnt lgkmcnt(8)\n\t"
    "v_pk_maximum3_f16 v249, v250, v251, v249\n\t"
    "v_mfma_f32_16x16x32_f16 a[80:83], a[144:147], a[216:219], a[80:83]\n\t"
    "v_pk_mad_u16 v240, v240, 8, s29 op_sel:[0,0,1] op_sel_hi:[1,0,1]\n\t"
    "s_nop 0\n\t"
    "v_mfma_f32_16x16x32_f16 a[80:83], a[148:151], a[220:223], a[80:83]\n\t"
    "global_load_dwordx4 a[132:135], v242, s[16:17] offset:-3072 sc1\n\t"
    "v_mfma_f32_16x16x32_f16 a[80:83], a[152:155], a[224:227], a[80:83]\n\t"
    "v_pk_mad_u16 v241, v241, 8, s29 op_sel:[0,0,1] op_sel_hi:[1,0,1]\n\t"
    "v_mfma_f32_16x16x32_f16 a[80:83], a[156:159], a[228:231], a[80:83]\n\t"
    "v_pk_mad_u16 v250, v250, 8, s29 op_sel:[0,0,1] op_sel_hi:[1,0,1]\n\t"
    "v_mfma_f32_16x16x32_f16 a[88:91], a[156:159], a[232:235], a[88:91]\n\t"
    "v_pk_mad_u16 v251, v251, 8, s29 op_sel:[0,0,1] op_sel_hi:[1,0,1]\n\t"
    "v_mfma_f32_16x16x32_f16 a[88:91], a[152:155], a[228:231], a[88:91]\n\t"
    "s_waitcnt lgkmcnt(2)\n\t"
    "v_bfi_b32 v219, s24, v240, v219\n\t"
    "v_mfma_f32_16x16x32_f16 a[88:91], a[148:151], a[224:227], a[88:91]\n\t"
    "v_bfi_b32 v223, s24, v241, v223\n\t"
    "s_nop 0\n\t"
    "v_mfma_f32_16x16x32_f16 a[88:91], a[144:147], a[220:223], a[88:91]\n\t"
    "v_bfi_b32 v227, s24, v250, v227\n\t"
    "v_mfma_f32_16x16x32_f16 a[96:99], a[144:147], a[224:227], a[96:99]\n\t"
    "global_load_dwordx4 a[136:139], v242, s[16:17] offset:-2048 sc1\n\t"
    "v_mfma_f32_16x16x32_f16 a[96:99], a[148:151], a[228:231], a[96:99]\n\t"
    "v_bfi_b32 v231, s24, v251, v231\n\t"
    "v_pk_add_f16 v240, v219, v227\n\t"
    "v_mfma_f32_16x16x32_f16 a[96:99], a[152:155], a[232:235], a[96:99]\n\t"
    "v_pk_add_f16 v241, v223, v231\n\t"
    "v_mfma_f32_16x16x32_f16 a[96:99], a[156:159], a[236:239], a[96:99]\n\t"
    "v_pk_fma_f16 v252, v231, 0.5, v227 op_sel_hi:[1,0,1]\n\t"
    "v_mfma_f32_16x16x32_f16 a[104:107], a[156:159], a[240:243], a[104:107]\n\t"
    "v_pk_add_f16 v250, v240, v241\n\t"
    "v_mfma_f32_16x16x32_f16 a[104:107], a[152:155], a[236:239], a[104:107]\n\t"
    "v_pk_add_f16 v251, v240, v241 neg_lo:[0,1] neg_hi:[0,1]\n\t"
    "v_mfma_f32_16x16x32_f16 a[104:107], a[148:151], a[232:235], a[104:107]\n\t"
    "v_mfma_f32_16x16x32_f16 a[104:107], a[144:147], a[228:231], a[104:107]\n\t"
    "global_load_dwordx4 a[140:143], v242, s[16:17] offset:-1024 sc1\n\t"
    "ds_write_addtid_b32 v250 offset:768\n\t"
    "v_mfma_f32_16x16x32_f16 a[112:115], a[144:147], a[232:235], a[112:115]\n\t"
    "v_pk_fma_f16 v240, v227, 4.0, v219 op_sel_hi:[1,0,1]\n\t"
    "v_mfma_f32_16x16x32_f16 a[112:115], a[148:151], a[236:239], a[112:115]\n\t"
    "ds_write_addtid_b32 v251 offset:9728\n\t"
    "v_mfma_f32_16x16x32_f16 a[112:115], a[152:155], a[240:243], a[112:115]\n\t"
    "v_pk_fma_f16 v241, v231, 4.0, v223 op_sel_hi:[1,0,1]\n\t"
    "v_mfma_f32_16x16x32_f16 a[112:115], a[156:159], a[244:247], a[112:115]\n\t"
    "v_pk_fma_f16 v252, v252, 0.5, v223 op_sel_hi:[1,0,1]\n\t"
    "v_mfma_f32_16x16x32_f16 a[120:123], a[156:159], a[248:251], a[120:123]\n\t"
    "v_pk_fma_f16 v250, v241, 2.0, v240 op_sel_hi:[1,0,1]\n\t"
    "v_mfma_f32_16x16x32_f16 a[120:123], a[152:155], a[244:247], a[120:123]\n\t"
    "v_pk_fma_f16 v251, v241, -2.0, v240 op_sel_hi:[1,0,1]\n\t"
    "v_mfma_f32_16x16x32_f16 a[120:123], a[148:151], a[240:243], a[120:123]\n\t"
    "ds_write_addtid_b32 v250 offset:18688\n\t"
    "v_mfma_f32_16x16x32_f16 a[120:123], a[144:147], a[236:239], a[120:123]\n\t"
    "s_waitcnt vmcnt(16)\n\t"
    "v_pk_fma_f16 v252, v252, 0.5, v219 op_sel_hi:[1,0,1]\n\t"
    "v_mfma_f32_16x16x32_f16 a[68:71], a[160:163], a[208:211], a[68:71]\n\t"
    "ds_read_b128 a[208:211], v245 offset:35840\n\t"
    "ds_write_addtid_b32 v251 offset:27648\n\t"
    "v_mfma_f32_16x16x32_f16 a[68:71], a[164:167], a[212:215], a[68:71]\n\t"
    "ds_write_addtid_b32 v252 offset:36608\n\t"
    "s_nop 0\n\t"
    "v_mfma_f32_16x16x32_f16 a[68:71], a[168:171], a[216:219], a[68:71]\n\t"
    "global_load_dwordx4 a[144:147], v242, s[16:17] offset:0 sc1\n\t"
    "v_mfma_f32_16x16x32_f16 a[68:71], a[172:175], a[220:223], a[68:71]\n\t"
    "buffer_load_dwordx4 v[224:227], v244, s[8:11], s25 offen offset:0\n\t"
    "v_mfma_f32_16x16x32_f16 a[76:79], a[172:175], a[224:227], a[76:79]\n\t"
    "s_waitcnt vmcnt(36)\n\t"
    "v_pk_maximum3_f16 v240, v128, v128, s29 op_sel:[0,0,0] op_sel_hi:[1,1,0] neg_lo:[0,1,0] neg_hi:[0,1,0]\n\t"
    "v_mfma_f32_16x16x32_f16 a[76:79], a[168:171], a[220:223], a[76:79]\n\t"
    "v_pk_maximum3_f16 v241, v129, v129, s29 op_sel:[0,0,0] op_sel_hi:[1,1,0] neg_lo:[0,1,0] neg_hi:[0,1,0]\n\t"
    "s_nop 0\n\t"
    "v_mfma_f32_16x16x32_f16 a[76:79], a[164:167], a[216:219], a[76:79]\n\t"
    "v_pk_maximum3_f16 v250, v130, v130, s29 op_sel:[0,0,0] op_sel_hi:[1,1,0] neg_lo:[0,1,0] neg_hi:[0,1,0]\n\t"
    "v_mfma_f32_16x16x32_f16 a[76:79], a[160:163], a[212:215], a[76:79]\n\t"
    "ds_read_b128 a[212:215], v245 offset:35872\n\t"
    "v_pk_maximum3_f16 v251, v131, v131, s29 op_sel:[0,0,0] op_sel_hi:[1,1,0] neg_lo:[0,1,0] neg_hi:[0,1,0]\n\t"
    "v_mfma_f32_16x16x32_f16 a[84:87], a[160:163], a[216:219], a[84:87]\n\t"
    "ds_read_b128 a[216:219], v245 offset:35904\n\t"
    "v_mfma_f32_16x16x32_f16 a[84:87], a[164:167], a[220:223], a[84:87]\n\t"
    "global_load_dwordx4 a[148:151], v242, s[16:17] offset:1024 sc1\n\t"
    "v_mfma_f32_16x16x32_f16 a[84:87], a[168:171], a[224:227], a[84:87]\n\t"
    "buffer_load_dwordx4 v[216:219], v244, s[8:11], s25 offen offset:64\n\t"
    "v_mfma_f32_16x16x32_f16 a[84:87], a[172:175], a[228:231], a[84:87]\n\t"
    "v_pk_maximum3_f16 v249, v240, v241, v249\n\t"
    "v_pk_maximum3_f16 v249, v250, v251, v249\n\t"
    "v_mfma_f32_16x16x32_f16 a[92:95], a[172:175], a[232:235], a[92:95]\n\t"
    "v_pk_mad_u16 v240, v240, 8, s29 op_sel:[0,0,1] op_sel_hi:[1,0,1]\n\t"
    "v_mfma_f32_16x16x32_f16 a[92:95], a[168:171], a[228:231], a[92:95]\n\t"
    "v_pk_mad_u16 v241, v241, 8, s29 op_sel:[0,0,1] op_sel_hi:[1,0,1]\n\t"
    "v_mfma_f32_16x16x32_f16 a[92:95], a[164:167], a[224:227], a[92:95]\n\t"
    "v_mfma_f32_16x16x32_f16 a[92:95], a[160:163], a[220:223], a[92:95]\n\t"
    "ds_read_b128 a[220:223], v245 offset:35936\n\t"
    "v_pk_mad_u16 v250, v250, 8, s29 op_sel:[0,0,1] op_sel_hi:[1,0,1]\n\t"
    "v_mfma_f32_16x16x32_f16 a[100:103], a[160:163], a[224:227], a[100:103]\n\t"
    "ds_read_b128 a[224:227], v245 offset:35968\n\t"
    "v_pk_mad_u16 v251, v251, 8, s29 op_sel:[0,0,1] op_sel_hi:[1,0,1]\n\t"
    "v_mfma_f32_16x16x32_f16 a[100:103], a[164:167], a[228:231], a[100:103]\n\t"
    "global_load_dwordx4 a[152:155], v242, s[16:17] offset:2048 sc1\n\t"
    "v_mfma_f32_16x16x32_f16 a[100:103], a[168:171], a[232:235], a[100:103]\n\t"
    "v_bfi_b32 v128, s24, v240, v128\n\t"
    "v_mfma_f32_16x16x32_f16 a[100:103], a[172:175], a[236:239], a[100:103]\n\t"
    "v_bfi_b32 v129, s24, v241, v129\n\t"
    "v_mfma_f32_16x16x32_f16 a[108:111], a[172:175], a[240:243], a[108:111]\n\t"
    "v_mfma_f32_16x16x32_f16 a[108:111], a[168:171], a[236:239], a[108:111]\n\t"
    "v_bfi_b32 v130, s24, v250, v130\n\t"
    "v_mfma_f32_16x16x32_f16 a[108:111], a[164:167], a[232:235], a[108:111]\n\t"
    "v_bfi_b32 v131, s24, v251, v131\n\t"
    "v_mfma_f32_16x16x32_f16 a[108:111], a[160:163], a[228:231], a[108:111]\n\t"
    "ds_read_b128 a[228:231], v245 offset:36000\n\t"
    "v_mfma_f32_16x16x32_f16 a[116:119], a[160:163], a[232:235], a[116:119]\n\t"
    "ds_read_b128 a[232:235], v245 offset:36032\n\t"
    "s_waitcnt vmcnt(34)\n\t"
    "v_mfma_f32_16x16x32_f16 a[116:119], a[164:167], a[236:239], a[116:119]\n\t"
    "global_load_dwordx4 a[156:159], v242, s[16:17] offset:3072 sc1\n\t"
    "s_nop 0\n\t"
    "v_mfma_f32_16x16x32_f16 a[116:119], a[168:171], a[240:243], a[116:119]\n\t"
    "v_add_u32 v242, 8192, v242\n\t"
    "v_pk_maximum3_f16 v240, v132, v132, s29 op_sel:[0,0,0] op_sel_hi:[1,1,0] neg_lo:[0,1,0] neg_hi:[0,1,0]\n\t"
    "v_mfma_f32_16x16x32_f16 a[116:119], a[172:175], a[244:247], a[116:119]\n\t"
    "v_pk_maximum3_f16 v241, v133, v133, s29 op_sel:[0,0,0] op_sel_hi:[1,1,0] neg_lo:[0,1,0] neg_hi:[0,1,0]\n\t"
    "v_mfma_f32_16x16x32_f16 a[124:127], a[172:175], a[248:251], a[124:127]\n\t"
    "ds_read_b128 a[248:251], v246 offset:35904\n\t"
    "v_pk_maximum3_f16 v250, v134, v134, s29 op_sel:[0,0,0] op_sel_hi:[1,1,0] neg_lo:[0,1,0] neg_hi:[0,1,0]\n\t"
    "v_mfma_f32_16x16x32_f16 a[124:127], a[168:171], a[244:247], a[124:127]\n\t"
    "ds_read_b128 a[244:247], v246 offset:35872\n\t"
    "v_pk_maximum3_f16 v251, v135, v135, s29 op_sel:[0,0,0] op_sel_hi:[1,1,0] neg_lo:[0,1,0] neg_hi:[0,1,0]\n\t"
    "v_mfma_f32_16x16x32_f16 a[124:127], a[164:167], a[240:243], a[124:127]\n\t"
    "ds_read_b128 a[240:243], v247 offset:35840\n\t"
    "v_mfma_f32_16x16x32_f16 a[124:127], a[160:163], a[236:239], a[124:127]\n\t"
    "s_waitcnt vmcnt(16) lgkmcnt(5)\n\t"
    "v_pk_maximum3_f16 v249, v240, v241, v249\n\t"
    "v_mfma_f32_16x16x32_f16 v[136:139], a[176:179], a[208:211], v[136:139]\n\t"
    "ds_read_b128 a[236:239], v245 offset:36064\n\t"
    "v_pk_maximum3_f16 v249, v250, v251, v249\n\t"
    "v_mfma_f32_16x16x32_f16 v[136:139], a[180:183], a[212:215], v[136:139]\n\t"
    "s_nop 0\n\t"
    "v_mfma_f32_16x16x32_f16 v[136:139], a[184:187], a[216:219], v[136:139]\n\t"
    "global_load_dwordx4 a[160:163], v242, s[16:17] offset:-4096 sc1\n\t"
    "v_mfma_f32_16x16x32_f16 v[136:139], a[188:191], a[220:223], v[136:139]\n\t"
    "v_pk_mad_u16 v240, v240, 8, s29 op_sel:[0,0,1] op_sel_hi:[1,0,1]\n\t"
    "v_pk_mad_u16 v241, v241, 8, s29 op_sel:[0,0,1] op_sel_hi:[1,0,1]\n\t"
    "v_mfma_f32_16x16x32_f16 v[144:147], a[188:191], a[224:227], v[144:147]\n\t"
    "v_add_u32_e64 v245, s26, v245\n\t"
    "v_mfma_f32_16x16x32_f16 v[144:147], a[184:187], a[220:223], v[144:147]\n\t"
    "v_pk_mad_u16 v250, v250, 8, s29 op_sel:[0,0,1] op_sel_hi:[1,0,1]\n\t"
    "v_mfma_f32_16x16x32_f16 v[144:147], a[180:183], a[216:219], v[144:147]\n\t"
    "v_pk_mad_u16 v251, v251, 8, s29 op_sel:[0,0,1] op_sel_hi:[1,0,1]\n\t"
    "v_mfma_f32_16x16x32_f16 v[144:147], a[176:179], a[212:215], v[144:147]\n\t"
    "s_waitcnt lgkmcnt(4)\n\t"
    "s_nop 0\n\t"
    "v_mfma_f32_16x16x32_f16 v[152:155], a[176:179], a[216:219], v[152:155]\n\t"
    "v_add_u32_e64 v246, s26, v246\n\t"
    "v_bfi_b32 v132, s24, v240, v132\n\t"
    "v_mfma_f32_16x16x32_f16 v[152:155], a[180:183], a[220:223], v[152:155]\n\t"
    "global_load_dwordx4 a[164:167], v242, s[16:17] offset:-3072 sc1\n\t"
    "v_mfma_f32_16x16x32_f16 v[152:155], a[184:187], a[224:227], v[152:155]\n\t"
    "v_bfi_b32 v133, s24, v241, v133\n\t"
    "v_mfma_f32_16x16x32_f16 v[152:155], a[188:191], a[228:231], v[152:155]\n\t"
    "v_bfi_b32 v134, s24, v250, v134\n\t"
    "v_mfma_f32_16x16x32_f16 v[160:163], a[188:191], a[232:235], v[160:163]\n\t"
    "v_bfi_b32 v135, s24, v251, v135\n\t"
    "v_mfma_f32_16x16x32_f16 v[160:163], a[184:187], a[228:231], v[160:163]\n\t"
    "s_waitcnt lgkmcnt(0)\n\t"
    "s_add_u32 s27, s27, 64\n\t"
    "v_mfma_f32_16x16x32_f16 v[160:163], a[180:183], a[224:227], v[160:163]\n\t"
    "s_waitcnt vmcnt(23)\n\t"
    "s_nop 0\n\t"
    "v_mfma_f32_16x16x32_f16 v[160:163], a[176:179], a[220:223], v[160:163]\n\t"
    "v_pk_maximum3_f16 v240, v232, v232, s29 op_sel:[0,0,0] op_sel_hi:[1,1,0] neg_lo:[0,1,0] neg_hi:[0,1,0]\n\t"
    "v_mfma_f32_16x16x32_f16 v[168:171], a[176:179], a[224:227], v[168:171]\n\t"
    "global_load_dwordx4 a[168:171], v242, s[16:17] offset:-2048 sc1\n\t"
    "v_mfma_f32_16x16x32_f16 v[168:171], a[180:183], a[228:231], v[168:171]\n\t"
    "v_pk_maximum3_f16 v241, v233, v233, s29 op_sel:[0,0,0] op_sel_hi:[1,1,0] neg_lo:[0,1,0] neg_hi:[0,1,0]\n\t"
    "v_mfma_f32_16x16x32_f16 v[168:171], a[184:187], a[232:235], v[168:171]\n\t"
    "s_sub_u32 s27, s27, s26\n\t"
    "v_pk_maximum3_f16 v250, v234, v234, s29 op_sel:[0,0,0] op_sel_hi:[1,1,0] neg_lo:[0,1,0] neg_hi:[0,1,0]\n\t"
    "v_mfma_f32_16x16x32_f16 v[168:171], a[188:191], a[236:239], v[168:171]\n\t"
    "v_pk_maximum3_f16 v251, v235, v235, s29 op_sel:[0,0,0] op_sel_hi:[1,1,0] neg_lo:[0,1,0] neg_hi:[0,1,0]\n\t"
    "s_nop 0\n\t"
    "v_mfma_f32_16x16x32_f16 v[176:179], a[188:191], a[240:243], v[176:179]\n\t"
    "v_mfma_f32_16x16x32_f16 v[176:179], a[184:187], a[236:239], v[176:179]\n\t"
    "v_pk_maximum3_f16 v249, v240, v241, v249\n\t"
    "v_mfma_f32_16x16x32_f16 v[176:179], a[180:183], a[232:235], v[176:179]\n\t"
    "v_pk_maximum3_f16 v249, v250, v251, v249\n\t"
    "v_mfma_f32_16x16x32_f16 v[176:179], a[176:179], a[228:231], v[176:179]\n\t"
    "global_load_dwordx4 a[172:175], v242, s[16:17] offset:-1024 sc1\n\t"
    "v_mfma_f32_16x16x32_f16 v[184:187], a[176:179], a[232:235], v[184:187]\n\t"
    "v_mad_i32_i24 v247, v248, s27, v246\n\t"
    "v_pk_mad_u16 v240, v240, 8, s29 op_sel:[0,0,1] op_sel_hi:[1,0,1]\n\t"
    "v_mfma_f32_16x16x32_f16 v[184:187], a[180:183], a[236:239], v[184:187]\n\t"
    "v_pk_mad_u16 v241, v241, 8, s29 op_sel:[0,0,1] op_sel_hi:[1,0,1]\n\t"
    "v_mfma_f32_16x16x32_f16 v[184:187], a[184:187], a[240:243], v[184:187]\n\t"
    "v_mfma_f32_16x16x32_f16 v[184:187], a[188:191], a[244:247], v[184:187]\n\t"
    "v_pk_mad_u16 v250, v250, 8, s29 op_sel:[0,0,1] op_sel_hi:[1,0,1]\n\t"
    "v_mfma_f32_16x16x32_f16 v[192:195], a[188:191], a[248:251], v[192:195]\n\t"
    "buffer_load_dwordx4 v[228:231], v244, s[8:11], s25 offen offset:1024\n\t"
    "v_mfma_f32_16x16x32_f16 v[192:195], a[184:187], a[244:247], v[192:195]\n\t"
    "v_pk_mad_u16 v251, v251, 8, s29 op_sel:[0,0,1] op_sel_hi:[1,0,1]\n\t"
    "v_mfma_f32_16x16x32_f16 v[192:195], a[180:183], a[240:243], v[192:195]\n\t"
    "v_bfi_b32 v232, s24, v240, v232\n\t"
    "v_mfma_f32_16x16x32_f16 v[192:195], a[176:179], a[236:239], v[192:195]\n\t"
    "s_waitcnt vmcnt(16) lgkmcnt(0)\n\t"
    "s_barrier\n\t"
    "v_mfma_f32_16x16x32_f16 v[140:143], a[192:195], a[208:211], v[140:143]\n\t"
    "ds_read_b128 a[208:211], v245 offset:0\n\t"
    "v_bfi_b32 v233, s24, v241, v233\n\t"
    "v_mfma_f32_16x16x32_f16 v[140:143], a[196:199], a[212:215], v[140:143]\n\t"
    "buffer_load_dwordx4 v[220:223], v244, s[8:11], s25 offen offset:1088\n\t"
    "v_mfma_f32_16x16x32_f16 v[140:143], a[200:203], a[216:219], v[140:143]\n\t"
    "global_load_dwordx4 a[176:179], v242, s[16:17] offset:0 sc1\n\t"
    "v_mfma_f32_16x16x32_f16 v[140:143], a[204:207], a[220:223], v[140:143]\n\t"
    "s_sub_u32 s30, s30, s26\n\t"
    "v_bfi_b32 v234, s24, v250, v234\n\t"
    "v_mfma_f32_16x16x32_f16 v[148:151], a[204:207], a[224:227], v[148:151]\n\t"
    "v_bfi_b32 v235, s24, v251, v235\n\t"
    "s_waitcnt vmcnt(19)\n\t"
    "v_mfma_f32_16x16x32_f16 v[148:151], a[200:203], a[220:223], v[148:151]\n\t"
    "v_pk_maximum3_f16 v240, v236, v236, s29 op_sel:[0,0,0] op_sel_hi:[1,1,0] neg_lo:[0,1,0] neg_hi:[0,1,0]\n\t"
    "v_mfma_f32_16x16x32_f16 v[148:151], a[196:199], a[216:219], v[148:151]\n\t"
    "v_pk_maximum3_f16 v241, v237, v237, s29 op_sel:[0,0,0] op_sel_hi:[1,1,0] neg_lo:[0,1,0] neg_hi:[0,1,0]\n\t"
    "v_mfma_f32_16x16x32_f16 v[148:151], a[192:195], a[212:215], v[148:151]\n\t"
    "ds_read_b128 a[212:215], v245 offset:32\n\t"
    "v_mfma_f32_16x16x32_f16 v[156:159], a[192:195], a[216:219], v[156:159]\n\t"
    "ds_read_b128 a[216:219], v245 offset:64\n\t"
    "v_pk_maximum3_f16 v250, v238, v238, s29 op_sel:[0,0,0] op_sel_hi:[1,1,0] neg_lo:[0,1,0] neg_hi:[0,1,0]\n\t"
    "v_mfma_f32_16x16x32_f16 v[156:159], a[196:199], a[220:223], v[156:159]\n\t"
    "global_load_dwordx4 a[180:183], v242, s[16:17] offset:1024 sc1\n\t"
    "v_mfma_f32_16x16x32_f16 v[156:159], a[200:203], a[224:227], v[156:159]\n\t"
    "v_pk_maximum3_f16 v251, v239, v239, s29 op_sel:[0,0,0] op_sel_hi:[1,1,0] neg_lo:[0,1,0] neg_hi:[0,1,0]\n\t"
    "v_mfma_f32_16x16x32_f16 v[156:159], a[204:207], a[228:231], v[156:159]\n\t"
    "v_pk_maximum3_f16 v249, v240, v241, v249\n\t"
    "v_mfma_f32_16x16x32_f16 v[164:167], a[204:207], a[232:235], v[164:167]\n\t"
    "v_pk_maximum3_f16 v249, v250, v251, v249\n\t"
    "v_mfma_f32_16x16x32_f16 v[164:167], a[200:203], a[228:231], v[164:167]\n\t"
    "v_mfma_f32_16x16x32_f16 v[164:167], a[196:199], a[224:227], v[164:167]\n\t"
    "s_sub_u32 s26, 0, s26\n\t"
    "v_pk_mad_u16 v240, v240, 8, s29 op_sel:[0,0,1] op_sel_hi:[1,0,1]\n\t"
    "v_mfma_f32_16x16x32_f16 v[164:167], a[192:195], a[220:223], v[164:167]\n\t"
    "ds_read_b128 a[220:223], v245 offset:96\n\t"
    "v_pk_mad_u16 v241, v241, 8, s29 op_sel:[0,0,1] op_sel_hi:[1,0,1]\n\t"
    "v_mfma_f32_16x16x32_f16 v[172:175], a[192:195], a[224:227], v[172:175]\n\t"
    "ds_read_b128 a[224:227], v245 offset:128\n\t"
    "s_nop 0\n\t"
    "v_mfma_f32_16x16x32_f16 v[172:175], a[196:199], a[228:231], v[172:175]\n\t"
    "global_load_dwordx4 a[184:187], v242, s[16:17] offset:2048 sc1\n\t"
    "v_mfma_f32_16x16x32_f16 v[172:175], a[200:203], a[232:235], v[172:175]\n\t"
    "v_pk_mad_u16 v250, v250, 8, s29 op_sel:[0,0,1] op_sel_hi:[1,0,1]\n\t"
    "v_pk_mad_u16 v251, v251, 8, s29 op_sel:[0,0,1] op_sel_hi:[1,0,1]\n\t"
    "v_mfma_f32_16x16x32_f16 v[172:175], a[204:207], a[236:239], v[172:175]\n\t"
    "v_mfma_f32_16x16x32_f16 v[180:183], a[204:207], a[240:243], v[180:183]\n\t"
    "v_bfi_b32 v236, s24, v240, v236\n\t"
    "v_mfma_f32_16x16x32_f16 v[180:183], a[200:203], a[236:239], v[180:183]\n\t"
    "v_bfi_b32 v237, s24, v241, v237\n\t"
    "v_mfma_f32_16x16x32_f16 v[180:183], a[196:199], a[232:235], v[180:183]\n\t"
    "v_mfma_f32_16x16x32_f16 v[180:183], a[192:195], a[228:231], v[180:183]\n\t"
    "ds_read_b128 a[228:231], v245 offset:160\n\t"
    "v_bfi_b32 v238, s24, v250, v238\n\t"
    "v_mfma_f32_16x16x32_f16 v[188:191], a[192:195], a[232:235], v[188:191]\n\t"
    "ds_read_b128 a[232:235], v245 offset:192\n\t"
    "v_bfi_b32 v239, s24, v251, v239\n\t"
    "v_mfma_f32_16x16x32_f16 v[188:191], a[196:199], a[236:239], v[188:191]\n\t"
    "global_load_dwordx4 a[188:191], v242, s[16:17] offset:3072 sc1\n\t"
    "v_mfma_f32_16x16x32_f16 v[188:191], a[200:203], a[240:243], v[188:191]\n\t"
    "v_add_u32 v242, 8192, v242\n\t"
    "v_mfma_f32_16x16x32_f16 v[188:191], a[204:207], a[244:247], v[188:191]\n\t"
    "v_mfma_f32_16x16x32_f16 v[196:199], a[204:207], a[248:251], v[196:199]\n\t"
    "ds_read_b128 a[248:251], v246 offset:64\n\t"
    "v_mfma_f32_16x16x32_f16 v[196:199], a[200:203], a[244:247], v[196:199]\n\t"
    "ds_read_b128 a[244:247], v246 offset:32\n\t"
    "v_mfma_f32_16x16x32_f16 v[196:199], a[196:199], a[240:243], v[196:199]\n\t"
    "s_waitcnt vmcnt(16) lgkmcnt(4)\n\t"
    "s_nop 0\n\t"
    "v_mfma_f32_16x16x32_f16 v[196:199], a[192:195], a[236:239], v[196:199]\n\t"
    "s_cmp_lg_u32 s25, 512\n\t"
    "s_cbranch_scc1 L_T_%=\n\t"
    ".p2align 3\n\t"
    "v_mfma_f32_16x16x32_f16 v[0:3], a[128:131], a[208:211], v[0:3]\n\t"
    "ds_read_b128 a[240:243], v247 offset:0\n\t"
    "v_mfma_f32_16x16x32_f16 v[0:3], a[132:135], a[212:215], v[0:3]\n\t"
    "ds_read_b128 a[236:239], v245 offset:224\n\t"
    "v_mfma_f32_16x16x32_f16 v[0:3], a[136:139], a[216:219], v[0:3]\n\t"
    "global_load_dwordx4 a[192:195], v242, s[16:17] offset:-4096 sc1\n\t"
    "v_mfma_f32_16x16x32_f16 v[0:3], a[140:143], a[220:223], v[0:3]\n\t"
    "v_mfma_f32_16x16x32_f16 v[8:11], a[140:143], a[224:227], v[8:11]\n\t"
    "v_mfma_f32_16x16x32_f16 v[8:11], a[136:139], a[220:223], v[8:11]\n\t"
    "v_mfma_f32_16x16x32_f16 v[8:11], a[132:135], a[216:219], v[8:11]\n\t"
    "v_mfma_f32_16x16x32_f16 v[8:11], a[128:131], a[212:215], v[8:11]\n\t"
    "s_waitcnt lgkmcnt(4)\n\t"
    "s_nop 0\n\t"
    "v_mfma_f32_16x16x32_f16 v[16:19], a[128:131], a[216:219], v[16:19]\n\t"
    "v_mfma_f32_16x16x32_f16 v[16:19], a[132:135], a[220:223], v[16:19]\n\t"
    "global_load_dwordx4 a[196:199], v242, s[16:17] offset:-3072 sc1\n\t"
    "v_mfma_f32_16x16x32_f16 v[16:19], a[136:139], a[224:227], v[16:19]\n\t"
    "v_mfma_f32_16x16x32_f16 v[16:19], a[140:143], a[228:231], v[16:19]\n\t"
    "v_mfma_f32_16x16x32_f16 v[24:27], a[140:143], a[232:235], v[24:27]\n\t"
    "v_accvgpr_read_b32 v250, a252\n\t"
    "v_mfma_f32_16x16x32_f16 v[24:27], a[136:139], a[228:231], v[24:27]\n\t"
    "s_waitcnt lgkmcnt(0)\n\t"
    "s_nop 0\n\t"
    "v_mfma_f32_16x16x32_f16 v[24:27], a[132:135], a[224:227], v[24:27]\n\t"
    "v_mfma_f32_16x16x32_f16 v[24:27], a[128:131], a[220:223], v[24:27]\n\t"
    "v_accvgpr_read_b32 v251, a253\n\t"
    "v_mfma_f32_16x16x32_f16 v[32:35], a[128:131], a[224:227], v[32:35]\n\t"
    "global_load_dwordx4 a[200:203], v242, s[16:17] offset:-2048 sc1\n\t"
    "v_mfma_f32_16x16x32_f16 v[32:35], a[132:135], a[228:231], v[32:35]\n\t"
    "v_mfma_f32_16x16x32_f16 v[32:35], a[136:139], a[232:235], v[32:35]\n\t"
    "v_and_b32 v250, 0xff, v250\n\t"
    "v_mfma_f32_16x16x32_f16 v[32:35], a[140:143], a[236:239], v[32:35]\n\t"
    "v_mfma_f32_16x16x32_f16 v[40:43], a[140:143], a[240:243], v[40:43]\n\t"
    "v_mfma_f32_16x16x32_f16 v[40:43], a[136:139], a[236:239], v[40:43]\n\t"
    "v_max_u32_dpp v250, v250, v250 row_ror:8 row_mask:0xf bank_mask:0xf\n\t"
    "v_mfma_f32_16x16x32_f16 v[40:43], a[132:135], a[232:235], v[40:43]\n\t"
    "v_mfma_f32_16x16x32_f16 v[40:43], a[128:131], a[228:231], v[40:43]\n\t"
    "global_load_dwordx4 a[204:207], v242, s[16:17] offset:-1024 sc1\n\t"
    "v_mfma_f32_16x16x32_f16 v[48:51], a[128:131], a[232:235], v[48:51]\n\t"
    "v_max_u32_dpp v250, v250, v250 row_ror:4 row_mask:0xf bank_mask:0xf\n\t"
    "v_mfma_f32_16x16x32_f16 v[48:51], a[132:135], a[236:239], v[48:51]\n\t"
    "v_mfma_f32_16x16x32_f16 v[48:51], a[136:139], a[240:243], v[48:51]\n\t"
    "v_mfma_f32_16x16x32_f16 v[48:51], a[140:143], a[244:247], v[48:51]\n\t"
    "v_max_u32_dpp v250, v250, v250 row_ror:2 row_mask:0xf bank_mask:0xf\n\t"
    "v_mfma_f32_16x16x32_f16 v[56:59], a[140:143], a[248:251], v[56:59]\n\t"
    "v_mfma_f32_16x16x32_f16 v[56:59], a[136:139], a[244:247], v[56:59]\n\t"
    "v_mfma_f32_16x16x32_f16 v[56:59], a[132:135], a[240:243], v[56:59]\n\t"
    "v_max_u32_dpp v250, v250, v250 row_ror:1 row_mask:0xf bank_mask:0xf\n\t"
    "v_mfma_f32_16x16x32_f16 v[56:59], a[128:131], a[236:239], v[56:59]\n\t"
    "s_waitcnt vmcnt(14)\n\t"
    "s_nop 0\n\t"
    "v_mfma_f32_16x16x32_f16 v[4:7], a[144:147], a[208:211], v[4:7]\n\t"
    "ds_read_b128 a[208:211], v245 offset:8960\n\t"
    "v_mfma_f32_16x16x32_f16 v[4:7], a[148:151], a[212:215], v[4:7]\n\t"
    "v_readlane_b32 s36, v250, 0\n\t"
    "v_mfma_f32_16x16x32_f16 v[4:7], a[152:155], a[216:219], v[4:7]\n\t"
    "global_load_dwordx4 a[128:131], v242, s[16:17] offset:0 sc1\n\t"
    "v_mfma_f32_16x16x32_f16 v[4:7], a[156:159], a[220:223], v[4:7]\n\t"
    "v_mfma_f32_16x16x32_f16 v[12:15], a[156:159], a[224:227], v[12:15]\n\t"
    "v_mfma_f32_16x16x32_f16 v[12:15], a[152:155], a[220:223], v[12:15]\n\t"
    "v_readlane_b32 s37, v250, 16\n\t"
    "v_mfma_f32_16x16x32_f16 v[12:15], a[148:151], a[216:219], v[12:15]\n\t"
    "v_mfma_f32_16x16x32_f16 v[12:15], a[144:147], a[212:215], v[12:15]\n\t"
    "ds_read_b128 a[212:215], v245 offset:8992\n\t"
    "v_mfma_f32_16x16x32_f16 v[20:23], a[144:147], a[216:219], v[20:23]\n\t"
    "ds_read_b128 a[216:219], v245 offset:9024\n\t"
    "v_readlane_b32 s38, v250, 32\n\t"
    "v_mfma_f32_16x16x32_f16 v[20:23], a[148:151], a[220:223], v[20:23]\n\t"
    "global_load_dwordx4 a[132:135], v242, s[16:17] offset:1024 sc1\n\t"
    "v_mfma_f32_16x16x32_f16 v[20:23], a[152:155], a[224:227], v[20:23]\n\t"
    "v_mfma_f32_16x16x32_f16 v[20:23], a[156:159], a[228:231], v[20:23]\n\t"
    "v_readlane_b32 s39, v250, 48\n\t"
    "v_mfma_f32_16x16x32_f16 v[28:31], a[156:159], a[232:235], v[28:31]\n\t"
    "v_mfma_f32_16x16x32_f16 v[28:31], a[152:155], a[228:231], v[28:31]\n\t"
    "v_mfma_f32_16x16x32_f16 v[28:31], a[148:151], a[224:227], v[28:31]\n\t"
    "s_max_u32 s71, s36, s37\n\t"
    "s_nop 0\n\t"
    "v_mfma_f32_16x16x32_f16 v[28:31], a[144:147], a[220:223], v[28:31]\n\t"
    "ds_read_b128 a[220:223], v245 offset:9056\n\t"
    "v_mfma_f32_16x16x32_f16 v[36:39], a[144:147], a[224:227], v[36:39]\n\t"
    "ds_read_b128 a[224:227], v245 offset:9088\n\t"
    "v_mfma_f32_16x16x32_f16 v[36:39], a[148:151], a[228:231], v[36:39]\n\t"
    "global_load_dwordx4 a[136:139], v242, s[16:17] offset:2048 sc1\n\t"
    "v_mfma_f32_16x16x32_f16 v[36:39], a[152:155], a[232:235], v[36:39]\n\t"
    "s_max_u32 s71, s71, s38\n\t"
    "s_nop 0\n\t"
    "v_mfma_f32_16x16x32_f16 v[36:39], a[156:159], a[236:239], v[36:39]\n\t"
    "v_mfma_f32_16x16x32_f16 v[44:47], a[156:159], a[240:243], v[44:47]\n\t"
    "s_max_u32 s71, s71, s39\n\t"
    "s_nop 0\n\t"
    "v_mfma_f32_16x16x32_f16 v[44:47], a[152:155], a[236:239], v[44:47]\n\t"
    "v_mfma_f32_16x16x32_f16 v[44:47], a[148:151], a[232:235], v[44:47]\n\t"
    "v_mfma_f32_16x16x32_f16 v[44:47], a[144:147], a[228:231], v[44:47]\n\t"
    "ds_read_b128 a[228:231], v245 offset:9120\n\t"
    "v_lshrrev_b32_e64 v232, 16, v249\n\t"
    "v_mfma_f32_16x16x32_f16 v[52:55], a[144:147], a[232:235], v[52:55]\n\t"
    "ds_read_b128 a[232:235], v245 offset:9152\n\t"
    "v_mfma_f32_16x16x32_f16 v[52:55], a[148:151], a[236:239], v[52:55]\n\t"
    "global_load_dwordx4 a[140:143], v242, s[16:17] offset:3072 sc1\n\t"
    "v_mfma_f32_16x16x32_f16 v[52:55], a[152:155], a[240:243], v[52:55]\n\t"
    "v_add_u32 v242, 8192, v242\n\t"
    "v_and_b32 v249, 0xffff, v249\n\t"
    "v_mfma_f32_16x16x32_f16 v[52:55], a[156:159], a[244:247], v[52:55]\n\t"
    "v_mfma_f32_16x16x32_f16 v[60:63], a[156:159], a[248:251], v[60:63]\n\t"
    "ds_read_b128 a[248:251], v246 offset:9024\n\t"
    "v_mfma_f32_16x16x32_f16 v[60:63], a[152:155], a[244:247], v[60:63]\n\t"
    "ds_read_b128 a[244:247], v246 offset:8992\n\t"
    "v_mfma_f32_16x16x32_f16 v[60:63], a[148:151], a[240:243], v[60:63]\n\t"
    "ds_read_b128 a[240:243], v247 offset:8960\n\t"
    "v_max_u32_e64 v249, v249, v232\n\t"
    "v_mfma_f32_16x16x32_f16 v[60:63], a[144:147], a[236:239], v[60:63]\n\t"
    "s_waitcnt vmcnt(14) lgkmcnt(5)\n\t"
    "s_nop 0\n\t"
    "v_mfma_f32_16x16x32_f16 v[64:67], a[160:163], a[208:211], v[64:67]\n\t"
    "ds_read_b128 a[236:239], v245 offset:9184\n\t"
    "v_mfma_f32_16x16x32_f16 v[64:67], a[164:167], a[212:215], v[64:67]\n\t"
    "v_max_u32_dpp v249, v249, v249 row_ror:8 row_mask:0xf bank_mask:0xf\n\t"
    "v_mfma_f32_16x16x32_f16 v[64:67], a[168:171], a[216:219], v[64:67]\n\t"
    "global_load_dwordx4 a[144:147], v242, s[16:17] offset:-4096 sc1\n\t"
    "v_mfma_f32_16x16x32_f16 v[64:67], a[172:175], a[220:223], v[64:67]\n\t"
    "v_mfma_f32_16x16x32_f16 v[72:75], a[172:175], a[224:227], v[72:75]\n\t"
    "v_max_u32_dpp v249, v249, v249 row_ror:4 row_mask:0xf bank_mask:0xf\n\t"
    "v_mfma_f32_16x16x32_f16 v[72:75], a[168:171], a[220:223], v[72:75]\n\t"
    "v_mfma_f32_16x16x32_f16 v[72:75], a[164:167], a[216:219], v[72:75]\n\t"
    "v_mfma_f32_16x16x32_f16 v[72:75], a[160:163], a[212:215], v[72:75]\n\t"
    "s_waitcnt lgkmcnt(4)\n\t"
    "v_max_u32_dpp v249, v249, v249 row_ror:2 row_mask:0xf bank_mask:0xf\n\t"
    "v_mfma_f32_16x16x32_f16 v[80:83], a[160:163], a[216:219], v[80:83]\n\t"
    "s_nop 0\n\t"
    "v_mfma_f32_16x16x32_f16 v[80:83], a[164:167], a[220:223], v[80:83]\n\t"
    "global_load_dwordx4 a[148:151], v242, s[16:17] offset:-3072 sc1\n\t"
    "v_mfma_f32_16x16x32_f16 v[80:83], a[168:171], a[224:227], v[80:83]\n\t"
    "v_max_u32_dpp v249, v249, v249 row_ror:1 row_mask:0xf bank_mask:0xf\n\t"
    "v_mfma_f32_16x16x32_f16 v[80:83], a[172:175], a[228:231], v[80:83]\n\t"
    "v_mfma_f32_16x16x32_f16 v[88:91], a[172:175], a[232:235], v[88:91]\n\t"
    "v_mfma_f32_16x16x32_f16 v[88:91], a[168:171], a[228:231], v[88:91]\n\t"
    "s_waitcnt lgkmcnt(0)\n\t"
    "v_readlane_b32 s36, v249, 0\n\t"
    "v_mfma_f32_16x16x32_f16 v[88:91], a[164:167], a[224:227], v[88:91]\n\t"
    "s_nop 0\n\t"
    "v_mfma_f32_16x16x32_f16 v[88:91], a[160:163], a[220:223], v[88:91]\n\t"
    "v_mfma_f32_16x16x32_f16 v[96:99], a[160:163], a[224:227], v[96:99]\n\t"
    "global_load_dwordx4 a[152:155], v242, s[16:17] offset:-2048 sc1\n\t"
    "v_mfma_f32_16x16x32_f16 v[96:99], a[164:167], a[228:231], v[96:99]\n\t"
    "v_readlane_b32 s37, v249, 16\n\t"
    "v_mfma_f32_16x16x32_f16 v[96:99], a[168:171], a[232:235], v[96:99]\n\t"
    "v_mfma_f32_16x16x32_f16 v[96:99], a[172:175], a[236:239], v[96:99]\n\t"
    "v_readlane_b32 s38, v249, 32\n\t"
    "v_mfma_f32_16x16x32_f16 v[104:107], a[172:175], a[240:243], v[104:107]\n\t"
    "v_mfma_f32_16x16x32_f16 v[104:107], a[168:171], a[236:239], v[104:107]\n\t"
    "v_mfma_f32_16x16x32_f16 v[104:107], a[164:167], a[232:235], v[104:107]\n\t"
    "v_mfma_f32_16x16x32_f16 v[104:107], a[160:163], a[228:231], v[104:107]\n\t"
    "global_load_dwordx4 a[156:159], v242, s[16:17] offset:-1024 sc1\n\t"
    "v_mfma_f32_16x16x32_f16 v[112:115], a[160:163], a[232:235], v[112:115]\n\t"
    "v_readlane_b32 s39, v249, 48\n\t"
    "v_mfma_f32_16x16x32_f16 v[112:115], a[164:167], a[236:239], v[112:115]\n\t"
    "v_mfma_f32_16x16x32_f16 v[112:115], a[168:171], a[240:243], v[112:115]\n\t"
    "s_max_u32 s40, s36, s37\n\t"
    "s_nop 0\n\t"
    "v_mfma_f32_16x16x32_f16 v[112:115], a[172:175], a[244:247], v[112:115]\n\t"
    "v_mfma_f32_16x16x32_f16 v[120:123], a[172:175], a[248:251], v[120:123]\n\t"
    "v_mfma_f32_16x16x32_f16 v[120:123], a[168:171], a[244:247], v[120:123]\n\t"
    "s_max_u32 s40, s40, s38\n\t"
    "s_nop 0\n\t"
    "v_mfma_f32_16x16x32_f16 v[120:123], a[164:167], a[240:243], v[120:123]\n\t"
    "v_mfma_f32_16x16x32_f16 v[120:123], a[160:163], a[236:239], v[120:123]\n\t"
    "s_waitcnt vmcnt(12)\n\t"
    "s_nop 0\n\t"
    "v_mfma_f32_16x16x32_f16 v[68:71], a[176:179], a[208:211], v[68:71]\n\t"
    "ds_read_b128 a[208:211], v245 offset:17920\n\t"
    "s_max_u32 s40, s40, s39\n\t"
    "v_mfma_f32_16x16x32_f16 v[68:71], a[180:183], a[212:215], v[68:71]\n\t"
    "s_nop 0\n\t"
    "v_mfma_f32_16x16x32_f16 v[68:71], a[184:187], a[216:219], v[68:71]\n\t"
    "global_load_dwordx4 a[160:163], v242, s[16:17] offset:0 sc1\n\t"
    "v_mfma_f32_16x16x32_f16 v[68:71], a[188:191], a[220:223], v[68:71]\n\t"
    "s_lshr_b32 s36, s92, 8\n\t"
    "s_nop 0\n\t"
    "v_mfma_f32_16x16x32_f16 v[76:79], a[188:191], a[224:227], v[76:79]\n\t"
    "v_mfma_f32_16x16x32_f16 v[76:79], a[184:187], a[220:223], v[76:79]\n\t"
    "v_mfma_f32_16x16x32_f16 v[76:79], a[180:183], a[216:219], v[76:79]\n\t"
    "s_add_u32 s36, s36, 161792\n\t"
    "v_mfma_f32_16x16x32_f16 v[76:79], a[176:179], a[212:215], v[76:79]\n\t"
    "ds_read_b128 a[212:215], v245 offset:17952\n\t"
    "v_mfma_f32_16x16x32_f16 v[84:87], a[176:179], a[216:219], v[84:87]\n\t"
    "ds_read_b128 a[216:219], v245 offset:17984\n\t"
    "v_mfma_f32_16x16x32_f16 v[84:87], a[180:183], a[220:223], v[84:87]\n\t"
    "global_load_dwordx4 a[164:167], v242, s[16:17] offset:1024 sc1\n\t"
    "v_mfma_f32_16x16x32_f16 v[84:87], a[184:187], a[224:227], v[84:87]\n\t"
    "s_lshl_b32 s71, s71, 16\n\t"
    "s_nop 0\n\t"
    "v_mfma_f32_16x16x32_f16 v[84:87], a[188:191], a[228:231], v[84:87]\n\t"
    "v_mfma_f32_16x16x32_f16 v[92:95], a[188:191], a[232:235], v[92:95]\n\t"
    "s_or_b32 s40, s40, s71\n\t"
    "s_nop 0\n\t"
    "v_mfma_f32_16x16x32_f16 v[92:95], a[184:187], a[228:231], v[92:95]\n\t"
    "v_mfma_f32_16x16x32_f16 v[92:95], a[180:183], a[224:227], v[92:95]\n\t"
    "v_mfma_f32_16x16x32_f16 v[92:95], a[176:179], a[220:223], v[92:95]\n\t"
    "ds_read_b128 a[220:223], v245 offset:18016\n\t"
    "v_mfma_f32_16x16x32_f16 v[100:103], a[176:179], a[224:227], v[100:103]\n\t"
    "ds_read_b128 a[224:227], v245 offset:18048\n\t"
    "v_mov_b32_e64 v232, s36\n\t"
    "v_mfma_f32_16x16x32_f16 v[100:103], a[180:183], a[228:231], v[100:103]\n\t"
    "global_load_dwordx4 a[168:171], v242, s[16:17] offset:2048 sc1\n\t"
    "v_mfma_f32_16x16x32_f16 v[100:103], a[184:187], a[232:235], v[100:103]\n\t"
    "v_mfma_f32_16x16x32_f16 v[100:103], a[188:191], a[236:239], v[100:103]\n\t"
    "v_mov_b32_e64 v233, s40\n\t"
    "v_mfma_f32_16x16x32_f16 v[108:111], a[188:191], a[240:243], v[108:111]\n\t"
    "v_mfma_f32_16x16x32_f16 v[108:111], a[184:187], a[236:239], v[108:111]\n\t"
    "v_mfma_f32_16x16x32_f16 v[108:111], a[180:183], a[232:235], v[108:111]\n\t"
    "ds_write_b32 v232, v233\n\t"
    "v_mfma_f32_16x16x32_f16 v[108:111], a[176:179], a[228:231], v[108:111]\n\t"
    "ds_read_b128 a[228:231], v245 offset:18080\n\t"
    "v_mfma_f32_16x16x32_f16 v[116:119], a[176:179], a[232:235], v[116:119]\n\t"
    "ds_read_b128 a[232:235], v245 offset:18112\n\t"
    "v_mfma_f32_16x16x32_f16 v[116:119], a[180:183], a[236:239], v[116:119]\n\t"
    "global_load_dwordx4 a[172:175], v242, s[16:17] offset:3072 sc1\n\t"
    "v_mfma_f32_16x16x32_f16 v[116:119], a[184:187], a[240:243], v[116:119]\n\t"
    "v_add_u32 v242, 8192, v242\n\t"
    "v_mov_b32 v232, 161792\n\t"
    "v_mfma_f32_16x16x32_f16 v[116:119], a[188:191], a[244:247], v[116:119]\n\t"
    "v_mfma_f32_16x16x32_f16 v[124:127], a[188:191], a[248:251], v[124:127]\n\t"
    "ds_read_b128 a[248:251], v246 offset:17984\n\t"
    "v_mfma_f32_16x16x32_f16 v[124:127], a[184:187], a[244:247], v[124:127]\n\t"
    "ds_read_b128 a[244:247], v246 offset:17952\n\t"
    "v_mfma_f32_16x16x32_f16 v[124:127], a[180:183], a[240:243], v[124:127]\n\t"
    "ds_read_b128 a[240:243], v247 offset:17920\n\t"
    "s_waitcnt lgkmcnt(5)\n\t"
    "v_mfma_f32_16x16x32_f16 v[124:127], a[176:179], a[236:239], v[124:127]\n\t"
    "s_waitcnt vmcnt(12) lgkmcnt(6)\n\t"
    "s_barrier\n\t"
    "v_mfma_f32_16x16x32_f16 a[0:3], a[192:195], a[208:211], a[0:3]\n\t"
    "ds_read_b128 a[236:239], v245 offset:18144\n\t"
    "s_nop 0\n\t"
    "v_mfma_f32_16x16x32_f16 a[0:3], a[196:199], a[212:215], a[0:3]\n\t"
    "v_mfma_f32_16x16x32_f16 a[0:3], a[200:203], a[216:219], a[0:3]\n\t"
    "global_load_dwordx4 a[176:179], v242, s[16:17] offset:-4096 sc1\n\t"
    "ds_read_b128 v[236:239], v232\n\t"
    "v_mfma_f32_16x16x32_f16 a[0:3], a[204:207], a[220:223], a[0:3]\n\t"
    "v_mfma_f32_16x16x32_f16 a[8:11], a[204:207], a[224:227], a[8:11]\n\t"
    "v_mfma_f32_16x16x32_f16 a[8:11], a[200:203], a[220:223], a[8:11]\n\t"
    "v_mfma_f32_16x16x32_f16 a[8:11], a[196:199], a[216:219], a[8:11]\n\t"
    "s_waitcnt lgkmcnt(0)\n\t"
    "s_nop 0\n\t"
    "v_mfma_f32_16x16x32_f16 a[8:11], a[192:195], a[212:215], a[8:11]\n\t"
    "s_waitcnt lgkmcnt(5)\n\t"
    "s_nop 0\n\t"
    "v_mfma_f32_16x16x32_f16 a[16:19], a[192:195], a[216:219], a[16:19]\n\t"
    "v_mfma_f32_16x16x32_f16 a[16:19], a[196:199], a[220:223], a[16:19]\n\t"
    "global_load_dwordx4 a[180:183], v242, s[16:17] offset:-3072 sc1\n\t"
    "v_mfma_f32_16x16x32_f16 a[16:19], a[200:203], a[224:227], a[16:19]\n\t"
    "v_pk_max_u16 v236, v236, v237\n\t"
    "v_mfma_f32_16x16x32_f16 a[16:19], a[204:207], a[228:231], a[16:19]\n\t"
    "v_mfma_f32_16x16x32_f16 a[24:27], a[204:207], a[232:235], a[24:27]\n\t"
    "v_pk_max_u16 v238, v238, v239\n\t"
    "v_mfma_f32_16x16x32_f16 a[24:27], a[200:203], a[228:231], a[24:27]\n\t"
    "s_waitcnt lgkmcnt(1)\n\t"
    "s_nop 0\n\t"
    "v_mfma_f32_16x16x32_f16 a[24:27], a[196:199], a[224:227], a[24:27]\n\t"
    "v_mfma_f32_16x16x32_f16 a[24:27], a[192:195], a[220:223], a[24:27]\n\t"
    "v_pk_max_u16 v236, v236, v238\n\t"
    "v_mfma_f32_16x16x32_f16 a[32:35], a[192:195], a[224:227], a[32:35]\n\t"
    "global_load_dwordx4 a[184:187], v242, s[16:17] offset:-2048 sc1\n\t"
    "v_mfma_f32_16x16x32_f16 a[32:35], a[196:199], a[228:231], a[32:35]\n\t"
    "v_mfma_f32_16x16x32_f16 a[32:35], a[200:203], a[232:235], a[32:35]\n\t"
    "v_readfirstlane_b32 s45, v236\n\t"
    "s_nop 0\n\t"
    "v_mfma_f32_16x16x32_f16 a[32:35], a[204:207], a[236:239], a[32:35]\n\t"
    "v_mfma_f32_16x16x32_f16 a[40:43], a[204:207], a[240:243], a[40:43]\n\t"
    "v_mfma_f32_16x16x32_f16 a[40:43], a[200:203], a[236:239], a[40:43]\n\t"
    "s_mov_b32 s62, 0xbb2aaaab\n\t"
    "v_mfma_f32_16x16x32_f16 a[40:43], a[196:199], a[232:235], a[40:43]\n\t"
    "v_mfma_f32_16x16x32_f16 a[40:43], a[192:195], a[228:231], a[40:43]\n\t"
    "global_load_dwordx4 a[188:191], v242, s[16:17] offset:-1024 sc1\n\t"
    "v_mfma_f32_16x16x32_f16 a[48:51], a[192:195], a[232:235], a[48:51]\n\t"
    "s_mov_b32 s63, 0x3c2aaaab\n\t"
    "v_mfma_f32_16x16x32_f16 a[48:51], a[196:199], a[236:239], a[48:51]\n\t"
    "v_mfma_f32_16x16x32_f16 a[48:51], a[200:203], a[240:243], a[48:51]\n\t"
    "v_mfma_f32_16x16x32_f16 a[48:51], a[204:207], a[244:247], a[48:51]\n\t"
    "s_mov_b32 s64, 0x3d000000\n\t"
    "v_mfma_f32_16x16x32_f16 a[56:59], a[204:207], a[248:251], a[56:59]\n\t"
    "v_mfma_f32_16x16x32_f16 a[56:59], a[200:203], a[244:247], a[56:59]\n\t"
    "v_mfma_f32_16x16x32_f16 a[56:59], a[196:199], a[240:243], a[56:59]\n\t"
    "s_mov_b32 s65, 0xc22aaaab\n\t"
    "v_mfma_f32_16x16x32_f16 a[56:59], a[192:195], a[236:239], a[56:59]\n\t"
    "s_waitcnt vmcnt(12)\n\t"
    "s_nop 0\n\t"
    "v_mfma_f32_16x16x32_f16 a[4:7], a[128:131], a[208:211], a[4:7]\n\t"
    "ds_read_b128 a[208:211], v245 offset:26880\n\t"
    "v_mfma_f32_16x16x32_f16 a[4:7], a[132:135], a[212:215], a[4:7]\n\t"
    "v_mfma_f32_16x16x32_f16 a[4:7], a[136:139], a[216:219], a[4:7]\n\t"
    "global_load_dwordx4 a[192:195], v242, s[16:17] offset:0 sc1\n\t"
    "v_mfma_f32_16x16x32_f16 a[4:7], a[140:143], a[220:223], a[4:7]\n\t"
    "s_mov_b32 s66, 0xbe2aaaab\n\t"
    "v_mfma_f32_16x16x32_f16 a[12:15], a[140:143], a[224:227], a[12:15]\n\t"
    "v_mfma_f32_16x16x32_f16 a[12:15], a[136:139], a[220:223], a[12:15]\n\t"
    "s_mov_b32 s67, 0xbfb60b61\n\t"
    "v_mfma_f32_16x16x32_f16 a[12:15], a[132:135], a[216:219], a[12:15]\n\t"
    "v_mfma_f32_16x16x32_f16 a[12:15], a[128:131], a[212:215], a[12:15]\n\t"
    "ds_read_b128 a[212:215], v245 offset:26912\n\t"
    "v_mfma_f32_16x16x32_f16 a[20:23], a[128:131], a[216:219], a[20:23]\n\t"
    "ds_read_b128 a[216:219], v245 offset:26944\n\t"
    "v_mov_b32 v128, 0xc1c00000\n\t"
    "v_mfma_f32_16x16x32_f16 a[20:23], a[132:135], a[220:223], a[20:23]\n\t"
    "global_load_dwordx4 a[196:199], v242, s[16:17] offset:1024 sc1\n\t"
    "v_mfma_f32_16x16x32_f16 a[20:23], a[136:139], a[224:227], a[20:23]\n\t"
    "v_mfma_f32_16x16x32_f16 a[20:23], a[140:143], a[228:231], a[20:23]\n\t"
    "v_mov_b32 v129, 0x3fb60b61\n\t"
    "v_mfma_f32_16x16x32_f16 a[28:31], a[140:143], a[232:235], a[28:31]\n\t"
    "v_mfma_f32_16x16x32_f16 a[28:31], a[136:139], a[228:231], a[28:31]\n\t"
    "v_mfma_f32_16x16x32_f16 a[28:31], a[132:135], a[224:227], a[28:31]\n\t"
    "s_add_u32 s68, 0x3f851592, s46\n\t"
    "v_mfma_f32_16x16x32_f16 a[28:31], a[128:131], a[220:223], a[28:31]\n\t"
    "ds_read_b128 a[220:223], v245 offset:26976\n\t"
    "v_mfma_f32_16x16x32_f16 a[36:39], a[128:131], a[224:227], a[36:39]\n\t"
    "ds_read_b128 a[224:227], v245 offset:27008\n\t"
    "v_mfma_f32_16x16x32_f16 a[36:39], a[132:135], a[228:231], a[36:39]\n\t"
    "global_load_dwordx4 a[200:203], v242, s[16:17] offset:2048 sc1\n\t"
    "v_mfma_f32_16x16x32_f16 a[36:39], a[136:139], a[232:235], a[36:39]\n\t"
    "s_add_u32 s69, 0x3d317218, s46\n\t"
    "v_mfma_f32_16x16x32_f16 a[36:39], a[140:143], a[236:239], a[36:39]\n\t"
    "v_mfma_f32_16x16x32_f16 a[44:47], a[140:143], a[240:243], a[44:47]\n\t"
    "s_add_u32 s70, 0x3e317218, s46\n\t"
    "v_mfma_f32_16x16x32_f16 a[44:47], a[136:139], a[236:239], a[44:47]\n\t"
    "v_mfma_f32_16x16x32_f16 a[44:47], a[132:135], a[232:235], a[44:47]\n\t"
    "v_mfma_f32_16x16x32_f16 a[44:47], a[128:131], a[228:231], a[44:47]\n\t"
    "ds_read_b128 a[228:231], v245 offset:27040\n\t"
    "s_add_u32 s74, 0xbe851592, s46\n\t"
    "v_mfma_f32_16x16x32_f16 a[52:55], a[128:131], a[232:235], a[52:55]\n\t"
    "ds_read_b128 a[232:235], v245 offset:27072\n\t"
    "v_mfma_f32_16x16x32_f16 a[52:55], a[132:135], a[236:239], a[52:55]\n\t"
    "global_load_dwordx4 a[204:207], v242, s[16:17] offset:3072 sc1\n\t"
    "v_mfma_f32_16x16x32_f16 a[52:55], a[136:139], a[240:243], a[52:55]\n\t"
    "v_add_u32 v242, 8192, v242\n\t"
    "v_mfma_f32_16x16x32_f16 a[52:55], a[140:143], a[244:247], a[52:55]\n\t"
    "s_sub_u32 s75, 0x40a4258a, s46\n\t"
    "v_mfma_f32_16x16x32_f16 a[60:63], a[140:143], a[248:251], a[60:63]\n\t"
    "ds_read_b128 a[248:251], v246 offset:26944\n\t"
    "v_mfma_f32_16x16x32_f16 a[60:63], a[136:139], a[244:247], a[60:63]\n\t"
    "ds_read_b128 a[244:247], v246 offset:26912\n\t"
    "v_mfma_f32_16x16x32_f16 a[60:63], a[132:135], a[240:243], a[60:63]\n\t"
    "ds_read_b128 a[240:243], v247 offset:26880\n\t"
    "s_sub_u32 s76, 0x3eaf16fa, s46\n\t"
    "v_mfma_f32_16x16x32_f16 a[60:63], a[128:131], a[236:239], a[60:63]\n\t"
    "s_waitcnt vmcnt(12) lgkmcnt(5)\n\t"
    "s_nop 0\n\t"
    "v_mfma_f32_16x16x32_f16 a[64:67], a[144:147], a[208:211], a[64:67]\n\t"
    "ds_read_b128 a[236:239], v245 offset:27104\n\t"
    "v_mfma_f32_16x16x32_f16 a[64:67], a[148:151], a[212:215], a[64:67]\n\t"
    "s_sub_u32 s77, 0xbf83513b, s46\n\t"
    "v_mfma_f32_16x16x32_f16 a[64:67], a[152:155], a[216:219], a[64:67]\n\t"
    "v_mfma_f32_16x16x32_f16 a[64:67], a[156:159], a[220:223], a[64:67]\n\t"
    "v_mfma_f32_16x16x32_f16 a[72:75], a[156:159], a[224:227], a[72:75]\n\t"
    "v_mov_b32_e64 v130, s77\n\t"
    "v_mfma_f32_16x16x32_f16 a[72:75], a[152:155], a[220:223], a[72:75]\n\t"
    "v_mfma_f32_16x16x32_f16 a[72:75], a[148:151], a[216:219], a[72:75]\n\t"
    "v_mfma_f32_16x16x32_f16 a[72:75], a[144:147], a[212:215], a[72:75]\n\t"
    "s_waitcnt lgkmcnt(4)\n\t"
    "v_mbcnt_lo_u32_b32 v240, -1, 0\n\t"
    "v_mfma_f32_16x16x32_f16 a[80:83], a[144:147], a[216:219], a[80:83]\n\t"
    "s_nop 0\n\t"
    "v_mfma_f32_16x16x32_f16 a[80:83], a[148:151], a[220:223], a[80:83]\n\t"
    "v_mfma_f32_16x16x32_f16 a[80:83], a[152:155], a[224:227], a[80:83]\n\t"
    "v_mbcnt_hi_u32_b32 v240, -1, v240\n\t"
    "v_mfma_f32_16x16x32_f16 a[80:83], a[156:159], a[228:231], a[80:83]\n\t"
    "v_mfma_f32_16x16x32_f16 a[88:91], a[156:159], a[232:235], a[88:91]\n\t"
    "v_mfma_f32_16x16x32_f16 a[88:91], a[152:155], a[228:231], a[88:91]\n\t"
    "s_waitcnt lgkmcnt(0)\n\t"
    "s_lshr_b32 s36, s92, 9\n\t"
    "v_mfma_f32_16x16x32_f16 a[88:91], a[148:151], a[224:227], a[88:91]\n\t"
    "v_mfma_f32_16x16x32_f16 a[88:91], a[144:147], a[220:223], a[88:91]\n\t"
    "v_mfma_f32_16x16x32_f16 a[96:99], a[144:147], a[224:227], a[96:99]\n\t"
    "v_lshrrev_b32_e64 v241, 5, v240\n\t"
    "v_mfma_f32_16x16x32_f16 a[96:99], a[148:151], a[228:231], a[96:99]\n\t"
    "v_mfma_f32_16x16x32_f16 a[96:99], a[152:155], a[232:235], a[96:99]\n\t"
    "v_mfma_f32_16x16x32_f16 a[96:99], a[156:159], a[236:239], a[96:99]\n\t"
    "v_mfma_f32_16x16x32_f16 a[104:107], a[156:159], a[240:243], a[104:107]\n\t"
    "v_or_b32_e64 v241, s36, v241\n\t"
    "v_mfma_f32_16x16x32_f16 a[104:107], a[152:155], a[236:239], a[104:107]\n\t"
    "v_mfma_f32_16x16x32_f16 a[104:107], a[148:151], a[232:235], a[104:107]\n\t"
    "v_mfma_f32_16x16x32_f16 a[104:107], a[144:147], a[228:231], a[104:107]\n\t"
    "v_xor_b32_e64 v241, v241, v240\n\t"
    "v_mfma_f32_16x16x32_f16 a[112:115], a[144:147], a[232:235], a[112:115]\n\t"
    "v_mfma_f32_16x16x32_f16 a[112:115], a[148:151], a[236:239], a[112:115]\n\t"
    "v_mfma_f32_16x16x32_f16 a[112:115], a[152:155], a[240:243], a[112:115]\n\t"
    "v_and_b32_e64 v241, 7, v241\n\t"
    "v_mfma_f32_16x16x32_f16 a[112:115], a[156:159], a[244:247], a[112:115]\n\t"
    "v_mfma_f32_16x16x32_f16 a[120:123], a[156:159], a[248:251], a[120:123]\n\t"
    "v_mfma_f32_16x16x32_f16 a[120:123], a[152:155], a[244:247], a[120:123]\n\t"
    "v_and_b32_e64 v243, 15, v240\n\t"
    "v_mfma_f32_16x16x32_f16 a[120:123], a[148:151], a[240:243], a[120:123]\n\t"
    "v_mfma_f32_16x16x32_f16 a[120:123], a[144:147], a[236:239], a[120:123]\n\t"
    "s_waitcnt vmcnt(8)\n\t"
    "s_nop 0\n\t"
    "v_mfma_f32_16x16x32_f16 a[68:71], a[160:163], a[208:211], a[68:71]\n\t"
    "ds_read_b128 a[208:211], v245 offset:35840\n\t"
    "v_lshl_or_b32 v243, v243, 3, v241\n\t"
    "v_mfma_f32_16x16x32_f16 a[68:71], a[164:167], a[212:215], a[68:71]\n\t"
    "v_mfma_f32_16x16x32_f16 a[68:71], a[168:171], a[216:219], a[68:71]\n\t"
    "v_mfma_f32_16x16x32_f16 a[68:71], a[172:175], a[220:223], a[68:71]\n\t"
    "v_bfe_u32 v241, v240, 4, 1\n\t"
    "v_mfma_f32_16x16x32_f16 a[76:79], a[172:175], a[224:227], a[76:79]\n\t"
    "v_mfma_f32_16x16x32_f16 a[76:79], a[168:171], a[220:223], a[76:79]\n\t"
    "v_mfma_f32_16x16x32_f16 a[76:79], a[164:167], a[216:219], a[76:79]\n\t"
    "v_lshl_or_b32 v243, v243, 1, v241\n\t"
    "v_mfma_f32_16x16x32_f16 a[76:79], a[160:163], a[212:215], a[76:79]\n\t"
    "ds_read_b128 a[212:215], v245 offset:35872\n\t"
    "v_mfma_f32_16x16x32_f16 a[84:87], a[160:163], a[216:219], a[84:87]\n\t"
    "ds_read_b128 a[216:219], v245 offset:35904\n\t"
    "v_mfma_f32_16x16x32_f16 a[84:87], a[164:167], a[220:223], a[84:87]\n\t"
    "v_lshlrev_b32_e64 v243, 3, v243\n\t"
    "v_mfma_f32_16x16x32_f16 a[84:87], a[168:171], a[224:227], a[84:87]\n\t"
    "v_mfma_f32_16x16x32_f16 a[84:87], a[172:175], a[228:231], a[84:87]\n\t"
    "v_mfma_f32_16x16x32_f16 a[92:95], a[172:175], a[232:235], a[92:95]\n\t"
    "v_mfma_f32_16x16x32_f16 a[92:95], a[168:171], a[228:231], a[92:95]\n\t"
    "v_add_u32 v243, 90112, v243\n\t"
    "v_mfma_f32_16x16x32_f16 a[92:95], a[164:167], a[224:227], a[92:95]\n\t"
    "v_mfma_f32_16x16x32_f16 a[92:95], a[160:163], a[220:223], a[92:95]\n\t"
    "ds_read_b128 a[220:223], v245 offset:35936\n\t"
    "v_mfma_f32_16x16x32_f16 a[100:103], a[160:163], a[224:227], a[100:103]\n\t"
    "ds_read_b128 a[224:227], v245 offset:35968\n\t"
    "v_lshrrev_b32_e64 v241, 3, v240\n\t"
    "v_mfma_f32_16x16x32_f16 a[100:103], a[164:167], a[228:231], a[100:103]\n\t"
    "v_mfma_f32_16x16x32_f16 a[100:103], a[168:171], a[232:235], a[100:103]\n\t"
    "v_mfma_f32_16x16x32_f16 a[100:103], a[172:175], a[236:239], a[100:103]\n\t"
    "v_xor_b32_e64 v241, v241, v240\n\t"
    "v_mfma_f32_16x16x32_f16 a[108:111], a[172:175], a[240:243], a[108:111]\n\t"
    "v_mfma_f32_16x16x32_f16 a[108:111], a[168:171], a[236:239], a[108:111]\n\t"
    "v_mfma_f32_16x16x32_f16 a[108:111], a[164:167], a[232:235], a[108:111]\n\t"
    "s_lshl_b32 s36, s92, 1\n\t"
    "s_nop 0\n\t"
    "v_mfma_f32_16x16x32_f16 a[108:111], a[160:163], a[228:231], a[108:111]\n\t"
    "ds_read_b128 a[228:231], v245 offset:36000\n\t"
    "v_mfma_f32_16x16x32_f16 a[116:119], a[160:163], a[232:235], a[116:119]\n\t"
    "ds_read_b128 a[232:235], v245 offset:36032\n\t"
    "v_mfma_f32_16x16x32_f16 a[116:119], a[164:167], a[236:239], a[116:119]\n\t"
    "s_add_u32 s36, s36, 90112\n\t"
    "v_mfma_f32_16x16x32_f16 a[116:119], a[168:171], a[240:243], a[116:119]\n\t"
    "v_mfma_f32_16x16x32_f16 a[116:119], a[172:175], a[244:247], a[116:119]\n\t"
    "v_mfma_f32_16x16x32_f16 a[124:127], a[172:175], a[248:251], a[124:127]\n\t"
    "ds_read_b128 a[248:251], v246 offset:35904\n\t"
    "v_lshl_add_u32 v244, v241, 4, s36\n\t"
    "v_mfma_f32_16x16x32_f16 a[124:127], a[168:171], a[244:247], a[124:127]\n\t"
    "ds_read_b128 a[244:247], v246 offset:35872\n\t"
    "v_mfma_f32_16x16x32_f16 a[124:127], a[164:167], a[240:243], a[124:127]\n\t"
    "ds_read_b128 a[240:243], v247 offset:35840\n\t"
    "v_mfma_f32_16x16x32_f16 a[124:127], a[160:163], a[236:239], a[124:127]\n\t"
    "s_waitcnt vmcnt(4) lgkmcnt(5)\n\t"
    "v_add_u32 v248, 0x40000, v251\n\t"
    "v_mfma_f32_16x16x32_f16 v[136:139], a[176:179], a[208:211], v[136:139]\n\t"
    "ds_read_b128 a[236:239], v245 offset:36064\n\t"
    "s_nop 0\n\t"
    "v_mfma_f32_16x16x32_f16 v[136:139], a[180:183], a[212:215], v[136:139]\n\t"
    "v_mfma_f32_16x16x32_f16 v[136:139], a[184:187], a[216:219], v[136:139]\n\t"
    "s_add_u32 s28, s92, 1024\n\t"
    "v_mfma_f32_16x16x32_f16 v[136:139], a[188:191], a[220:223], v[136:139]\n\t"
    "v_mfma_f32_16x16x32_f16 v[144:147], a[188:191], a[224:227], v[144:147]\n\t"
    "v_mfma_f32_16x16x32_f16 v[144:147], a[184:187], a[220:223], v[144:147]\n\t"
    "v_mfma_f32_16x16x32_f16 v[144:147], a[180:183], a[216:219], v[144:147]\n\t"
    "s_add_u32 s25, s28, 0x7000\n\t"
    "v_mfma_f32_16x16x32_f16 v[144:147], a[176:179], a[212:215], v[144:147]\n\t"
    "s_waitcnt lgkmcnt(4)\n\t"
    "s_nop 0\n\t"
    "v_mfma_f32_16x16x32_f16 v[152:155], a[176:179], a[216:219], v[152:155]\n\t"
    "v_mfma_f32_16x16x32_f16 v[152:155], a[180:183], a[220:223], v[152:155]\n\t"
    "s_and_b32 s25, s25, 0x7fff\n\t"
    "v_mfma_f32_16x16x32_f16 v[152:155], a[184:187], a[224:227], v[152:155]\n\t"
    "v_mfma_f32_16x16x32_f16 v[152:155], a[188:191], a[228:231], v[152:155]\n\t"
    "v_mfma_f32_16x16x32_f16 v[160:163], a[188:191], a[232:235], v[160:163]\n\t"
    "v_add_u32_e64 v240, s28, v251\n\t"
    "v_mfma_f32_16x16x32_f16 v[160:163], a[184:187], a[228:231], v[160:163]\n\t"
    "s_waitcnt lgkmcnt(0)\n\t"
    "s_nop 0\n\t"
    "v_mfma_f32_16x16x32_f16 v[160:163], a[180:183], a[224:227], v[160:163]\n\t"
    "v_mfma_f32_16x16x32_f16 v[160:163], a[176:179], a[220:223], v[160:163]\n\t"
    "v_add_u32_e64 v241, s28, v248\n\t"
    "v_mfma_f32_16x16x32_f16 v[168:171], a[176:179], a[224:227], v[168:171]\n\t"
    "v_mfma_f32_16x16x32_f16 v[168:171], a[180:183], a[228:231], v[168:171]\n\t"
    "v_mfma_f32_16x16x32_f16 v[168:171], a[184:187], a[232:235], v[168:171]\n\t"
    "s_sub_u32 s25, s25, s28\n\t"
    "s_nop 0\n\t"
    "v_mfma_f32_16x16x32_f16 v[168:171], a[188:191], a[236:239], v[168:171]\n\t"
    "v_mfma_f32_16x16x32_f16 v[176:179], a[188:191], a[240:243], v[176:179]\n\t"
    "v_mfma_f32_16x16x32_f16 v[176:179], a[184:187], a[236:239], v[176:179]\n\t"
    "s_movk_i32 s28, 0x1000\n\t"
    "s_nop 0\n\t"
    "v_mfma_f32_16x16x32_f16 v[176:179], a[180:183], a[232:235], v[176:179]\n\t"
    "v_mfma_f32_16x16x32_f16 v[176:179], a[176:179], a[228:231], v[176:179]\n\t"
    "v_mfma_f32_16x16x32_f16 v[184:187], a[176:179], a[232:235], v[184:187]\n\t"
    "s_sub_u32 s25, s25, 0x6000\n\t"
    "v_mfma_f32_16x16x32_f16 v[184:187], a[180:183], a[236:239], v[184:187]\n\t"
    "v_mfma_f32_16x16x32_f16 v[184:187], a[184:187], a[240:243], v[184:187]\n\t"
    "v_mfma_f32_16x16x32_f16 v[184:187], a[188:191], a[244:247], v[184:187]\n\t"
    "v_mfma_f32_16x16x32_f16 v[192:195], a[188:191], a[248:251], v[192:195]\n\t"
    "v_mfma_f32_16x16x32_f16 v[192:195], a[184:187], a[244:247], v[192:195]\n\t"
    "v_mfma_f32_16x16x32_f16 v[192:195], a[180:183], a[240:243], v[192:195]\n\t"
    "v_mfma_f32_16x16x32_f16 v[192:195], a[176:179], a[236:239], v[192:195]\n\t"
    "s_waitcnt vmcnt(0)\n\t"
    "s_nop 0\n\t"
    "v_mfma_f32_16x16x32_f16 v[140:143], a[192:195], a[208:211], v[140:143]\n\t"
    "v_accvgpr_read_b32 v200, a0\n\t"
    "v_mfma_f32_16x16x32_f16 v[140:143], a[196:199], a[212:215], v[140:143]\n\t"
    "v_accvgpr_read_b32 v201, a1\n\t"
    "v_mfma_f32_16x16x32_f16 v[140:143], a[200:203], a[216:219], v[140:143]\n\t"
    "v_mfma_f32_16x16x32_f16 v[140:143], a[204:207], a[220:223], v[140:143]\n\t"
    "v_accvgpr_read_b32 v202, a2\n\t"
    "v_mfma_f32_16x16x32_f16 v[148:151], a[204:207], a[224:227], v[148:151]\n\t"
    "v_accvgpr_read_b32 v203, a3\n\t"
    "v_mfma_f32_16x16x32_f16 v[148:151], a[200:203], a[220:223], v[148:151]\n\t"
    "v_mfma_f32_16x16x32_f16 v[148:151], a[196:199], a[216:219], v[148:151]\n\t"
    "v_accvgpr_read_b32 v204, a4\n\t"
    "v_mfma_f32_16x16x32_f16 v[148:151], a[192:195], a[212:215], v[148:151]\n\t"
    "v_mfma_f32_16x16x32_f16 v[156:159], a[192:195], a[216:219], v[156:159]\n\t"
    "v_accvgpr_read_b32 v205, a5\n\t"
    "v_mfma_f32_16x16x32_f16 v[156:159], a[196:199], a[220:223], v[156:159]\n\t"
    "v_accvgpr_read_b32 v206, a6\n\t"
    "v_mfma_f32_16x16x32_f16 v[156:159], a[200:203], a[224:227], v[156:159]\n\t"
    "v_mfma_f32_16x16x32_f16 v[156:159], a[204:207], a[228:231], v[156:159]\n\t"
    "v_accvgpr_read_b32 v207, a7\n\t"
    "v_mfma_f32_16x16x32_f16 v[164:167], a[204:207], a[232:235], v[164:167]\n\t"
    "v_mfma_f32_16x16x32_f16 v[164:167], a[200:203], a[228:231], v[164:167]\n\t"
    "v_accvgpr_read_b32 v208, a64\n\t"
    "v_mfma_f32_16x16x32_f16 v[164:167], a[196:199], a[224:227], v[164:167]\n\t"
    "v_accvgpr_read_b32 v209, a65\n\t"
    "v_mfma_f32_16x16x32_f16 v[164:167], a[192:195], a[220:223], v[164:167]\n\t"
    "v_mfma_f32_16x16x32_f16 v[172:175], a[192:195], a[224:227], v[172:175]\n\t"
    "v_accvgpr_read_b32 v210, a66\n\t"
    "v_mfma_f32_16x16x32_f16 v[172:175], a[196:199], a[228:231], v[172:175]\n\t"
    "v_accvgpr_read_b32 v211, a67\n\t"
    "v_mfma_f32_16x16x32_f16 v[172:175], a[200:203], a[232:235], v[172:175]\n\t"
    "v_mfma_f32_16x16x32_f16 v[172:175], a[204:207], a[236:239], v[172:175]\n\t"
    "v_accvgpr_read_b32 v212, a68\n\t"
    "v_mfma_f32_16x16x32_f16 v[180:183], a[204:207], a[240:243], v[180:183]\n\t"
    "v_mfma_f32_16x16x32_f16 v[180:183], a[200:203], a[236:239], v[180:183]\n\t"
    "v_accvgpr_read_b32 v213, a69\n\t"
    "v_mfma_f32_16x16x32_f16 v[180:183], a[196:199], a[232:235], v[180:183]\n\t"
    "v_accvgpr_read_b32 v214, a70\n\t"
    "v_mfma_f32_16x16x32_f16 v[180:183], a[192:195], a[228:231], v[180:183]\n\t"
    "v_mfma_f32_16x16x32_f16 v[188:191], a[192:195], a[232:235], v[188:191]\n\t"
    "v_accvgpr_read_b32 v215, a71\n\t"
    "v_mfma_f32_16x16x32_f16 v[188:191], a[196:199], a[236:239], v[188:191]\n\t"
    "v_mfma_f32_16x16x32_f16 v[188:191], a[200:203], a[240:243], v[188:191]\n\t"
    "v_mfma_f32_16x16x32_f16 v[188:191], a[204:207], a[244:247], v[188:191]\n\t"
    "v_mfma_f32_16x16x32_f16 v[196:199], a[204:207], a[248:251], v[196:199]\n\t"
    "v_mfma_f32_16x16x32_f16 v[196:199], a[200:203], a[244:247], v[196:199]\n\t"
    "v_mfma_f32_16x16x32_f16 v[196:199], a[196:199], a[240:243], v[196:199]\n\t"
    "v_mfma_f32_16x16x32_f16 v[196:199], a[192:195], a[236:239], v[196:199]\n\t"
    ".p2align 3\n\t"
    "s_waitcnt vmcnt(0)\n\t"
    "s_nop 0\n\t"
    "v_pk_mul_f32 v[4:5], v[4:5], s[58:59] op_sel:[0,1] op_sel_hi:[1,1] neg_lo:[1,0] neg_hi:[1,0]\n\t"
    "v_pk_mul_f32 v[6:7], v[6:7], s[58:59] op_sel:[0,1] op_sel_hi:[1,1] neg_lo:[1,0] neg_hi:[1,0]\n\t"
    "v_pk_add_f32 v[216:217], v[0:1], v[64:65] neg_lo:[0,1] neg_hi:[0,1]\n\t"
    "v_pk_add_f32 v[218:219], v[2:3], v[66:67] neg_lo:[0,1] neg_hi:[0,1]\n\t"
    "v_pk_fma_f32 v[220:221], v[68:69], s[58:59], v[4:5] op_sel:[0,1,0] op_sel_hi:[1,1,1]\n\t"
    "v_pk_fma_f32 v[222:223], v[70:71], s[58:59], v[6:7] op_sel:[0,1,0] op_sel_hi:[1,1,1]\n\t"
    "v_pk_add_f32 v[0:1], v[0:1], v[64:65]\n\t"
    "v_pk_add_f32 v[2:3], v[2:3], v[66:67]\n\t"
    "v_pk_fma_f32 v[4:5], v[68:69], s[58:59], v[4:5] op_sel:[0,1,0] op_sel_hi:[1,1,1] neg_lo:[1,0,0] neg_hi:[1,0,0]\n\t"
    "v_pk_fma_f32 v[6:7], v[70:71], s[58:59], v[6:7] op_sel:[0,1,0] op_sel_hi:[1,1,1] neg_lo:[1,0,0] neg_hi:[1,0,0]\n\t"
    "v_pk_add_f32 v[64:65], v[200:201], v[208:209]\n\t"
    "v_pk_add_f32 v[66:67], v[202:203], v[210:211]\n\t"
    "v_pk_add_f32 v[68:69], v[204:205], v[212:213]\n\t"
    "v_pk_add_f32 v[70:71], v[206:207], v[214:215]\n\t"
    "v_pk_add_f32 v[200:201], v[200:201], v[208:209] neg_lo:[0,1] neg_hi:[0,1]\n\t"
    "v_pk_add_f32 v[202:203], v[202:203], v[210:211] neg_lo:[0,1] neg_hi:[0,1]\n\t"
    "v_pk_add_f32 v[204:205], v[204:205], v[212:213] neg_lo:[0,1] neg_hi:[0,1]\n\t"
    "v_pk_add_f32 v[206:207], v[206:207], v[214:215] neg_lo:[0,1] neg_hi:[0,1]\n\t"
    "v_pk_fma_f32 v[208:209], v[0:1], -4.0, v[64:65] op_sel:[0,0,0] op_sel_hi:[1,0,1]\n\t"
    "v_pk_fma_f32 v[210:211], v[2:3], -4.0, v[66:67] op_sel:[0,0,0] op_sel_hi:[1,0,1]\n\t"
    "v_pk_fma_f32 v[212:213], v[68:69], s[60:61], v[4:5] op_sel:[0,1,0] op_sel_hi:[1,1,1] neg_lo:[1,0,1] neg_hi:[1,0,1]\n\t"
    "v_pk_fma_f32 v[214:215], v[70:71], s[60:61], v[6:7] op_sel:[0,1,0] op_sel_hi:[1,1,1] neg_lo:[1,0,1] neg_hi:[1,0,1]\n\t"
    "v_pk_fma_f32 v[0:1], v[0:1], s[50:51], v[64:65] op_sel:[0,1,0] op_sel_hi:[1,1,1] neg_lo:[1,0,0] neg_hi:[1,0,0]\n\t"
    "v_pk_fma_f32 v[2:3], v[2:3], s[50:51], v[66:67] op_sel:[0,1,0] op_sel_hi:[1,1,1] neg_lo:[1,0,0] neg_hi:[1,0,0]\n\t"
    "v_pk_fma_f32 v[4:5], v[4:5], s[50:51], v[212:213] op_sel:[0,0,0] op_sel_hi:[1,0,1] neg_lo:[1,0,1] neg_hi:[1,0,1]\n\t"
    "v_pk_fma_f32 v[6:7], v[6:7], s[50:51], v[214:215] op_sel:[0,0,0] op_sel_hi:[1,0,1] neg_lo:[1,0,1] neg_hi:[1,0,1]\n\t"
    "v_pk_fma_f32 v[136:137], v[216:217], s[48:49], v[136:137] op_sel:[0,1,0] op_sel_hi:[1,1,1]\n\t"
    "v_pk_fma_f32 v[138:139], v[218:219], s[48:49], v[138:139] op_sel:[0,1,0] op_sel_hi:[1,1,1]\n\t"
    "v_pk_fma_f32 v[140:141], v[220:221], s[68:69], v[140:141] op_sel:[0,0,0] op_sel_hi:[1,0,1]\n\t"
    "v_pk_fma_f32 v[142:143], v[222:223], s[68:69], v[142:143] op_sel:[0,0,0] op_sel_hi:[1,0,1]\n\t"
    "v_pk_fma_f32 v[200:201], v[216:217], -2.0, v[200:201] op_sel:[0,0,0] op_sel_hi:[1,0,1]\n\t"
    "v_pk_fma_f32 v[202:203], v[218:219], -2.0, v[202:203] op_sel:[0,0,0] op_sel_hi:[1,0,1]\n\t"
    "v_pk_fma_f32 v[204:205], v[204:205], s[60:61], v[220:221] op_sel:[0,0,0] op_sel_hi:[1,0,1] neg_lo:[1,0,1] neg_hi:[1,0,1]\n\t"
    "v_pk_fma_f32 v[206:207], v[206:207], s[60:61], v[222:223] op_sel:[0,0,0] op_sel_hi:[1,0,1] neg_lo:[1,0,1] neg_hi:[1,0,1]\n\t"
    "v_pk_fma_f32 v[136:137], v[208:209], s[62:63], v[136:137] op_sel:[0,0,0] op_sel_hi:[1,0,1]\n\t"
    "v_pk_fma_f32 v[138:139], v[210:211], s[62:63], v[138:139] op_sel:[0,0,0] op_sel_hi:[1,0,1]\n\t"
    "v_pk_fma_f32 v[140:141], v[212:213], s[68:69], v[140:141] op_sel:[0,1,0] op_sel_hi:[1,1,1]\n\t"
    "v_pk_fma_f32 v[142:143], v[214:215], s[68:69], v[142:143] op_sel:[0,1,0] op_sel_hi:[1,1,1]\n\t"
    "v_pk_fma_f32 v[136:137], v[0:1], s[62:63], v[136:137] op_sel:[0,1,0] op_sel_hi:[1,1,1]\n\t"
    "v_pk_fma_f32 v[138:139], v[2:3], s[62:63], v[138:139] op_sel:[0,1,0] op_sel_hi:[1,1,1]\n\t"
    "v_pk_fma_f32 v[140:141], v[4:5], s[70:71], v[140:141] op_sel:[0,0,0] op_sel_hi:[1,0,1]\n\t"
    "v_pk_fma_f32 v[142:143], v[6:7], s[70:71], v[142:143] op_sel:[0,0,0] op_sel_hi:[1,0,1]\n\t"
    "v_pk_fma_f32 v[136:137], v[200:201], s[64:65], v[136:137] op_sel:[0,0,0] op_sel_hi:[1,0,1]\n\t"
    "v_pk_fma_f32 v[138:139], v[202:203], s[64:65], v[138:139] op_sel:[0,0,0] op_sel_hi:[1,0,1]\n\t"
    "v_pk_fma_f32 v[140:141], v[204:205], s[74:75], v[140:141] op_sel:[0,0,0] op_sel_hi:[1,0,1]\n\t"
    "v_pk_fma_f32 v[142:143], v[206:207], s[74:75], v[142:143] op_sel:[0,0,0] op_sel_hi:[1,0,1]\n\t"
    "v_pk_fma_f32 v[200:201], v[136:137], s[64:65], v[200:201] op_sel:[0,1,0] op_sel_hi:[1,1,1]\n\t"
    "v_pk_fma_f32 v[202:203], v[138:139], s[64:65], v[202:203] op_sel:[0,1,0] op_sel_hi:[1,1,1]\n\t"
    "v_pk_fma_f32 v[204:205], v[140:141], s[74:75], v[204:205] op_sel:[0,1,0] op_sel_hi:[1,1,1]\n\t"
    "v_pk_fma_f32 v[206:207], v[142:143], s[74:75], v[206:207] op_sel:[0,1,0] op_sel_hi:[1,1,1]\n\t"
    "v_pk_fma_f32 v[216:217], v[136:137], s[66:67], v[216:217] op_sel:[0,1,0] op_sel_hi:[1,1,1]\n\t"
    "v_pk_fma_f32 v[218:219], v[138:139], s[66:67], v[218:219] op_sel:[0,1,0] op_sel_hi:[1,1,1]\n\t"
    "v_pk_fma_f32 v[220:221], v[140:141], s[76:77], v[220:221] op_sel:[0,0,0] op_sel_hi:[1,0,1]\n\t"
    "v_pk_fma_f32 v[222:223], v[142:143], s[76:77], v[222:223] op_sel:[0,0,0] op_sel_hi:[1,0,1]\n\t"
    "v_pk_fma_f32 v[64:65], v[200:201], s[66:67], v[216:217] op_sel:[0,0,0] op_sel_hi:[1,0,1]\n\t"
    "v_pk_fma_f32 v[66:67], v[202:203], s[66:67], v[218:219] op_sel:[0,0,0] op_sel_hi:[1,0,1]\n\t"
    "v_pk_fma_f32 v[68:69], v[220:221], s[50:51], v[204:205] op_sel:[0,0,0] op_sel_hi:[1,0,1] neg_lo:[1,0,1] neg_hi:[1,0,1]\n\t"
    "v_pk_fma_f32 v[70:71], v[222:223], s[50:51], v[206:207] op_sel:[0,0,0] op_sel_hi:[1,0,1] neg_lo:[1,0,1] neg_hi:[1,0,1]\n\t"
    "v_pk_fma_f32 v[0:1], v[0:1], s[60:61], v[128:129] op_sel:[0,0,0] op_sel_hi:[1,0,0] neg_lo:[1,0,0] neg_hi:[1,0,0]\n\t"
    "v_pk_fma_f32 v[2:3], v[2:3], s[60:61], v[128:129] op_sel:[0,0,0] op_sel_hi:[1,0,0] neg_lo:[1,0,0] neg_hi:[1,0,0]\n\t"
    "v_exp_f32 v4, v4\n\t"
    "v_exp_f32 v5, v5\n\t"
    "v_exp_f32 v6, v6\n\t"
    "v_exp_f32 v7, v7\n\t"
    "v_exp_f32 v0, v0 clamp\n\t"
    "v_exp_f32 v1, v1 clamp\n\t"
    "v_exp_f32 v2, v2 clamp\n\t"
    "v_exp_f32 v3, v3 clamp\n\t"
    "v_pk_fma_f32 v[0:1], v[0:1], s[52:53], 0.5 op_sel:[0,1,0] op_sel_hi:[1,1,0]\n\t"
    "v_pk_fma_f32 v[2:3], v[2:3], s[52:53], 0.5 op_sel:[0,1,0] op_sel_hi:[1,1,0]\n\t"
    "v_pk_fma_f32 v[4:5], v[0:1], v[4:5], v[0:1]\n\t"
    "v_pk_fma_f32 v[6:7], v[2:3], v[6:7], v[2:3]\n\t"
    "v_rcp_f32 v4, v4\n\t"
    "v_rcp_f32 v5, v5\n\t"
    "v_rcp_f32 v6, v6\n\t"
    "v_rcp_f32 v7, v7\n\t"
    "v_pk_fma_f32 v[0:1], v[0:1], v[4:5], v[4:5] neg_lo:[0,0,1] neg_hi:[0,0,1]\n\t"
    "v_pk_fma_f32 v[2:3], v[2:3], v[6:7], v[6:7] neg_lo:[0,0,1] neg_hi:[0,0,1]\n\t"
    "v_cvt_pk_bf16_f32 v232, v0, v1\n\t"
    "v_cvt_pk_bf16_f32 v233, v2, v3\n\t"
    "v_pk_fma_f32 v[200:201], v[200:201], s[58:59], v[128:129] op_sel:[0,1,0] op_sel_hi:[1,1,0]\n\t"
    "v_pk_fma_f32 v[202:203], v[202:203], s[58:59], v[128:129] op_sel:[0,1,0] op_sel_hi:[1,1,0]\n\t"
    "v_exp_f32 v204, v204\n\t"
    "v_exp_f32 v205, v205\n\t"
    "v_exp_f32 v206, v206\n\t"
    "v_exp_f32 v207, v207\n\t"
    "v_exp_f32 v200, v200 clamp\n\t"
    "v_exp_f32 v201, v201 clamp\n\t"
    "v_exp_f32 v202, v202 clamp\n\t"
    "v_exp_f32 v203, v203 clamp\n\t"
    "v_pk_fma_f32 v[200:201], v[200:201], s[52:53], 0.5 op_sel:[0,1,0] op_sel_hi:[1,1,0]\n\t"
    "v_pk_fma_f32 v[202:203], v[202:203], s[52:53], 0.5 op_sel:[0,1,0] op_sel_hi:[1,1,0]\n\t"
    "v_pk_fma_f32 v[204:205], v[200:201], v[204:205], v[200:201]\n\t"
    "v_pk_fma_f32 v[206:207], v[202:203], v[206:207], v[202:203]\n\t"
    "v_rcp_f32 v204, v204\n\t"
    "v_rcp_f32 v205, v205\n\t"
    "v_rcp_f32 v206, v206\n\t"
    "v_rcp_f32 v207, v207\n\t"
    "v_pk_fma_f32 v[200:201], v[200:201], v[204:205], v[204:205] neg_lo:[0,0,1] neg_hi:[0,0,1]\n\t"
    "v_pk_fma_f32 v[202:203], v[202:203], v[206:207], v[206:207] neg_lo:[0,0,1] neg_hi:[0,0,1]\n\t"
    "v_cvt_pk_bf16_f32 v234, v200, v201\n\t"
    "v_cvt_pk_bf16_f32 v235, v202, v203\n\t"
    "v_pk_fma_f32 v[208:209], v[208:209], s[60:61], v[128:129] op_sel:[0,0,0] op_sel_hi:[1,0,0]\n\t"
    "v_pk_fma_f32 v[210:211], v[210:211], s[60:61], v[128:129] op_sel:[0,0,0] op_sel_hi:[1,0,0]\n\t"
    "v_exp_f32 v212, v212\n\t"
    "v_exp_f32 v213, v213\n\t"
    "v_exp_f32 v214, v214\n\t"
    "v_exp_f32 v215, v215\n\t"
    "v_exp_f32 v208, v208 clamp\n\t"
    "v_exp_f32 v209, v209 clamp\n\t"
    "v_exp_f32 v210, v210 clamp\n\t"
    "v_exp_f32 v211, v211 clamp\n\t"
    "v_pk_fma_f32 v[208:209], v[208:209], s[52:53], 0.5 op_sel:[0,1,0] op_sel_hi:[1,1,0]\n\t"
    "v_pk_fma_f32 v[210:211], v[210:211], s[52:53], 0.5 op_sel:[0,1,0] op_sel_hi:[1,1,0]\n\t"
    "v_pk_fma_f32 v[212:213], v[208:209], v[212:213], v[208:209]\n\t"
    "v_pk_fma_f32 v[214:215], v[210:211], v[214:215], v[210:211]\n\t"
    "v_rcp_f32 v212, v212\n\t"
    "v_rcp_f32 v213, v213\n\t"
    "v_rcp_f32 v214, v214\n\t"
    "v_rcp_f32 v215, v215\n\t"
    "v_pk_fma_f32 v[208:209], v[208:209], v[212:213], v[212:213] neg_lo:[0,0,1] neg_hi:[0,0,1]\n\t"
    "v_pk_fma_f32 v[210:211], v[210:211], v[214:215], v[214:215] neg_lo:[0,0,1] neg_hi:[0,0,1]\n\t"
    "v_cvt_pk_bf16_f32 v236, v208, v209\n\t"
    "v_cvt_pk_bf16_f32 v237, v210, v211\n\t"
    "ds_write2st64_b64 v243, v[232:233], v[234:235] offset0:0 offset1:4\n\t"
    "v_accvgpr_read_b32 v200, a8\n\t"
    "v_accvgpr_read_b32 v201, a9\n\t"
    "v_accvgpr_read_b32 v202, a10\n\t"
    "v_accvgpr_read_b32 v203, a11\n\t"
    "v_accvgpr_read_b32 v204, a12\n\t"
    "v_accvgpr_read_b32 v205, a13\n\t"
    "v_accvgpr_read_b32 v206, a14\n\t"
    "v_accvgpr_read_b32 v207, a15\n\t"
    "v_accvgpr_read_b32 v208, a72\n\t"
    "v_accvgpr_read_b32 v209, a73\n\t"
    "v_accvgpr_read_b32 v210, a74\n\t"
    "v_accvgpr_read_b32 v211, a75\n\t"
    "v_accvgpr_read_b32 v212, a76\n\t"
    "v_accvgpr_read_b32 v213, a77\n\t"
    "v_accvgpr_read_b32 v214, a78\n\t"
    "v_accvgpr_read_b32 v215, a79\n\t"
    "v_pk_mul_f32 v[12:13], v[12:13], s[58:59] op_sel:[0,1] op_sel_hi:[1,1] neg_lo:[1,0] neg_hi:[1,0]\n\t"
    "v_pk_mul_f32 v[14:15], v[14:15], s[58:59] op_sel:[0,1] op_sel_hi:[1,1] neg_lo:[1,0] neg_hi:[1,0]\n\t"
    "v_pk_add_f32 v[216:217], v[8:9], v[72:73] neg_lo:[0,1] neg_hi:[0,1]\n\t"
    "v_pk_add_f32 v[218:219], v[10:11], v[74:75] neg_lo:[0,1] neg_hi:[0,1]\n\t"
    "v_pk_fma_f32 v[220:221], v[76:77], s[58:59], v[12:13] op_sel:[0,1,0] op_sel_hi:[1,1,1]\n\t"
    "v_pk_fma_f32 v[222:223], v[78:79], s[58:59], v[14:15] op_sel:[0,1,0] op_sel_hi:[1,1,1]\n\t"
    "v_pk_add_f32 v[8:9], v[8:9], v[72:73]\n\t"
    "v_pk_add_f32 v[10:11], v[10:11], v[74:75]\n\t"
    "v_pk_fma_f32 v[12:13], v[76:77], s[58:59], v[12:13] op_sel:[0,1,0] op_sel_hi:[1,1,1] neg_lo:[1,0,0] neg_hi:[1,0,0]\n\t"
    "v_pk_fma_f32 v[14:15], v[78:79], s[58:59], v[14:15] op_sel:[0,1,0] op_sel_hi:[1,1,1] neg_lo:[1,0,0] neg_hi:[1,0,0]\n\t"
    "v_pk_add_f32 v[72:73], v[200:201], v[208:209]\n\t"
    "v_pk_add_f32 v[74:75], v[202:203], v[210:211]\n\t"
    "v_pk_add_f32 v[76:77], v[204:205], v[212:213]\n\t"
    "v_pk_add_f32 v[78:79], v[206:207], v[214:215]\n\t"
    "v_pk_add_f32 v[200:201], v[200:201], v[208:209] neg_lo:[0,1] neg_hi:[0,1]\n\t"
    "v_pk_add_f32 v[202:203], v[202:203], v[210:211] neg_lo:[0,1] neg_hi:[0,1]\n\t"
    "v_pk_add_f32 v[204:205], v[204:205], v[212:213] neg_lo:[0,1] neg_hi:[0,1]\n\t"
    "v_pk_add_f32 v[206:207], v[206:207], v[214:215] neg_lo:[0,1] neg_hi:[0,1]\n\t"
    "v_pk_fma_f32 v[208:209], v[8:9], -4.0, v[72:73] op_sel:[0,0,0] op_sel_hi:[1,0,1]\n\t"
    "v_pk_fma_f32 v[210:211], v[10:11], -4.0, v[74:75] op_sel:[0,0,0] op_sel_hi:[1,0,1]\n\t"
    "v_pk_fma_f32 v[212:213], v[76:77], s[60:61], v[12:13] op_sel:[0,1,0] op_sel_hi:[1,1,1] neg_lo:[1,0,1] neg_hi:[1,0,1]\n\t"
    "v_pk_fma_f32 v[214:215], v[78:79], s[60:61], v[14:15] op_sel:[0,1,0] op_sel_hi:[1,1,1] neg_lo:[1,0,1] neg_hi:[1,0,1]\n\t"
    "v_pk_fma_f32 v[8:9], v[8:9], s[50:51], v[72:73] op_sel:[0,1,0] op_sel_hi:[1,1,1] neg_lo:[1,0,0] neg_hi:[1,0,0]\n\t"
    "v_pk_fma_f32 v[10:11], v[10:11], s[50:51], v[74:75] op_sel:[0,1,0] op_sel_hi:[1,1,1] neg_lo:[1,0,0] neg_hi:[1,0,0]\n\t"
    "v_pk_fma_f32 v[12:13], v[12:13], s[50:51], v[212:213] op_sel:[0,0,0] op_sel_hi:[1,0,1] neg_lo:[1,0,1] neg_hi:[1,0,1]\n\t"
    "v_pk_fma_f32 v[14:15], v[14:15], s[50:51], v[214:215] op_sel:[0,0,0] op_sel_hi:[1,0,1] neg_lo:[1,0,1] neg_hi:[1,0,1]\n\t"
    "v_pk_fma_f32 v[144:145], v[216:217], s[48:49], v[144:145] op_sel:[0,1,0] op_sel_hi:[1,1,1]\n\t"
    "v_pk_fma_f32 v[146:147], v[218:219], s[48:49], v[146:147] op_sel:[0,1,0] op_sel_hi:[1,1,1]\n\t"
    "v_pk_fma_f32 v[148:149], v[220:221], s[68:69], v[148:149] op_sel:[0,0,0] op_sel_hi:[1,0,1]\n\t"
    "v_pk_fma_f32 v[150:151], v[222:223], s[68:69], v[150:151] op_sel:[0,0,0] op_sel_hi:[1,0,1]\n\t"
    "v_pk_fma_f32 v[200:201], v[216:217], -2.0, v[200:201] op_sel:[0,0,0] op_sel_hi:[1,0,1]\n\t"
    "v_pk_fma_f32 v[202:203], v[218:219], -2.0, v[202:203] op_sel:[0,0,0] op_sel_hi:[1,0,1]\n\t"
    "v_pk_fma_f32 v[204:205], v[204:205], s[60:61], v[220:221] op_sel:[0,0,0] op_sel_hi:[1,0,1] neg_lo:[1,0,1] neg_hi:[1,0,1]\n\t"
    "v_pk_fma_f32 v[206:207], v[206:207], s[60:61], v[222:223] op_sel:[0,0,0] op_sel_hi:[1,0,1] neg_lo:[1,0,1] neg_hi:[1,0,1]\n\t"
    "v_pk_fma_f32 v[144:145], v[208:209], s[62:63], v[144:145] op_sel:[0,0,0] op_sel_hi:[1,0,1]\n\t"
    "v_pk_fma_f32 v[146:147], v[210:211], s[62:63], v[146:147] op_sel:[0,0,0] op_sel_hi:[1,0,1]\n\t"
    "v_pk_fma_f32 v[148:149], v[212:213], s[68:69], v[148:149] op_sel:[0,1,0] op_sel_hi:[1,1,1]\n\t"
    "v_pk_fma_f32 v[150:151], v[214:215], s[68:69], v[150:151] op_sel:[0,1,0] op_sel_hi:[1,1,1]\n\t"
    "v_pk_fma_f32 v[144:145], v[8:9], s[62:63], v[144:145] op_sel:[0,1,0] op_sel_hi:[1,1,1]\n\t"
    "v_pk_fma_f32 v[146:147], v[10:11], s[62:63], v[146:147] op_sel:[0,1,0] op_sel_hi:[1,1,1]\n\t"
    "v_pk_fma_f32 v[148:149], v[12:13], s[70:71], v[148:149] op_sel:[0,0,0] op_sel_hi:[1,0,1]\n\t"
    "v_pk_fma_f32 v[150:151], v[14:15], s[70:71], v[150:151] op_sel:[0,0,0] op_sel_hi:[1,0,1]\n\t"
    "v_pk_fma_f32 v[144:145], v[200:201], s[64:65], v[144:145] op_sel:[0,0,0] op_sel_hi:[1,0,1]\n\t"
    "v_pk_fma_f32 v[146:147], v[202:203], s[64:65], v[146:147] op_sel:[0,0,0] op_sel_hi:[1,0,1]\n\t"
    "v_pk_fma_f32 v[148:149], v[204:205], s[74:75], v[148:149] op_sel:[0,0,0] op_sel_hi:[1,0,1]\n\t"
    "v_pk_fma_f32 v[150:151], v[206:207], s[74:75], v[150:151] op_sel:[0,0,0] op_sel_hi:[1,0,1]\n\t"
    "v_pk_fma_f32 v[200:201], v[144:145], s[64:65], v[200:201] op_sel:[0,1,0] op_sel_hi:[1,1,1]\n\t"
    "v_pk_fma_f32 v[202:203], v[146:147], s[64:65], v[202:203] op_sel:[0,1,0] op_sel_hi:[1,1,1]\n\t"
    "v_pk_fma_f32 v[204:205], v[148:149], s[74:75], v[204:205] op_sel:[0,1,0] op_sel_hi:[1,1,1]\n\t"
    "v_pk_fma_f32 v[206:207], v[150:151], s[74:75], v[206:207] op_sel:[0,1,0] op_sel_hi:[1,1,1]\n\t"
    "v_pk_fma_f32 v[216:217], v[144:145], s[66:67], v[216:217] op_sel:[0,1,0] op_sel_hi:[1,1,1]\n\t"
    "v_pk_fma_f32 v[218:219], v[146:147], s[66:67], v[218:219] op_sel:[0,1,0] op_sel_hi:[1,1,1]\n\t"
    "v_pk_fma_f32 v[220:221], v[148:149], s[76:77], v[220:221] op_sel:[0,0,0] op_sel_hi:[1,0,1]\n\t"
    "v_pk_fma_f32 v[222:223], v[150:151], s[76:77], v[222:223] op_sel:[0,0,0] op_sel_hi:[1,0,1]\n\t"
    "v_pk_fma_f32 v[72:73], v[200:201], s[66:67], v[216:217] op_sel:[0,0,0] op_sel_hi:[1,0,1]\n\t"
    "v_pk_fma_f32 v[74:75], v[202:203], s[66:67], v[218:219] op_sel:[0,0,0] op_sel_hi:[1,0,1]\n\t"
    "v_pk_fma_f32 v[76:77], v[220:221], s[50:51], v[204:205] op_sel:[0,0,0] op_sel_hi:[1,0,1] neg_lo:[1,0,1] neg_hi:[1,0,1]\n\t"
    "v_pk_fma_f32 v[78:79], v[222:223], s[50:51], v[206:207] op_sel:[0,0,0] op_sel_hi:[1,0,1] neg_lo:[1,0,1] neg_hi:[1,0,1]\n\t"
    "v_pk_fma_f32 v[72:73], v[136:137], s[66:67], v[72:73] op_sel:[0,1,0] op_sel_hi:[1,1,1] neg_lo:[1,0,0] neg_hi:[1,0,0]\n\t"
    "v_pk_fma_f32 v[74:75], v[138:139], s[66:67], v[74:75] op_sel:[0,1,0] op_sel_hi:[1,1,1] neg_lo:[1,0,0] neg_hi:[1,0,0]\n\t"
    "v_pk_fma_f32 v[76:77], v[140:141], s[76:77], v[76:77] op_sel:[0,1,0] op_sel_hi:[1,1,1]\n\t"
    "v_pk_fma_f32 v[78:79], v[142:143], s[76:77], v[78:79] op_sel:[0,1,0] op_sel_hi:[1,1,1]\n\t"
    "v_pk_fma_f32 v[72:73], v[72:73], s[56:57], v[128:129] op_sel:[0,0,0] op_sel_hi:[1,0,0]\n\t"
    "v_pk_fma_f32 v[74:75], v[74:75], s[56:57], v[128:129] op_sel:[0,0,0] op_sel_hi:[1,0,0]\n\t"
    "v_exp_f32 v76, v76\n\t"
    "v_exp_f32 v77, v77\n\t"
    "v_exp_f32 v78, v78\n\t"
    "v_exp_f32 v79, v79\n\t"
    "v_exp_f32 v72, v72 clamp\n\t"
    "v_exp_f32 v73, v73 clamp\n\t"
    "v_exp_f32 v74, v74 clamp\n\t"
    "v_exp_f32 v75, v75 clamp\n\t"
    "v_pk_fma_f32 v[72:73], v[72:73], s[52:53], 0.5 op_sel:[0,1,0] op_sel_hi:[1,1,0]\n\t"
    "v_pk_fma_f32 v[74:75], v[74:75], s[52:53], 0.5 op_sel:[0,1,0] op_sel_hi:[1,1,0]\n\t"
    "v_pk_fma_f32 v[76:77], v[72:73], v[76:77], v[72:73]\n\t"
    "v_pk_fma_f32 v[78:79], v[74:75], v[78:79], v[74:75]\n\t"
    "v_rcp_f32 v76, v76\n\t"
    "v_rcp_f32 v77, v77\n\t"
    "v_rcp_f32 v78, v78\n\t"
    "v_rcp_f32 v79, v79\n\t"
    "v_pk_fma_f32 v[72:73], v[72:73], v[76:77], v[76:77] neg_lo:[0,0,1] neg_hi:[0,0,1]\n\t"
    "v_pk_fma_f32 v[74:75], v[74:75], v[78:79], v[78:79] neg_lo:[0,0,1] neg_hi:[0,0,1]\n\t"
    "v_cvt_pk_bf16_f32 v238, v72, v73\n\t"
    "v_cvt_pk_bf16_f32 v239, v74, v75\n\t"
    "ds_write2st64_b64 v243, v[236:237], v[238:239] offset0:8 offset1:12\n\t"
    "v_pk_fma_f32 v[8:9], v[8:9], s[60:61], v[128:129] op_sel:[0,0,0] op_sel_hi:[1,0,0] neg_lo:[1,0,0] neg_hi:[1,0,0]\n\t"
    "v_pk_fma_f32 v[10:11], v[10:11], s[60:61], v[128:129] op_sel:[0,0,0] op_sel_hi:[1,0,0] neg_lo:[1,0,0] neg_hi:[1,0,0]\n\t"
    "v_exp_f32 v12, v12\n\t"
    "v_exp_f32 v13, v13\n\t"
    "v_exp_f32 v14, v14\n\t"
    "v_exp_f32 v15, v15\n\t"
    "v_exp_f32 v8, v8 clamp\n\t"
    "v_exp_f32 v9, v9 clamp\n\t"
    "v_exp_f32 v10, v10 clamp\n\t"
    "v_exp_f32 v11, v11 clamp\n\t"
    "v_pk_fma_f32 v[8:9], v[8:9], s[52:53], 0.5 op_sel:[0,1,0] op_sel_hi:[1,1,0]\n\t"
    "v_pk_fma_f32 v[10:11], v[10:11], s[52:53], 0.5 op_sel:[0,1,0] op_sel_hi:[1,1,0]\n\t"
    "v_pk_fma_f32 v[12:13], v[8:9], v[12:13], v[8:9]\n\t"
    "v_pk_fma_f32 v[14:15], v[10:11], v[14:15], v[10:11]\n\t"
    "v_rcp_f32 v12, v12\n\t"
    "v_rcp_f32 v13, v13\n\t"
    "v_rcp_f32 v14, v14\n\t"
    "v_rcp_f32 v15, v15\n\t"
    "v_pk_fma_f32 v[8:9], v[8:9], v[12:13], v[12:13] neg_lo:[0,0,1] neg_hi:[0,0,1]\n\t"
    "v_pk_fma_f32 v[10:11], v[10:11], v[14:15], v[14:15] neg_lo:[0,0,1] neg_hi:[0,0,1]\n\t"
    "v_cvt_pk_bf16_f32 v232, v8, v9\n\t"
    "v_cvt_pk_bf16_f32 v233, v10, v11\n\t"
    "v_mfma_f32_32x32x16_bf16 a[128:143], a[208:211], a[208:211], 0\n\t"
    "s_waitcnt lgkmcnt(0)\n\t"
    "s_barrier\n\t"
    "ds_read_b128 v[224:227], v244 offset:0\n\t"
    "v_pk_fma_f32 v[200:201], v[200:201], s[58:59], v[128:129] op_sel:[0,1,0] op_sel_hi:[1,1,0]\n\t"
    "v_pk_fma_f32 v[202:203], v[202:203], s[58:59], v[128:129] op_sel:[0,1,0] op_sel_hi:[1,1,0]\n\t"
    "v_exp_f32 v204, v204\n\t"
    "v_exp_f32 v205, v205\n\t"
    "v_exp_f32 v206, v206\n\t"
    "v_exp_f32 v207, v207\n\t"
    "v_exp_f32 v200, v200 clamp\n\t"
    "v_exp_f32 v201, v201 clamp\n\t"
    "v_exp_f32 v202, v202 clamp\n\t"
    "v_exp_f32 v203, v203 clamp\n\t"
    "v_pk_fma_f32 v[200:201], v[200:201], s[52:53], 0.5 op_sel:[0,1,0] op_sel_hi:[1,1,0]\n\t"
    "v_pk_fma_f32 v[202:203], v[202:203], s[52:53], 0.5 op_sel:[0,1,0] op_sel_hi:[1,1,0]\n\t"
    "v_pk_fma_f32 v[204:205], v[200:201], v[204:205], v[200:201]\n\t"
    "v_pk_fma_f32 v[206:207], v[202:203], v[206:207], v[202:203]\n\t"
    "v_rcp_f32 v204, v204\n\t"
    "v_rcp_f32 v205, v205\n\t"
    "v_rcp_f32 v206, v206\n\t"
    "v_rcp_f32 v207, v207\n\t"
    "v_pk_fma_f32 v[200:201], v[200:201], v[204:205], v[204:205] neg_lo:[0,0,1] neg_hi:[0,0,1]\n\t"
    "v_pk_fma_f32 v[202:203], v[202:203], v[206:207], v[206:207] neg_lo:[0,0,1] neg_hi:[0,0,1]\n\t"
    "v_cvt_pk_bf16_f32 v234, v200, v201\n\t"
    "v_cvt_pk_bf16_f32 v235, v202, v203\n\t"
    "s_waitcnt lgkmcnt(0)\n\t"
    "s_nop 0\n\t"
    "buffer_store_dwordx4 v[224:227], v240, s[12:15], 0 offen sc1\n\t"
    "v_pk_fma_f32 v[208:209], v[208:209], s[60:61], v[128:129] op_sel:[0,0,0] op_sel_hi:[1,0,0]\n\t"
    "v_pk_fma_f32 v[210:211], v[210:211], s[60:61], v[128:129] op_sel:[0,0,0] op_sel_hi:[1,0,0]\n\t"
    "v_exp_f32 v212, v212\n\t"
    "v_exp_f32 v213, v213\n\t"
    "v_exp_f32 v214, v214\n\t"
    "v_exp_f32 v215, v215\n\t"
    "v_exp_f32 v208, v208 clamp\n\t"
    "v_exp_f32 v209, v209 clamp\n\t"
    "v_exp_f32 v210, v210 clamp\n\t"
    "v_exp_f32 v211, v211 clamp\n\t"
    "v_pk_fma_f32 v[208:209], v[208:209], s[52:53], 0.5 op_sel:[0,1,0] op_sel_hi:[1,1,0]\n\t"
    "v_pk_fma_f32 v[210:211], v[210:211], s[52:53], 0.5 op_sel:[0,1,0] op_sel_hi:[1,1,0]\n\t"
    "v_pk_fma_f32 v[212:213], v[208:209], v[212:213], v[208:209]\n\t"
    "v_pk_fma_f32 v[214:215], v[210:211], v[214:215], v[210:211]\n\t"
    "v_rcp_f32 v212, v212\n\t"
    "v_rcp_f32 v213, v213\n\t"
    "v_rcp_f32 v214, v214\n\t"
    "v_rcp_f32 v215, v215\n\t"
    "v_pk_fma_f32 v[208:209], v[208:209], v[212:213], v[212:213] neg_lo:[0,0,1] neg_hi:[0,0,1]\n\t"
    "v_pk_fma_f32 v[210:211], v[210:211], v[214:215], v[214:215] neg_lo:[0,0,1] neg_hi:[0,0,1]\n\t"
    "v_cvt_pk_bf16_f32 v236, v208, v209\n\t"
    "v_cvt_pk_bf16_f32 v237, v210, v211\n\t"
    "ds_read_b128 v[228:231], v244 offset:1024\n\t"
    "ds_write2st64_b64 v243, v[232:233], v[234:235] offset0:16 offset1:20\n\t"
    "v_accvgpr_read_b32 v200, a16\n\t"
    "v_accvgpr_read_b32 v201, a17\n\t"
    "v_accvgpr_read_b32 v202, a18\n\t"
    "v_accvgpr_read_b32 v203, a19\n\t"
    "v_accvgpr_read_b32 v204, a20\n\t"
    "v_accvgpr_read_b32 v205, a21\n\t"
    "v_accvgpr_read_b32 v206, a22\n\t"
    "v_accvgpr_read_b32 v207, a23\n\t"
    "v_accvgpr_read_b32 v208, a80\n\t"
    "v_accvgpr_read_b32 v209, a81\n\t"
    "v_accvgpr_read_b32 v210, a82\n\t"
    "v_accvgpr_read_b32 v211, a83\n\t"
    "v_accvgpr_read_b32 v212, a84\n\t"
    "v_accvgpr_read_b32 v213, a85\n\t"
    "v_accvgpr_read_b32 v214, a86\n\t"
    "v_accvgpr_read_b32 v215, a87\n\t"
    "v_pk_mul_f32 v[20:21], v[20:21], s[58:59] op_sel:[0,1] op_sel_hi:[1,1] neg_lo:[1,0] neg_hi:[1,0]\n\t"
    "v_pk_mul_f32 v[22:23], v[22:23], s[58:59] op_sel:[0,1] op_sel_hi:[1,1] neg_lo:[1,0] neg_hi:[1,0]\n\t"
    "v_pk_add_f32 v[216:217], v[16:17], v[80:81] neg_lo:[0,1] neg_hi:[0,1]\n\t"
    "v_pk_add_f32 v[218:219], v[18:19], v[82:83] neg_lo:[0,1] neg_hi:[0,1]\n\t"
    "v_pk_fma_f32 v[220:221], v[84:85], s[58:59], v[20:21] op_sel:[0,1,0] op_sel_hi:[1,1,1]\n\t"
    "v_pk_fma_f32 v[222:223], v[86:87], s[58:59], v[22:23] op_sel:[0,1,0] op_sel_hi:[1,1,1]\n\t"
    "v_pk_add_f32 v[16:17], v[16:17], v[80:81]\n\t"
    "v_pk_add_f32 v[18:19], v[18:19], v[82:83]\n\t"
    "v_pk_fma_f32 v[20:21], v[84:85], s[58:59], v[20:21] op_sel:[0,1,0] op_sel_hi:[1,1,1] neg_lo:[1,0,0] neg_hi:[1,0,0]\n\t"
    "v_pk_fma_f32 v[22:23], v[86:87], s[58:59], v[22:23] op_sel:[0,1,0] op_sel_hi:[1,1,1] neg_lo:[1,0,0] neg_hi:[1,0,0]\n\t"
    "v_pk_add_f32 v[80:81], v[200:201], v[208:209]\n\t"
    "v_pk_add_f32 v[82:83], v[202:203], v[210:211]\n\t"
    "v_pk_add_f32 v[84:85], v[204:205], v[212:213]\n\t"
    "v_pk_add_f32 v[86:87], v[206:207], v[214:215]\n\t"
    "v_pk_add_f32 v[200:201], v[200:201], v[208:209] neg_lo:[0,1] neg_hi:[0,1]\n\t"
    "v_pk_add_f32 v[202:203], v[202:203], v[210:211] neg_lo:[0,1] neg_hi:[0,1]\n\t"
    "v_pk_add_f32 v[204:205], v[204:205], v[212:213] neg_lo:[0,1] neg_hi:[0,1]\n\t"
    "v_pk_add_f32 v[206:207], v[206:207], v[214:215] neg_lo:[0,1] neg_hi:[0,1]\n\t"
    "v_pk_fma_f32 v[208:209], v[16:17], -4.0, v[80:81] op_sel:[0,0,0] op_sel_hi:[1,0,1]\n\t"
    "v_pk_fma_f32 v[210:211], v[18:19], -4.0, v[82:83] op_sel:[0,0,0] op_sel_hi:[1,0,1]\n\t"
    "v_pk_fma_f32 v[212:213], v[84:85], s[60:61], v[20:21] op_sel:[0,1,0] op_sel_hi:[1,1,1] neg_lo:[1,0,1] neg_hi:[1,0,1]\n\t"
    "v_pk_fma_f32 v[214:215], v[86:87], s[60:61], v[22:23] op_sel:[0,1,0] op_sel_hi:[1,1,1] neg_lo:[1,0,1] neg_hi:[1,0,1]\n\t"
    "v_pk_fma_f32 v[16:17], v[16:17], s[50:51], v[80:81] op_sel:[0,1,0] op_sel_hi:[1,1,1] neg_lo:[1,0,0] neg_hi:[1,0,0]\n\t"
    "v_pk_fma_f32 v[18:19], v[18:19], s[50:51], v[82:83] op_sel:[0,1,0] op_sel_hi:[1,1,1] neg_lo:[1,0,0] neg_hi:[1,0,0]\n\t"
    "v_pk_fma_f32 v[20:21], v[20:21], s[50:51], v[212:213] op_sel:[0,0,0] op_sel_hi:[1,0,1] neg_lo:[1,0,1] neg_hi:[1,0,1]\n\t"
    "v_pk_fma_f32 v[22:23], v[22:23], s[50:51], v[214:215] op_sel:[0,0,0] op_sel_hi:[1,0,1] neg_lo:[1,0,1] neg_hi:[1,0,1]\n\t"
    "v_pk_fma_f32 v[152:153], v[216:217], s[48:49], v[152:153] op_sel:[0,1,0] op_sel_hi:[1,1,1]\n\t"
    "v_pk_fma_f32 v[154:155], v[218:219], s[48:49], v[154:155] op_sel:[0,1,0] op_sel_hi:[1,1,1]\n\t"
    "v_pk_fma_f32 v[156:157], v[220:221], s[68:69], v[156:157] op_sel:[0,0,0] op_sel_hi:[1,0,1]\n\t"
    "v_pk_fma_f32 v[158:159], v[222:223], s[68:69], v[158:159] op_sel:[0,0,0] op_sel_hi:[1,0,1]\n\t"
    "v_pk_fma_f32 v[200:201], v[216:217], -2.0, v[200:201] op_sel:[0,0,0] op_sel_hi:[1,0,1]\n\t"
    "v_pk_fma_f32 v[202:203], v[218:219], -2.0, v[202:203] op_sel:[0,0,0] op_sel_hi:[1,0,1]\n\t"
    "v_pk_fma_f32 v[204:205], v[204:205], s[60:61], v[220:221] op_sel:[0,0,0] op_sel_hi:[1,0,1] neg_lo:[1,0,1] neg_hi:[1,0,1]\n\t"
    "v_pk_fma_f32 v[206:207], v[206:207], s[60:61], v[222:223] op_sel:[0,0,0] op_sel_hi:[1,0,1] neg_lo:[1,0,1] neg_hi:[1,0,1]\n\t"
    "v_pk_fma_f32 v[152:153], v[208:209], s[62:63], v[152:153] op_sel:[0,0,0] op_sel_hi:[1,0,1]\n\t"
    "v_pk_fma_f32 v[154:155], v[210:211], s[62:63], v[154:155] op_sel:[0,0,0] op_sel_hi:[1,0,1]\n\t"
    "v_pk_fma_f32 v[156:157], v[212:213], s[68:69], v[156:157] op_sel:[0,1,0] op_sel_hi:[1,1,1]\n\t"
    "v_pk_fma_f32 v[158:159], v[214:215], s[68:69], v[158:159] op_sel:[0,1,0] op_sel_hi:[1,1,1]\n\t"
    "v_pk_fma_f32 v[152:153], v[16:17], s[62:63], v[152:153] op_sel:[0,1,0] op_sel_hi:[1,1,1]\n\t"
    "v_pk_fma_f32 v[154:155], v[18:19], s[62:63], v[154:155] op_sel:[0,1,0] op_sel_hi:[1,1,1]\n\t"
    "v_pk_fma_f32 v[156:157], v[20:21], s[70:71], v[156:157] op_sel:[0,0,0] op_sel_hi:[1,0,1]\n\t"
    "v_pk_fma_f32 v[158:159], v[22:23], s[70:71], v[158:159] op_sel:[0,0,0] op_sel_hi:[1,0,1]\n\t"
    "v_pk_fma_f32 v[152:153], v[200:201], s[64:65], v[152:153] op_sel:[0,0,0] op_sel_hi:[1,0,1]\n\t"
    "v_pk_fma_f32 v[154:155], v[202:203], s[64:65], v[154:155] op_sel:[0,0,0] op_sel_hi:[1,0,1]\n\t"
    "v_pk_fma_f32 v[156:157], v[204:205], s[74:75], v[156:157] op_sel:[0,0,0] op_sel_hi:[1,0,1]\n\t"
    "v_pk_fma_f32 v[158:159], v[206:207], s[74:75], v[158:159] op_sel:[0,0,0] op_sel_hi:[1,0,1]\n\t"
    "v_pk_fma_f32 v[200:201], v[152:153], s[64:65], v[200:201] op_sel:[0,1,0] op_sel_hi:[1,1,1]\n\t"
    "v_pk_fma_f32 v[202:203], v[154:155], s[64:65], v[202:203] op_sel:[0,1,0] op_sel_hi:[1,1,1]\n\t"
    "v_pk_fma_f32 v[204:205], v[156:157], s[74:75], v[204:205] op_sel:[0,1,0] op_sel_hi:[1,1,1]\n\t"
    "v_pk_fma_f32 v[206:207], v[158:159], s[74:75], v[206:207] op_sel:[0,1,0] op_sel_hi:[1,1,1]\n\t"
    "v_pk_fma_f32 v[216:217], v[152:153], s[66:67], v[216:217] op_sel:[0,1,0] op_sel_hi:[1,1,1]\n\t"
    "v_pk_fma_f32 v[218:219], v[154:155], s[66:67], v[218:219] op_sel:[0,1,0] op_sel_hi:[1,1,1]\n\t"
    "v_pk_fma_f32 v[220:221], v[156:157], s[76:77], v[220:221] op_sel:[0,0,0] op_sel_hi:[1,0,1]\n\t"
    "v_pk_fma_f32 v[222:223], v[158:159], s[76:77], v[222:223] op_sel:[0,0,0] op_sel_hi:[1,0,1]\n\t"
    "v_pk_fma_f32 v[80:81], v[200:201], s[66:67], v[216:217] op_sel:[0,0,0] op_sel_hi:[1,0,1]\n\t"
    "v_pk_fma_f32 v[82:83], v[202:203], s[66:67], v[218:219] op_sel:[0,0,0] op_sel_hi:[1,0,1]\n\t"
    "v_pk_fma_f32 v[84:85], v[220:221], s[50:51], v[204:205] op_sel:[0,0,0] op_sel_hi:[1,0,1] neg_lo:[1,0,1] neg_hi:[1,0,1]\n\t"
    "v_pk_fma_f32 v[86:87], v[222:223], s[50:51], v[206:207] op_sel:[0,0,0] op_sel_hi:[1,0,1] neg_lo:[1,0,1] neg_hi:[1,0,1]\n\t"
    "v_pk_fma_f32 v[80:81], v[144:145], s[66:67], v[80:81] op_sel:[0,1,0] op_sel_hi:[1,1,1] neg_lo:[1,0,0] neg_hi:[1,0,0]\n\t"
    "v_pk_fma_f32 v[82:83], v[146:147], s[66:67], v[82:83] op_sel:[0,1,0] op_sel_hi:[1,1,1] neg_lo:[1,0,0] neg_hi:[1,0,0]\n\t"
    "v_pk_fma_f32 v[84:85], v[148:149], s[76:77], v[84:85] op_sel:[0,1,0] op_sel_hi:[1,1,1]\n\t"
    "v_pk_fma_f32 v[86:87], v[150:151], s[76:77], v[86:87] op_sel:[0,1,0] op_sel_hi:[1,1,1]\n\t"
    "v_pk_fma_f32 v[80:81], v[80:81], s[56:57], v[128:129] op_sel:[0,0,0] op_sel_hi:[1,0,0]\n\t"
    "v_pk_fma_f32 v[82:83], v[82:83], s[56:57], v[128:129] op_sel:[0,0,0] op_sel_hi:[1,0,0]\n\t"
    "v_exp_f32 v84, v84\n\t"
    "v_exp_f32 v85, v85\n\t"
    "v_exp_f32 v86, v86\n\t"
    "v_exp_f32 v87, v87\n\t"
    "v_exp_f32 v80, v80 clamp\n\t"
    "v_exp_f32 v81, v81 clamp\n\t"
    "v_exp_f32 v82, v82 clamp\n\t"
    "v_exp_f32 v83, v83 clamp\n\t"
    "v_pk_fma_f32 v[80:81], v[80:81], s[52:53], 0.5 op_sel:[0,1,0] op_sel_hi:[1,1,0]\n\t"
    "v_pk_fma_f32 v[82:83], v[82:83], s[52:53], 0.5 op_sel:[0,1,0] op_sel_hi:[1,1,0]\n\t"
    "v_pk_fma_f32 v[84:85], v[80:81], v[84:85], v[80:81]\n\t"
    "v_pk_fma_f32 v[86:87], v[82:83], v[86:87], v[82:83]\n\t"
    "v_rcp_f32 v84, v84\n\t"
    "v_rcp_f32 v85, v85\n\t"
    "v_rcp_f32 v86, v86\n\t"
    "v_rcp_f32 v87, v87\n\t"
    "v_pk_fma_f32 v[80:81], v[80:81], v[84:85], v[84:85] neg_lo:[0,0,1] neg_hi:[0,0,1]\n\t"
    "v_pk_fma_f32 v[82:83], v[82:83], v[86:87], v[86:87] neg_lo:[0,0,1] neg_hi:[0,0,1]\n\t"
    "v_cvt_pk_bf16_f32 v238, v80, v81\n\t"
    "v_cvt_pk_bf16_f32 v239, v82, v83\n\t"
    "s_waitcnt lgkmcnt(1)\n\t"
    "s_nop 0\n\t"
    "buffer_store_dwordx4 v[228:231], v241, s[12:15], 0 offen sc1\n\t"
    "ds_write2st64_b64 v243, v[236:237], v[238:239] offset0:24 offset1:28\n\t"
    "v_pk_fma_f32 v[16:17], v[16:17], s[60:61], v[128:129] op_sel:[0,0,0] op_sel_hi:[1,0,0] neg_lo:[1,0,0] neg_hi:[1,0,0]\n\t"
    "v_pk_fma_f32 v[18:19], v[18:19], s[60:61], v[128:129] op_sel:[0,0,0] op_sel_hi:[1,0,0] neg_lo:[1,0,0] neg_hi:[1,0,0]\n\t"
    "v_exp_f32 v20, v20\n\t"
    "v_exp_f32 v21, v21\n\t"
    "v_exp_f32 v22, v22\n\t"
    "v_exp_f32 v23, v23\n\t"
    "v_exp_f32 v16, v16 clamp\n\t"
    "v_exp_f32 v17, v17 clamp\n\t"
    "v_exp_f32 v18, v18 clamp\n\t"
    "v_exp_f32 v19, v19 clamp\n\t"
    "v_pk_fma_f32 v[16:17], v[16:17], s[52:53], 0.5 op_sel:[0,1,0] op_sel_hi:[1,1,0]\n\t"
    "v_pk_fma_f32 v[18:19], v[18:19], s[52:53], 0.5 op_sel:[0,1,0] op_sel_hi:[1,1,0]\n\t"
    "v_pk_fma_f32 v[20:21], v[16:17], v[20:21], v[16:17]\n\t"
    "v_pk_fma_f32 v[22:23], v[18:19], v[22:23], v[18:19]\n\t"
    "v_rcp_f32 v20, v20\n\t"
    "v_rcp_f32 v21, v21\n\t"
    "v_rcp_f32 v22, v22\n\t"
    "v_rcp_f32 v23, v23\n\t"
    "v_pk_fma_f32 v[16:17], v[16:17], v[20:21], v[20:21] neg_lo:[0,0,1] neg_hi:[0,0,1]\n\t"
    "v_pk_fma_f32 v[18:19], v[18:19], v[22:23], v[22:23] neg_lo:[0,0,1] neg_hi:[0,0,1]\n\t"
    "v_cvt_pk_bf16_f32 v232, v16, v17\n\t"
    "v_cvt_pk_bf16_f32 v233, v18, v19\n\t"
    "v_pk_fma_f32 v[200:201], v[200:201], s[58:59], v[128:129] op_sel:[0,1,0] op_sel_hi:[1,1,0]\n\t"
    "v_pk_fma_f32 v[202:203], v[202:203], s[58:59], v[128:129] op_sel:[0,1,0] op_sel_hi:[1,1,0]\n\t"
    "v_exp_f32 v204, v204\n\t"
    "v_exp_f32 v205, v205\n\t"
    "v_exp_f32 v206, v206\n\t"
    "v_exp_f32 v207, v207\n\t"
    "v_exp_f32 v200, v200 clamp\n\t"
    "v_exp_f32 v201, v201 clamp\n\t"
    "v_exp_f32 v202, v202 clamp\n\t"
    "v_exp_f32 v203, v203 clamp\n\t"
    "v_pk_fma_f32 v[200:201], v[200:201], s[52:53], 0.5 op_sel:[0,1,0] op_sel_hi:[1,1,0]\n\t"
    "v_pk_fma_f32 v[202:203], v[202:203], s[52:53], 0.5 op_sel:[0,1,0] op_sel_hi:[1,1,0]\n\t"
    "v_pk_fma_f32 v[204:205], v[200:201], v[204:205], v[200:201]\n\t"
    "v_pk_fma_f32 v[206:207], v[202:203], v[206:207], v[202:203]\n\t"
    "v_rcp_f32 v204, v204\n\t"
    "v_rcp_f32 v205, v205\n\t"
    "v_rcp_f32 v206, v206\n\t"
    "v_rcp_f32 v207, v207\n\t"
    "v_pk_fma_f32 v[200:201], v[200:201], v[204:205], v[204:205] neg_lo:[0,0,1] neg_hi:[0,0,1]\n\t"
    "v_pk_fma_f32 v[202:203], v[202:203], v[206:207], v[206:207] neg_lo:[0,0,1] neg_hi:[0,0,1]\n\t"
    "v_cvt_pk_bf16_f32 v234, v200, v201\n\t"
    "v_cvt_pk_bf16_f32 v235, v202, v203\n\t"
    "v_pk_fma_f32 v[208:209], v[208:209], s[60:61], v[128:129] op_sel:[0,0,0] op_sel_hi:[1,0,0]\n\t"
    "v_pk_fma_f32 v[210:211], v[210:211], s[60:61], v[128:129] op_sel:[0,0,0] op_sel_hi:[1,0,0]\n\t"
    "v_exp_f32 v212, v212\n\t"
    "v_exp_f32 v213, v213\n\t"
    "v_exp_f32 v214, v214\n\t"
    "v_exp_f32 v215, v215\n\t"
    "v_exp_f32 v208, v208 clamp\n\t"
    "v_exp_f32 v209, v209 clamp\n\t"
    "v_exp_f32 v210, v210 clamp\n\t"
    "v_exp_f32 v211, v211 clamp\n\t"
    "v_pk_fma_f32 v[208:209], v[208:209], s[52:53], 0.5 op_sel:[0,1,0] op_sel_hi:[1,1,0]\n\t"
    "v_pk_fma_f32 v[210:211], v[210:211], s[52:53], 0.5 op_sel:[0,1,0] op_sel_hi:[1,1,0]\n\t"
    "v_pk_fma_f32 v[212:213], v[208:209], v[212:213], v[208:209]\n\t"
    "v_pk_fma_f32 v[214:215], v[210:211], v[214:215], v[210:211]\n\t"
    "v_rcp_f32 v212, v212\n\t"
    "v_rcp_f32 v213, v213\n\t"
    "v_rcp_f32 v214, v214\n\t"
    "v_rcp_f32 v215, v215\n\t"
    "v_pk_fma_f32 v[208:209], v[208:209], v[212:213], v[212:213] neg_lo:[0,0,1] neg_hi:[0,0,1]\n\t"
    "v_pk_fma_f32 v[210:211], v[210:211], v[214:215], v[214:215] neg_lo:[0,0,1] neg_hi:[0,0,1]\n\t"
    "v_cvt_pk_bf16_f32 v236, v208, v209\n\t"
    "v_cvt_pk_bf16_f32 v237, v210, v211\n\t"
    "ds_write2st64_b64 v243, v[232:233], v[234:235] offset0:32 offset1:36\n\t"
    "v_accvgpr_read_b32 v200, a24\n\t"
    "v_accvgpr_read_b32 v201, a25\n\t"
    "v_accvgpr_read_b32 v202, a26\n\t"
    "v_accvgpr_read_b32 v203, a27\n\t"
    "v_accvgpr_read_b32 v204, a28\n\t"
    "v_accvgpr_read_b32 v205, a29\n\t"
    "v_accvgpr_read_b32 v206, a30\n\t"
    "v_accvgpr_read_b32 v207, a31\n\t"
    "v_accvgpr_read_b32 v208, a88\n\t"
    "v_accvgpr_read_b32 v209, a89\n\t"
    "v_accvgpr_read_b32 v210, a90\n\t"
    "v_accvgpr_read_b32 v211, a91\n\t"
    "v_accvgpr_read_b32 v212, a92\n\t"
    "v_accvgpr_read_b32 v213, a93\n\t"
    "v_accvgpr_read_b32 v214, a94\n\t"
    "v_accvgpr_read_b32 v215, a95\n\t"
    "v_pk_mul_f32 v[28:29], v[28:29], s[58:59] op_sel:[0,1] op_sel_hi:[1,1] neg_lo:[1,0] neg_hi:[1,0]\n\t"
    "v_pk_mul_f32 v[30:31], v[30:31], s[58:59] op_sel:[0,1] op_sel_hi:[1,1] neg_lo:[1,0] neg_hi:[1,0]\n\t"
    "v_pk_add_f32 v[216:217], v[24:25], v[88:89] neg_lo:[0,1] neg_hi:[0,1]\n\t"
    "v_pk_add_f32 v[218:219], v[26:27], v[90:91] neg_lo:[0,1] neg_hi:[0,1]\n\t"
    "v_pk_fma_f32 v[220:221], v[92:93], s[58:59], v[28:29] op_sel:[0,1,0] op_sel_hi:[1,1,1]\n\t"
    "v_pk_fma_f32 v[222:223], v[94:95], s[58:59], v[30:31] op_sel:[0,1,0] op_sel_hi:[1,1,1]\n\t"
    "v_pk_add_f32 v[24:25], v[24:25], v[88:89]\n\t"
    "v_pk_add_f32 v[26:27], v[26:27], v[90:91]\n\t"
    "v_pk_fma_f32 v[28:29], v[92:93], s[58:59], v[28:29] op_sel:[0,1,0] op_sel_hi:[1,1,1] neg_lo:[1,0,0] neg_hi:[1,0,0]\n\t"
    "v_pk_fma_f32 v[30:31], v[94:95], s[58:59], v[30:31] op_sel:[0,1,0] op_sel_hi:[1,1,1] neg_lo:[1,0,0] neg_hi:[1,0,0]\n\t"
    "v_pk_add_f32 v[88:89], v[200:201], v[208:209]\n\t"
    "v_pk_add_f32 v[90:91], v[202:203], v[210:211]\n\t"
    "v_pk_add_f32 v[92:93], v[204:205], v[212:213]\n\t"
    "v_pk_add_f32 v[94:95], v[206:207], v[214:215]\n\t"
    "v_pk_add_f32 v[200:201], v[200:201], v[208:209] neg_lo:[0,1] neg_hi:[0,1]\n\t"
    "v_pk_add_f32 v[202:203], v[202:203], v[210:211] neg_lo:[0,1] neg_hi:[0,1]\n\t"
    "v_pk_add_f32 v[204:205], v[204:205], v[212:213] neg_lo:[0,1] neg_hi:[0,1]\n\t"
    "v_pk_add_f32 v[206:207], v[206:207], v[214:215] neg_lo:[0,1] neg_hi:[0,1]\n\t"
    "v_pk_fma_f32 v[208:209], v[24:25], -4.0, v[88:89] op_sel:[0,0,0] op_sel_hi:[1,0,1]\n\t"
    "v_pk_fma_f32 v[210:211], v[26:27], -4.0, v[90:91] op_sel:[0,0,0] op_sel_hi:[1,0,1]\n\t"
    "v_pk_fma_f32 v[212:213], v[92:93], s[60:61], v[28:29] op_sel:[0,1,0] op_sel_hi:[1,1,1] neg_lo:[1,0,1] neg_hi:[1,0,1]\n\t"
    "v_pk_fma_f32 v[214:215], v[94:95], s[60:61], v[30:31] op_sel:[0,1,0] op_sel_hi:[1,1,1] neg_lo:[1,0,1] neg_hi:[1,0,1]\n\t"
    "v_pk_fma_f32 v[24:25], v[24:25], s[50:51], v[88:89] op_sel:[0,1,0] op_sel_hi:[1,1,1] neg_lo:[1,0,0] neg_hi:[1,0,0]\n\t"
    "v_pk_fma_f32 v[26:27], v[26:27], s[50:51], v[90:91] op_sel:[0,1,0] op_sel_hi:[1,1,1] neg_lo:[1,0,0] neg_hi:[1,0,0]\n\t"
    "v_pk_fma_f32 v[28:29], v[28:29], s[50:51], v[212:213] op_sel:[0,0,0] op_sel_hi:[1,0,1] neg_lo:[1,0,1] neg_hi:[1,0,1]\n\t"
    "v_pk_fma_f32 v[30:31], v[30:31], s[50:51], v[214:215] op_sel:[0,0,0] op_sel_hi:[1,0,1] neg_lo:[1,0,1] neg_hi:[1,0,1]\n\t"
    "v_pk_fma_f32 v[160:161], v[216:217], s[48:49], v[160:161] op_sel:[0,1,0] op_sel_hi:[1,1,1]\n\t"
    "v_pk_fma_f32 v[162:163], v[218:219], s[48:49], v[162:163] op_sel:[0,1,0] op_sel_hi:[1,1,1]\n\t"
    "v_pk_fma_f32 v[164:165], v[220:221], s[68:69], v[164:165] op_sel:[0,0,0] op_sel_hi:[1,0,1]\n\t"
    "v_pk_fma_f32 v[166:167], v[222:223], s[68:69], v[166:167] op_sel:[0,0,0] op_sel_hi:[1,0,1]\n\t"
    "v_pk_fma_f32 v[200:201], v[216:217], -2.0, v[200:201] op_sel:[0,0,0] op_sel_hi:[1,0,1]\n\t"
    "v_pk_fma_f32 v[202:203], v[218:219], -2.0, v[202:203] op_sel:[0,0,0] op_sel_hi:[1,0,1]\n\t"
    "v_pk_fma_f32 v[204:205], v[204:205], s[60:61], v[220:221] op_sel:[0,0,0] op_sel_hi:[1,0,1] neg_lo:[1,0,1] neg_hi:[1,0,1]\n\t"
    "v_pk_fma_f32 v[206:207], v[206:207], s[60:61], v[222:223] op_sel:[0,0,0] op_sel_hi:[1,0,1] neg_lo:[1,0,1] neg_hi:[1,0,1]\n\t"
    "v_pk_fma_f32 v[160:161], v[208:209], s[62:63], v[160:161] op_sel:[0,0,0] op_sel_hi:[1,0,1]\n\t"
    "v_pk_fma_f32 v[162:163], v[210:211], s[62:63], v[162:163] op_sel:[0,0,0] op_sel_hi:[1,0,1]\n\t"
    "v_pk_fma_f32 v[164:165], v[212:213], s[68:69], v[164:165] op_sel:[0,1,0] op_sel_hi:[1,1,1]\n\t"
    "v_pk_fma_f32 v[166:167], v[214:215], s[68:69], v[166:167] op_sel:[0,1,0] op_sel_hi:[1,1,1]\n\t"
    "v_pk_fma_f32 v[160:161], v[24:25], s[62:63], v[160:161] op_sel:[0,1,0] op_sel_hi:[1,1,1]\n\t"
    "v_pk_fma_f32 v[162:163], v[26:27], s[62:63], v[162:163] op_sel:[0,1,0] op_sel_hi:[1,1,1]\n\t"
    "v_pk_fma_f32 v[164:165], v[28:29], s[70:71], v[164:165] op_sel:[0,0,0] op_sel_hi:[1,0,1]\n\t"
    "v_pk_fma_f32 v[166:167], v[30:31], s[70:71], v[166:167] op_sel:[0,0,0] op_sel_hi:[1,0,1]\n\t"
    "v_pk_fma_f32 v[160:161], v[200:201], s[64:65], v[160:161] op_sel:[0,0,0] op_sel_hi:[1,0,1]\n\t"
    "v_pk_fma_f32 v[162:163], v[202:203], s[64:65], v[162:163] op_sel:[0,0,0] op_sel_hi:[1,0,1]\n\t"
    "v_pk_fma_f32 v[164:165], v[204:205], s[74:75], v[164:165] op_sel:[0,0,0] op_sel_hi:[1,0,1]\n\t"
    "v_pk_fma_f32 v[166:167], v[206:207], s[74:75], v[166:167] op_sel:[0,0,0] op_sel_hi:[1,0,1]\n\t"
    "v_pk_fma_f32 v[200:201], v[160:161], s[64:65], v[200:201] op_sel:[0,1,0] op_sel_hi:[1,1,1]\n\t"
    "v_pk_fma_f32 v[202:203], v[162:163], s[64:65], v[202:203] op_sel:[0,1,0] op_sel_hi:[1,1,1]\n\t"
    "v_pk_fma_f32 v[204:205], v[164:165], s[74:75], v[204:205] op_sel:[0,1,0] op_sel_hi:[1,1,1]\n\t"
    "v_pk_fma_f32 v[206:207], v[166:167], s[74:75], v[206:207] op_sel:[0,1,0] op_sel_hi:[1,1,1]\n\t"
    "v_pk_fma_f32 v[216:217], v[160:161], s[66:67], v[216:217] op_sel:[0,1,0] op_sel_hi:[1,1,1]\n\t"
    "v_pk_fma_f32 v[218:219], v[162:163], s[66:67], v[218:219] op_sel:[0,1,0] op_sel_hi:[1,1,1]\n\t"
    "v_pk_fma_f32 v[220:221], v[164:165], s[76:77], v[220:221] op_sel:[0,0,0] op_sel_hi:[1,0,1]\n\t"
    "v_pk_fma_f32 v[222:223], v[166:167], s[76:77], v[222:223] op_sel:[0,0,0] op_sel_hi:[1,0,1]\n\t"
    "v_pk_fma_f32 v[88:89], v[200:201], s[66:67], v[216:217] op_sel:[0,0,0] op_sel_hi:[1,0,1]\n\t"
    "v_pk_fma_f32 v[90:91], v[202:203], s[66:67], v[218:219] op_sel:[0,0,0] op_sel_hi:[1,0,1]\n\t"
    "v_pk_fma_f32 v[92:93], v[220:221], s[50:51], v[204:205] op_sel:[0,0,0] op_sel_hi:[1,0,1] neg_lo:[1,0,1] neg_hi:[1,0,1]\n\t"
    "v_pk_fma_f32 v[94:95], v[222:223], s[50:51], v[206:207] op_sel:[0,0,0] op_sel_hi:[1,0,1] neg_lo:[1,0,1] neg_hi:[1,0,1]\n\t"
    "v_pk_fma_f32 v[88:89], v[152:153], s[66:67], v[88:89] op_sel:[0,1,0] op_sel_hi:[1,1,1] neg_lo:[1,0,0] neg_hi:[1,0,0]\n\t"
    "v_pk_fma_f32 v[90:91], v[154:155], s[66:67], v[90:91] op_sel:[0,1,0] op_sel_hi:[1,1,1] neg_lo:[1,0,0] neg_hi:[1,0,0]\n\t"
    "v_pk_fma_f32 v[92:93], v[156:157], s[76:77], v[92:93] op_sel:[0,1,0] op_sel_hi:[1,1,1]\n\t"
    "v_pk_fma_f32 v[94:95], v[158:159], s[76:77], v[94:95] op_sel:[0,1,0] op_sel_hi:[1,1,1]\n\t"
    "v_pk_fma_f32 v[88:89], v[88:89], s[56:57], v[128:129] op_sel:[0,0,0] op_sel_hi:[1,0,0]\n\t"
    "v_pk_fma_f32 v[90:91], v[90:91], s[56:57], v[128:129] op_sel:[0,0,0] op_sel_hi:[1,0,0]\n\t"
    "v_exp_f32 v92, v92\n\t"
    "v_exp_f32 v93, v93\n\t"
    "v_exp_f32 v94, v94\n\t"
    "v_exp_f32 v95, v95\n\t"
    "v_exp_f32 v88, v88 clamp\n\t"
    "v_exp_f32 v89, v89 clamp\n\t"
    "v_exp_f32 v90, v90 clamp\n\t"
    "v_exp_f32 v91, v91 clamp\n\t"
    "v_pk_fma_f32 v[88:89], v[88:89], s[52:53], 0.5 op_sel:[0,1,0] op_sel_hi:[1,1,0]\n\t"
    "v_pk_fma_f32 v[90:91], v[90:91], s[52:53], 0.5 op_sel:[0,1,0] op_sel_hi:[1,1,0]\n\t"
    "v_pk_fma_f32 v[92:93], v[88:89], v[92:93], v[88:89]\n\t"
    "v_pk_fma_f32 v[94:95], v[90:91], v[94:95], v[90:91]\n\t"
    "v_rcp_f32 v92, v92\n\t"
    "v_rcp_f32 v93, v93\n\t"
    "v_rcp_f32 v94, v94\n\t"
    "v_rcp_f32 v95, v95\n\t"
    "v_pk_fma_f32 v[88:89], v[88:89], v[92:93], v[92:93] neg_lo:[0,0,1] neg_hi:[0,0,1]\n\t"
    "v_pk_fma_f32 v[90:91], v[90:91], v[94:95], v[94:95] neg_lo:[0,0,1] neg_hi:[0,0,1]\n\t"
    "v_cvt_pk_bf16_f32 v238, v88, v89\n\t"
    "v_cvt_pk_bf16_f32 v239, v90, v91\n\t"
    "ds_write2st64_b64 v243, v[236:237], v[238:239] offset0:40 offset1:44\n\t"
    "v_pk_fma_f32 v[24:25], v[24:25], s[60:61], v[128:129] op_sel:[0,0,0] op_sel_hi:[1,0,0] neg_lo:[1,0,0] neg_hi:[1,0,0]\n\t"
    "v_pk_fma_f32 v[26:27], v[26:27], s[60:61], v[128:129] op_sel:[0,0,0] op_sel_hi:[1,0,0] neg_lo:[1,0,0] neg_hi:[1,0,0]\n\t"
    "v_exp_f32 v28, v28\n\t"
    "v_exp_f32 v29, v29\n\t"
    "v_exp_f32 v30, v30\n\t"
    "v_exp_f32 v31, v31\n\t"
    "v_exp_f32 v24, v24 clamp\n\t"
    "v_exp_f32 v25, v25 clamp\n\t"
    "v_exp_f32 v26, v26 clamp\n\t"
    "v_exp_f32 v27, v27 clamp\n\t"
    "v_pk_fma_f32 v[24:25], v[24:25], s[52:53], 0.5 op_sel:[0,1,0] op_sel_hi:[1,1,0]\n\t"
    "v_pk_fma_f32 v[26:27], v[26:27], s[52:53], 0.5 op_sel:[0,1,0] op_sel_hi:[1,1,0]\n\t"
    "v_pk_fma_f32 v[28:29], v[24:25], v[28:29], v[24:25]\n\t"
    "v_pk_fma_f32 v[30:31], v[26:27], v[30:31], v[26:27]\n\t"
    "v_rcp_f32 v28, v28\n\t"
    "v_rcp_f32 v29, v29\n\t"
    "v_rcp_f32 v30, v30\n\t"
    "v_rcp_f32 v31, v31\n\t"
    "v_pk_fma_f32 v[24:25], v[24:25], v[28:29], v[28:29] neg_lo:[0,0,1] neg_hi:[0,0,1]\n\t"
    "v_pk_fma_f32 v[26:27], v[26:27], v[30:31], v[30:31] neg_lo:[0,0,1] neg_hi:[0,0,1]\n\t"
    "v_cvt_pk_bf16_f32 v232, v24, v25\n\t"
    "v_cvt_pk_bf16_f32 v233, v26, v27\n\t"
    "v_mfma_f32_32x32x16_bf16 a[128:143], a[208:211], a[208:211], 0\n\t"
    "s_waitcnt lgkmcnt(0)\n\t"
    "s_barrier\n\t"
    "ds_read_b128 v[224:227], v244 offset:8192\n\t"
    "v_pk_fma_f32 v[200:201], v[200:201], s[58:59], v[128:129] op_sel:[0,1,0] op_sel_hi:[1,1,0]\n\t"
    "v_pk_fma_f32 v[202:203], v[202:203], s[58:59], v[128:129] op_sel:[0,1,0] op_sel_hi:[1,1,0]\n\t"
    "v_exp_f32 v204, v204\n\t"
    "v_exp_f32 v205, v205\n\t"
    "v_exp_f32 v206, v206\n\t"
    "v_exp_f32 v207, v207\n\t"
    "v_exp_f32 v200, v200 clamp\n\t"
    "v_exp_f32 v201, v201 clamp\n\t"
    "v_exp_f32 v202, v202 clamp\n\t"
    "v_exp_f32 v203, v203 clamp\n\t"
    "v_pk_fma_f32 v[200:201], v[200:201], s[52:53], 0.5 op_sel:[0,1,0] op_sel_hi:[1,1,0]\n\t"
    "v_pk_fma_f32 v[202:203], v[202:203], s[52:53], 0.5 op_sel:[0,1,0] op_sel_hi:[1,1,0]\n\t"
    "v_pk_fma_f32 v[204:205], v[200:201], v[204:205], v[200:201]\n\t"
    "v_pk_fma_f32 v[206:207], v[202:203], v[206:207], v[202:203]\n\t"
    "v_rcp_f32 v204, v204\n\t"
    "v_rcp_f32 v205, v205\n\t"
    "v_rcp_f32 v206, v206\n\t"
    "v_rcp_f32 v207, v207\n\t"
    "v_pk_fma_f32 v[200:201], v[200:201], v[204:205], v[204:205] neg_lo:[0,0,1] neg_hi:[0,0,1]\n\t"
    "v_pk_fma_f32 v[202:203], v[202:203], v[206:207], v[206:207] neg_lo:[0,0,1] neg_hi:[0,0,1]\n\t"
    "v_cvt_pk_bf16_f32 v234, v200, v201\n\t"
    "v_cvt_pk_bf16_f32 v235, v202, v203\n\t"
    "v_add_u32 v240, s28, v240\n\t"
    "s_waitcnt lgkmcnt(0)\n\t"
    "buffer_store_dwordx4 v[224:227], v240, s[12:15], 0 offen sc1\n\t"
    "v_pk_fma_f32 v[208:209], v[208:209], s[60:61], v[128:129] op_sel:[0,0,0] op_sel_hi:[1,0,0]\n\t"
    "v_pk_fma_f32 v[210:211], v[210:211], s[60:61], v[128:129] op_sel:[0,0,0] op_sel_hi:[1,0,0]\n\t"
    "v_exp_f32 v212, v212\n\t"
    "v_exp_f32 v213, v213\n\t"
    "v_exp_f32 v214, v214\n\t"
    "v_exp_f32 v215, v215\n\t"
    "v_exp_f32 v208, v208 clamp\n\t"
    "v_exp_f32 v209, v209 clamp\n\t"
    "v_exp_f32 v210, v210 clamp\n\t"
    "v_exp_f32 v211, v211 clamp\n\t"
    "v_pk_fma_f32 v[208:209], v[208:209], s[52:53], 0.5 op_sel:[0,1,0] op_sel_hi:[1,1,0]\n\t"
    "v_pk_fma_f32 v[210:211], v[210:211], s[52:53], 0.5 op_sel:[0,1,0] op_sel_hi:[1,1,0]\n\t"
    "v_pk_fma_f32 v[212:213], v[208:209], v[212:213], v[208:209]\n\t"
    "v_pk_fma_f32 v[214:215], v[210:211], v[214:215], v[210:211]\n\t"
    "v_rcp_f32 v212, v212\n\t"
    "v_rcp_f32 v213, v213\n\t"
    "v_rcp_f32 v214, v214\n\t"
    "v_rcp_f32 v215, v215\n\t"
    "v_pk_fma_f32 v[208:209], v[208:209], v[212:213], v[212:213] neg_lo:[0,0,1] neg_hi:[0,0,1]\n\t"
    "v_pk_fma_f32 v[210:211], v[210:211], v[214:215], v[214:215] neg_lo:[0,0,1] neg_hi:[0,0,1]\n\t"
    "v_cvt_pk_bf16_f32 v236, v208, v209\n\t"
    "v_cvt_pk_bf16_f32 v237, v210, v211\n\t"
    "ds_read_b128 v[228:231], v244 offset:9216\n\t"
    "ds_write2st64_b64 v243, v[232:233], v[234:235] offset0:48 offset1:52\n\t"
    "v_accvgpr_read_b32 v200, a32\n\t"
    "v_accvgpr_read_b32 v201, a33\n\t"
    "v_accvgpr_read_b32 v202, a34\n\t"
    "v_accvgpr_read_b32 v203, a35\n\t"
    "v_accvgpr_read_b32 v204, a36\n\t"
    "v_accvgpr_read_b32 v205, a37\n\t"
    "v_accvgpr_read_b32 v206, a38\n\t"
    "v_accvgpr_read_b32 v207, a39\n\t"
    "v_accvgpr_read_b32 v208, a96\n\t"
    "v_accvgpr_read_b32 v209, a97\n\t"
    "v_accvgpr_read_b32 v210, a98\n\t"
    "v_accvgpr_read_b32 v211, a99\n\t"
    "v_accvgpr_read_b32 v212, a100\n\t"
    "v_accvgpr_read_b32 v213, a101\n\t"
    "v_accvgpr_read_b32 v214, a102\n\t"
    "v_accvgpr_read_b32 v215, a103\n\t"
    "v_pk_mul_f32 v[36:37], v[36:37], s[58:59] op_sel:[0,1] op_sel_hi:[1,1] neg_lo:[1,0] neg_hi:[1,0]\n\t"
    "v_pk_mul_f32 v[38:39], v[38:39], s[58:59] op_sel:[0,1] op_sel_hi:[1,1] neg_lo:[1,0] neg_hi:[1,0]\n\t"
    "v_pk_add_f32 v[216:217], v[32:33], v[96:97] neg_lo:[0,1] neg_hi:[0,1]\n\t"
    "v_pk_add_f32 v[218:219], v[34:35], v[98:99] neg_lo:[0,1] neg_hi:[0,1]\n\t"
    "v_pk_fma_f32 v[220:221], v[100:101], s[58:59], v[36:37] op_sel:[0,1,0] op_sel_hi:[1,1,1]\n\t"
    "v_pk_fma_f32 v[222:223], v[102:103], s[58:59], v[38:39] op_sel:[0,1,0] op_sel_hi:[1,1,1]\n\t"
    "v_pk_add_f32 v[32:33], v[32:33], v[96:97]\n\t"
    "v_pk_add_f32 v[34:35], v[34:35], v[98:99]\n\t"
    "v_pk_fma_f32 v[36:37], v[100:101], s[58:59], v[36:37] op_sel:[0,1,0] op_sel_hi:[1,1,1] neg_lo:[1,0,0] neg_hi:[1,0,0]\n\t"
    "v_pk_fma_f32 v[38:39], v[102:103], s[58:59], v[38:39] op_sel:[0,1,0] op_sel_hi:[1,1,1] neg_lo:[1,0,0] neg_hi:[1,0,0]\n\t"
    "v_pk_add_f32 v[96:97], v[200:201], v[208:209]\n\t"
    "v_pk_add_f32 v[98:99], v[202:203], v[210:211]\n\t"
    "v_pk_add_f32 v[100:101], v[204:205], v[212:213]\n\t"
    "v_pk_add_f32 v[102:103], v[206:207], v[214:215]\n\t"
    "v_pk_add_f32 v[200:201], v[200:201], v[208:209] neg_lo:[0,1] neg_hi:[0,1]\n\t"
    "v_pk_add_f32 v[202:203], v[202:203], v[210:211] neg_lo:[0,1] neg_hi:[0,1]\n\t"
    "v_pk_add_f32 v[204:205], v[204:205], v[212:213] neg_lo:[0,1] neg_hi:[0,1]\n\t"
    "v_pk_add_f32 v[206:207], v[206:207], v[214:215] neg_lo:[0,1] neg_hi:[0,1]\n\t"
    "v_pk_fma_f32 v[208:209], v[32:33], -4.0, v[96:97] op_sel:[0,0,0] op_sel_hi:[1,0,1]\n\t"
    "v_pk_fma_f32 v[210:211], v[34:35], -4.0, v[98:99] op_sel:[0,0,0] op_sel_hi:[1,0,1]\n\t"
    "v_pk_fma_f32 v[212:213], v[100:101], s[60:61], v[36:37] op_sel:[0,1,0] op_sel_hi:[1,1,1] neg_lo:[1,0,1] neg_hi:[1,0,1]\n\t"
    "v_pk_fma_f32 v[214:215], v[102:103], s[60:61], v[38:39] op_sel:[0,1,0] op_sel_hi:[1,1,1] neg_lo:[1,0,1] neg_hi:[1,0,1]\n\t"
    "v_pk_fma_f32 v[32:33], v[32:33], s[50:51], v[96:97] op_sel:[0,1,0] op_sel_hi:[1,1,1] neg_lo:[1,0,0] neg_hi:[1,0,0]\n\t"
    "v_pk_fma_f32 v[34:35], v[34:35], s[50:51], v[98:99] op_sel:[0,1,0] op_sel_hi:[1,1,1] neg_lo:[1,0,0] neg_hi:[1,0,0]\n\t"
    "v_pk_fma_f32 v[36:37], v[36:37], s[50:51], v[212:213] op_sel:[0,0,0] op_sel_hi:[1,0,1] neg_lo:[1,0,1] neg_hi:[1,0,1]\n\t"
    "v_pk_fma_f32 v[38:39], v[38:39], s[50:51], v[214:215] op_sel:[0,0,0] op_sel_hi:[1,0,1] neg_lo:[1,0,1] neg_hi:[1,0,1]\n\t"
    "v_pk_fma_f32 v[168:169], v[216:217], s[48:49], v[168:169] op_sel:[0,1,0] op_sel_hi:[1,1,1]\n\t"
    "v_pk_fma_f32 v[170:171], v[218:219], s[48:49], v[170:171] op_sel:[0,1,0] op_sel_hi:[1,1,1]\n\t"
    "v_pk_fma_f32 v[172:173], v[220:221], s[68:69], v[172:173] op_sel:[0,0,0] op_sel_hi:[1,0,1]\n\t"
    "v_pk_fma_f32 v[174:175], v[222:223], s[68:69], v[174:175] op_sel:[0,0,0] op_sel_hi:[1,0,1]\n\t"
    "v_pk_fma_f32 v[200:201], v[216:217], -2.0, v[200:201] op_sel:[0,0,0] op_sel_hi:[1,0,1]\n\t"
    "v_pk_fma_f32 v[202:203], v[218:219], -2.0, v[202:203] op_sel:[0,0,0] op_sel_hi:[1,0,1]\n\t"
    "v_pk_fma_f32 v[204:205], v[204:205], s[60:61], v[220:221] op_sel:[0,0,0] op_sel_hi:[1,0,1] neg_lo:[1,0,1] neg_hi:[1,0,1]\n\t"
    "v_pk_fma_f32 v[206:207], v[206:207], s[60:61], v[222:223] op_sel:[0,0,0] op_sel_hi:[1,0,1] neg_lo:[1,0,1] neg_hi:[1,0,1]\n\t"
    "v_pk_fma_f32 v[168:169], v[208:209], s[62:63], v[168:169] op_sel:[0,0,0] op_sel_hi:[1,0,1]\n\t"
    "v_pk_fma_f32 v[170:171], v[210:211], s[62:63], v[170:171] op_sel:[0,0,0] op_sel_hi:[1,0,1]\n\t"
    "v_pk_fma_f32 v[172:173], v[212:213], s[68:69], v[172:173] op_sel:[0,1,0] op_sel_hi:[1,1,1]\n\t"
    "v_pk_fma_f32 v[174:175], v[214:215], s[68:69], v[174:175] op_sel:[0,1,0] op_sel_hi:[1,1,1]\n\t"
    "v_pk_fma_f32 v[168:169], v[32:33], s[62:63], v[168:169] op_sel:[0,1,0] op_sel_hi:[1,1,1]\n\t"
    "v_pk_fma_f32 v[170:171], v[34:35], s[62:63], v[170:171] op_sel:[0,1,0] op_sel_hi:[1,1,1]\n\t"
    "v_pk_fma_f32 v[172:173], v[36:37], s[70:71], v[172:173] op_sel:[0,0,0] op_sel_hi:[1,0,1]\n\t"
    "v_pk_fma_f32 v[174:175], v[38:39], s[70:71], v[174:175] op_sel:[0,0,0] op_sel_hi:[1,0,1]\n\t"
    "v_pk_fma_f32 v[168:169], v[200:201], s[64:65], v[168:169] op_sel:[0,0,0] op_sel_hi:[1,0,1]\n\t"
    "v_pk_fma_f32 v[170:171], v[202:203], s[64:65], v[170:171] op_sel:[0,0,0] op_sel_hi:[1,0,1]\n\t"
    "v_pk_fma_f32 v[172:173], v[204:205], s[74:75], v[172:173] op_sel:[0,0,0] op_sel_hi:[1,0,1]\n\t"
    "v_pk_fma_f32 v[174:175], v[206:207], s[74:75], v[174:175] op_sel:[0,0,0] op_sel_hi:[1,0,1]\n\t"
    "v_pk_fma_f32 v[200:201], v[168:169], s[64:65], v[200:201] op_sel:[0,1,0] op_sel_hi:[1,1,1]\n\t"
    "v_pk_fma_f32 v[202:203], v[170:171], s[64:65], v[202:203] op_sel:[0,1,0] op_sel_hi:[1,1,1]\n\t"
    "v_pk_fma_f32 v[204:205], v[172:173], s[74:75], v[204:205] op_sel:[0,1,0] op_sel_hi:[1,1,1]\n\t"
    "v_pk_fma_f32 v[206:207], v[174:175], s[74:75], v[206:207] op_sel:[0,1,0] op_sel_hi:[1,1,1]\n\t"
    "v_pk_fma_f32 v[216:217], v[168:169], s[66:67], v[216:217] op_sel:[0,1,0] op_sel_hi:[1,1,1]\n\t"
    "v_pk_fma_f32 v[218:219], v[170:171], s[66:67], v[218:219] op_sel:[0,1,0] op_sel_hi:[1,1,1]\n\t"
    "v_pk_fma_f32 v[220:221], v[172:173], s[76:77], v[220:221] op_sel:[0,0,0] op_sel_hi:[1,0,1]\n\t"
    "v_pk_fma_f32 v[222:223], v[174:175], s[76:77], v[222:223] op_sel:[0,0,0] op_sel_hi:[1,0,1]\n\t"
    "v_pk_fma_f32 v[96:97], v[200:201], s[66:67], v[216:217] op_sel:[0,0,0] op_sel_hi:[1,0,1]\n\t"
    "v_pk_fma_f32 v[98:99], v[202:203], s[66:67], v[218:219] op_sel:[0,0,0] op_sel_hi:[1,0,1]\n\t"
    "v_pk_fma_f32 v[100:101], v[220:221], s[50:51], v[204:205] op_sel:[0,0,0] op_sel_hi:[1,0,1] neg_lo:[1,0,1] neg_hi:[1,0,1]\n\t"
    "v_pk_fma_f32 v[102:103], v[222:223], s[50:51], v[206:207] op_sel:[0,0,0] op_sel_hi:[1,0,1] neg_lo:[1,0,1] neg_hi:[1,0,1]\n\t"
    "v_pk_fma_f32 v[96:97], v[160:161], s[66:67], v[96:97] op_sel:[0,1,0] op_sel_hi:[1,1,1] neg_lo:[1,0,0] neg_hi:[1,0,0]\n\t"
    "v_pk_fma_f32 v[98:99], v[162:163], s[66:67], v[98:99] op_sel:[0,1,0] op_sel_hi:[1,1,1] neg_lo:[1,0,0] neg_hi:[1,0,0]\n\t"
    "v_pk_fma_f32 v[100:101], v[164:165], s[76:77], v[100:101] op_sel:[0,1,0] op_sel_hi:[1,1,1]\n\t"
    "v_pk_fma_f32 v[102:103], v[166:167], s[76:77], v[102:103] op_sel:[0,1,0] op_sel_hi:[1,1,1]\n\t"
    "v_pk_fma_f32 v[96:97], v[96:97], s[56:57], v[128:129] op_sel:[0,0,0] op_sel_hi:[1,0,0]\n\t"
    "v_pk_fma_f32 v[98:99], v[98:99], s[56:57], v[128:129] op_sel:[0,0,0] op_sel_hi:[1,0,0]\n\t"
    "v_exp_f32 v100, v100\n\t"
    "v_exp_f32 v101, v101\n\t"
    "v_exp_f32 v102, v102\n\t"
    "v_exp_f32 v103, v103\n\t"
    "v_exp_f32 v96, v96 clamp\n\t"
    "v_exp_f32 v97, v97 clamp\n\t"
    "v_exp_f32 v98, v98 clamp\n\t"
    "v_exp_f32 v99, v99 clamp\n\t"
    "v_pk_fma_f32 v[96:97], v[96:97], s[52:53], 0.5 op_sel:[0,1,0] op_sel_hi:[1,1,0]\n\t"
    "v_pk_fma_f32 v[98:99], v[98:99], s[52:53], 0.5 op_sel:[0,1,0] op_sel_hi:[1,1,0]\n\t"
    "v_pk_fma_f32 v[100:101], v[96:97], v[100:101], v[96:97]\n\t"
    "v_pk_fma_f32 v[102:103], v[98:99], v[102:103], v[98:99]\n\t"
    "v_rcp_f32 v100, v100\n\t"
    "v_rcp_f32 v101, v101\n\t"
    "v_rcp_f32 v102, v102\n\t"
    "v_rcp_f32 v103, v103\n\t"
    "v_pk_fma_f32 v[96:97], v[96:97], v[100:101], v[100:101] neg_lo:[0,0,1] neg_hi:[0,0,1]\n\t"
    "v_pk_fma_f32 v[98:99], v[98:99], v[102:103], v[102:103] neg_lo:[0,0,1] neg_hi:[0,0,1]\n\t"
    "v_cvt_pk_bf16_f32 v238, v96, v97\n\t"
    "v_cvt_pk_bf16_f32 v239, v98, v99\n\t"
    "v_add_u32 v241, s28, v241\n\t"
    "s_waitcnt lgkmcnt(1)\n\t"
    "buffer_store_dwordx4 v[228:231], v241, s[12:15], 0 offen sc1\n\t"
    "ds_write2st64_b64 v243, v[236:237], v[238:239] offset0:56 offset1:60\n\t"
    "v_pk_fma_f32 v[32:33], v[32:33], s[60:61], v[128:129] op_sel:[0,0,0] op_sel_hi:[1,0,0] neg_lo:[1,0,0] neg_hi:[1,0,0]\n\t"
    "v_pk_fma_f32 v[34:35], v[34:35], s[60:61], v[128:129] op_sel:[0,0,0] op_sel_hi:[1,0,0] neg_lo:[1,0,0] neg_hi:[1,0,0]\n\t"
    "v_exp_f32 v36, v36\n\t"
    "v_exp_f32 v37, v37\n\t"
    "v_exp_f32 v38, v38\n\t"
    "v_exp_f32 v39, v39\n\t"
    "v_exp_f32 v32, v32 clamp\n\t"
    "v_exp_f32 v33, v33 clamp\n\t"
    "v_exp_f32 v34, v34 clamp\n\t"
    "v_exp_f32 v35, v35 clamp\n\t"
    "v_pk_fma_f32 v[32:33], v[32:33], s[52:53], 0.5 op_sel:[0,1,0] op_sel_hi:[1,1,0]\n\t"
    "v_pk_fma_f32 v[34:35], v[34:35], s[52:53], 0.5 op_sel:[0,1,0] op_sel_hi:[1,1,0]\n\t"
    "v_pk_fma_f32 v[36:37], v[32:33], v[36:37], v[32:33]\n\t"
    "v_pk_fma_f32 v[38:39], v[34:35], v[38:39], v[34:35]\n\t"
    "v_rcp_f32 v36, v36\n\t"
    "v_rcp_f32 v37, v37\n\t"
    "v_rcp_f32 v38, v38\n\t"
    "v_rcp_f32 v39, v39\n\t"
    "v_pk_fma_f32 v[32:33], v[32:33], v[36:37], v[36:37] neg_lo:[0,0,1] neg_hi:[0,0,1]\n\t"
    "v_pk_fma_f32 v[34:35], v[34:35], v[38:39], v[38:39] neg_lo:[0,0,1] neg_hi:[0,0,1]\n\t"
    "v_cvt_pk_bf16_f32 v232, v32, v33\n\t"
    "v_cvt_pk_bf16_f32 v233, v34, v35\n\t"
    "ds_read_b128 v[224:227], v244 offset:16384\n\t"
    "v_pk_fma_f32 v[200:201], v[200:201], s[58:59], v[128:129] op_sel:[0,1,0] op_sel_hi:[1,1,0]\n\t"
    "v_pk_fma_f32 v[202:203], v[202:203], s[58:59], v[128:129] op_sel:[0,1,0] op_sel_hi:[1,1,0]\n\t"
    "v_exp_f32 v204, v204\n\t"
    "v_exp_f32 v205, v205\n\t"
    "v_exp_f32 v206, v206\n\t"
    "v_exp_f32 v207, v207\n\t"
    "v_exp_f32 v200, v200 clamp\n\t"
    "v_exp_f32 v201, v201 clamp\n\t"
    "v_exp_f32 v202, v202 clamp\n\t"
    "v_exp_f32 v203, v203 clamp\n\t"
    "v_pk_fma_f32 v[200:201], v[200:201], s[52:53], 0.5 op_sel:[0,1,0] op_sel_hi:[1,1,0]\n\t"
    "v_pk_fma_f32 v[202:203], v[202:203], s[52:53], 0.5 op_sel:[0,1,0] op_sel_hi:[1,1,0]\n\t"
    "v_pk_fma_f32 v[204:205], v[200:201], v[204:205], v[200:201]\n\t"
    "v_pk_fma_f32 v[206:207], v[202:203], v[206:207], v[202:203]\n\t"
    "v_rcp_f32 v204, v204\n\t"
    "v_rcp_f32 v205, v205\n\t"
    "v_rcp_f32 v206, v206\n\t"
    "v_rcp_f32 v207, v207\n\t"
    "v_pk_fma_f32 v[200:201], v[200:201], v[204:205], v[204:205] neg_lo:[0,0,1] neg_hi:[0,0,1]\n\t"
    "v_pk_fma_f32 v[202:203], v[202:203], v[206:207], v[206:207] neg_lo:[0,0,1] neg_hi:[0,0,1]\n\t"
    "v_cvt_pk_bf16_f32 v234, v200, v201\n\t"
    "v_cvt_pk_bf16_f32 v235, v202, v203\n\t"
    "v_add_u32 v240, s28, v240\n\t"
    "s_waitcnt lgkmcnt(0)\n\t"
    "buffer_store_dwordx4 v[224:227], v240, s[12:15], 0 offen sc1\n\t"
    "v_pk_fma_f32 v[208:209], v[208:209], s[60:61], v[128:129] op_sel:[0,0,0] op_sel_hi:[1,0,0]\n\t"
    "v_pk_fma_f32 v[210:211], v[210:211], s[60:61], v[128:129] op_sel:[0,0,0] op_sel_hi:[1,0,0]\n\t"
    "v_exp_f32 v212, v212\n\t"
    "v_exp_f32 v213, v213\n\t"
    "v_exp_f32 v214, v214\n\t"
    "v_exp_f32 v215, v215\n\t"
    "v_exp_f32 v208, v208 clamp\n\t"
    "v_exp_f32 v209, v209 clamp\n\t"
    "v_exp_f32 v210, v210 clamp\n\t"
    "v_exp_f32 v211, v211 clamp\n\t"
    "v_pk_fma_f32 v[208:209], v[208:209], s[52:53], 0.5 op_sel:[0,1,0] op_sel_hi:[1,1,0]\n\t"
    "v_pk_fma_f32 v[210:211], v[210:211], s[52:53], 0.5 op_sel:[0,1,0] op_sel_hi:[1,1,0]\n\t"
    "v_pk_fma_f32 v[212:213], v[208:209], v[212:213], v[208:209]\n\t"
    "v_pk_fma_f32 v[214:215], v[210:211], v[214:215], v[210:211]\n\t"
    "v_rcp_f32 v212, v212\n\t"
    "v_rcp_f32 v213, v213\n\t"
    "v_rcp_f32 v214, v214\n\t"
    "v_rcp_f32 v215, v215\n\t"
    "v_pk_fma_f32 v[208:209], v[208:209], v[212:213], v[212:213] neg_lo:[0,0,1] neg_hi:[0,0,1]\n\t"
    "v_pk_fma_f32 v[210:211], v[210:211], v[214:215], v[214:215] neg_lo:[0,0,1] neg_hi:[0,0,1]\n\t"
    "v_cvt_pk_bf16_f32 v236, v208, v209\n\t"
    "v_cvt_pk_bf16_f32 v237, v210, v211\n\t"
    "ds_read_b128 v[228:231], v244 offset:17408\n\t"
    "ds_write2st64_b64 v243, v[232:233], v[234:235] offset0:64 offset1:68\n\t"
    "v_accvgpr_read_b32 v200, a40\n\t"
    "v_accvgpr_read_b32 v201, a41\n\t"
    "v_accvgpr_read_b32 v202, a42\n\t"
    "v_accvgpr_read_b32 v203, a43\n\t"
    "v_accvgpr_read_b32 v204, a44\n\t"
    "v_accvgpr_read_b32 v205, a45\n\t"
    "v_accvgpr_read_b32 v206, a46\n\t"
    "v_accvgpr_read_b32 v207, a47\n\t"
    "v_accvgpr_read_b32 v208, a104\n\t"
    "v_accvgpr_read_b32 v209, a105\n\t"
    "v_accvgpr_read_b32 v210, a106\n\t"
    "v_accvgpr_read_b32 v211, a107\n\t"
    "v_accvgpr_read_b32 v212, a108\n\t"
    "v_accvgpr_read_b32 v213, a109\n\t"
    "v_accvgpr_read_b32 v214, a110\n\t"
    "v_accvgpr_read_b32 v215, a111\n\t"
    "v_pk_mul_f32 v[44:45], v[44:45], s[58:59] op_sel:[0,1] op_sel_hi:[1,1] neg_lo:[1,0] neg_hi:[1,0]\n\t"
    "v_pk_mul_f32 v[46:47], v[46:47], s[58:59] op_sel:[0,1] op_sel_hi:[1,1] neg_lo:[1,0] neg_hi:[1,0]\n\t"
    "v_pk_add_f32 v[216:217], v[40:41], v[104:105] neg_lo:[0,1] neg_hi:[0,1]\n\t"
    "v_pk_add_f32 v[218:219], v[42:43], v[106:107] neg_lo:[0,1] neg_hi:[0,1]\n\t"
    "v_pk_fma_f32 v[220:221], v[108:109], s[58:59], v[44:45] op_sel:[0,1,0] op_sel_hi:[1,1,1]\n\t"
    "v_pk_fma_f32 v[222:223], v[110:111], s[58:59], v[46:47] op_sel:[0,1,0] op_sel_hi:[1,1,1]\n\t"
    "v_pk_add_f32 v[40:41], v[40:41], v[104:105]\n\t"
    "v_pk_add_f32 v[42:43], v[42:43], v[106:107]\n\t"
    "v_pk_fma_f32 v[44:45], v[108:109], s[58:59], v[44:45] op_sel:[0,1,0] op_sel_hi:[1,1,1] neg_lo:[1,0,0] neg_hi:[1,0,0]\n\t"
    "v_pk_fma_f32 v[46:47], v[110:111], s[58:59], v[46:47] op_sel:[0,1,0] op_sel_hi:[1,1,1] neg_lo:[1,0,0] neg_hi:[1,0,0]\n\t"
    "v_pk_add_f32 v[104:105], v[200:201], v[208:209]\n\t"
    "v_pk_add_f32 v[106:107], v[202:203], v[210:211]\n\t"
    "v_pk_add_f32 v[108:109], v[204:205], v[212:213]\n\t"
    "v_pk_add_f32 v[110:111], v[206:207], v[214:215]\n\t"
    "v_pk_add_f32 v[200:201], v[200:201], v[208:209] neg_lo:[0,1] neg_hi:[0,1]\n\t"
    "v_pk_add_f32 v[202:203], v[202:203], v[210:211] neg_lo:[0,1] neg_hi:[0,1]\n\t"
    "v_pk_add_f32 v[204:205], v[204:205], v[212:213] neg_lo:[0,1] neg_hi:[0,1]\n\t"
    "v_pk_add_f32 v[206:207], v[206:207], v[214:215] neg_lo:[0,1] neg_hi:[0,1]\n\t"
    "v_pk_fma_f32 v[208:209], v[40:41], -4.0, v[104:105] op_sel:[0,0,0] op_sel_hi:[1,0,1]\n\t"
    "v_pk_fma_f32 v[210:211], v[42:43], -4.0, v[106:107] op_sel:[0,0,0] op_sel_hi:[1,0,1]\n\t"
    "v_pk_fma_f32 v[212:213], v[108:109], s[60:61], v[44:45] op_sel:[0,1,0] op_sel_hi:[1,1,1] neg_lo:[1,0,1] neg_hi:[1,0,1]\n\t"
    "v_pk_fma_f32 v[214:215], v[110:111], s[60:61], v[46:47] op_sel:[0,1,0] op_sel_hi:[1,1,1] neg_lo:[1,0,1] neg_hi:[1,0,1]\n\t"
    "v_pk_fma_f32 v[40:41], v[40:41], s[50:51], v[104:105] op_sel:[0,1,0] op_sel_hi:[1,1,1] neg_lo:[1,0,0] neg_hi:[1,0,0]\n\t"
    "v_pk_fma_f32 v[42:43], v[42:43], s[50:51], v[106:107] op_sel:[0,1,0] op_sel_hi:[1,1,1] neg_lo:[1,0,0] neg_hi:[1,0,0]\n\t"
    "v_pk_fma_f32 v[44:45], v[44:45], s[50:51], v[212:213] op_sel:[0,0,0] op_sel_hi:[1,0,1] neg_lo:[1,0,1] neg_hi:[1,0,1]\n\t"
    "v_pk_fma_f32 v[46:47], v[46:47], s[50:51], v[214:215] op_sel:[0,0,0] op_sel_hi:[1,0,1] neg_lo:[1,0,1] neg_hi:[1,0,1]\n\t"
    "v_pk_fma_f32 v[176:177], v[216:217], s[48:49], v[176:177] op_sel:[0,1,0] op_sel_hi:[1,1,1]\n\t"
    "v_pk_fma_f32 v[178:179], v[218:219], s[48:49], v[178:179] op_sel:[0,1,0] op_sel_hi:[1,1,1]\n\t"
    "v_pk_fma_f32 v[180:181], v[220:221], s[68:69], v[180:181] op_sel:[0,0,0] op_sel_hi:[1,0,1]\n\t"
    "v_pk_fma_f32 v[182:183], v[222:223], s[68:69], v[182:183] op_sel:[0,0,0] op_sel_hi:[1,0,1]\n\t"
    "v_pk_fma_f32 v[200:201], v[216:217], -2.0, v[200:201] op_sel:[0,0,0] op_sel_hi:[1,0,1]\n\t"
    "v_pk_fma_f32 v[202:203], v[218:219], -2.0, v[202:203] op_sel:[0,0,0] op_sel_hi:[1,0,1]\n\t"
    "v_pk_fma_f32 v[204:205], v[204:205], s[60:61], v[220:221] op_sel:[0,0,0] op_sel_hi:[1,0,1] neg_lo:[1,0,1] neg_hi:[1,0,1]\n\t"
    "v_pk_fma_f32 v[206:207], v[206:207], s[60:61], v[222:223] op_sel:[0,0,0] op_sel_hi:[1,0,1] neg_lo:[1,0,1] neg_hi:[1,0,1]\n\t"
    "v_pk_fma_f32 v[176:177], v[208:209], s[62:63], v[176:177] op_sel:[0,0,0] op_sel_hi:[1,0,1]\n\t"
    "v_pk_fma_f32 v[178:179], v[210:211], s[62:63], v[178:179] op_sel:[0,0,0] op_sel_hi:[1,0,1]\n\t"
    "v_pk_fma_f32 v[180:181], v[212:213], s[68:69], v[180:181] op_sel:[0,1,0] op_sel_hi:[1,1,1]\n\t"
    "v_pk_fma_f32 v[182:183], v[214:215], s[68:69], v[182:183] op_sel:[0,1,0] op_sel_hi:[1,1,1]\n\t"
    "v_pk_fma_f32 v[176:177], v[40:41], s[62:63], v[176:177] op_sel:[0,1,0] op_sel_hi:[1,1,1]\n\t"
    "v_pk_fma_f32 v[178:179], v[42:43], s[62:63], v[178:179] op_sel:[0,1,0] op_sel_hi:[1,1,1]\n\t"
    "v_pk_fma_f32 v[180:181], v[44:45], s[70:71], v[180:181] op_sel:[0,0,0] op_sel_hi:[1,0,1]\n\t"
    "v_pk_fma_f32 v[182:183], v[46:47], s[70:71], v[182:183] op_sel:[0,0,0] op_sel_hi:[1,0,1]\n\t"
    "v_pk_fma_f32 v[176:177], v[200:201], s[64:65], v[176:177] op_sel:[0,0,0] op_sel_hi:[1,0,1]\n\t"
    "v_pk_fma_f32 v[178:179], v[202:203], s[64:65], v[178:179] op_sel:[0,0,0] op_sel_hi:[1,0,1]\n\t"
    "v_pk_fma_f32 v[180:181], v[204:205], s[74:75], v[180:181] op_sel:[0,0,0] op_sel_hi:[1,0,1]\n\t"
    "v_pk_fma_f32 v[182:183], v[206:207], s[74:75], v[182:183] op_sel:[0,0,0] op_sel_hi:[1,0,1]\n\t"
    "v_pk_fma_f32 v[200:201], v[176:177], s[64:65], v[200:201] op_sel:[0,1,0] op_sel_hi:[1,1,1]\n\t"
    "v_pk_fma_f32 v[202:203], v[178:179], s[64:65], v[202:203] op_sel:[0,1,0] op_sel_hi:[1,1,1]\n\t"
    "v_pk_fma_f32 v[204:205], v[180:181], s[74:75], v[204:205] op_sel:[0,1,0] op_sel_hi:[1,1,1]\n\t"
    "v_pk_fma_f32 v[206:207], v[182:183], s[74:75], v[206:207] op_sel:[0,1,0] op_sel_hi:[1,1,1]\n\t"
    "v_pk_fma_f32 v[216:217], v[176:177], s[66:67], v[216:217] op_sel:[0,1,0] op_sel_hi:[1,1,1]\n\t"
    "v_pk_fma_f32 v[218:219], v[178:179], s[66:67], v[218:219] op_sel:[0,1,0] op_sel_hi:[1,1,1]\n\t"
    "v_pk_fma_f32 v[220:221], v[180:181], s[76:77], v[220:221] op_sel:[0,0,0] op_sel_hi:[1,0,1]\n\t"
    "v_pk_fma_f32 v[222:223], v[182:183], s[76:77], v[222:223] op_sel:[0,0,0] op_sel_hi:[1,0,1]\n\t"
    "v_pk_fma_f32 v[104:105], v[200:201], s[66:67], v[216:217] op_sel:[0,0,0] op_sel_hi:[1,0,1]\n\t"
    "v_pk_fma_f32 v[106:107], v[202:203], s[66:67], v[218:219] op_sel:[0,0,0] op_sel_hi:[1,0,1]\n\t"
    "v_pk_fma_f32 v[108:109], v[220:221], s[50:51], v[204:205] op_sel:[0,0,0] op_sel_hi:[1,0,1] neg_lo:[1,0,1] neg_hi:[1,0,1]\n\t"
    "v_pk_fma_f32 v[110:111], v[222:223], s[50:51], v[206:207] op_sel:[0,0,0] op_sel_hi:[1,0,1] neg_lo:[1,0,1] neg_hi:[1,0,1]\n\t"
    "v_pk_fma_f32 v[104:105], v[168:169], s[66:67], v[104:105] op_sel:[0,1,0] op_sel_hi:[1,1,1] neg_lo:[1,0,0] neg_hi:[1,0,0]\n\t"
    "v_pk_fma_f32 v[106:107], v[170:171], s[66:67], v[106:107] op_sel:[0,1,0] op_sel_hi:[1,1,1] neg_lo:[1,0,0] neg_hi:[1,0,0]\n\t"
    "v_pk_fma_f32 v[108:109], v[172:173], s[76:77], v[108:109] op_sel:[0,1,0] op_sel_hi:[1,1,1]\n\t"
    "v_pk_fma_f32 v[110:111], v[174:175], s[76:77], v[110:111] op_sel:[0,1,0] op_sel_hi:[1,1,1]\n\t"
    "v_pk_fma_f32 v[104:105], v[104:105], s[56:57], v[128:129] op_sel:[0,0,0] op_sel_hi:[1,0,0]\n\t"
    "v_pk_fma_f32 v[106:107], v[106:107], s[56:57], v[128:129] op_sel:[0,0,0] op_sel_hi:[1,0,0]\n\t"
    "v_exp_f32 v108, v108\n\t"
    "v_exp_f32 v109, v109\n\t"
    "v_exp_f32 v110, v110\n\t"
    "v_exp_f32 v111, v111\n\t"
    "v_exp_f32 v104, v104 clamp\n\t"
    "v_exp_f32 v105, v105 clamp\n\t"
    "v_exp_f32 v106, v106 clamp\n\t"
    "v_exp_f32 v107, v107 clamp\n\t"
    "v_pk_fma_f32 v[104:105], v[104:105], s[52:53], 0.5 op_sel:[0,1,0] op_sel_hi:[1,1,0]\n\t"
    "v_pk_fma_f32 v[106:107], v[106:107], s[52:53], 0.5 op_sel:[0,1,0] op_sel_hi:[1,1,0]\n\t"
    "v_pk_fma_f32 v[108:109], v[104:105], v[108:109], v[104:105]\n\t"
    "v_pk_fma_f32 v[110:111], v[106:107], v[110:111], v[106:107]\n\t"
    "v_rcp_f32 v108, v108\n\t"
    "v_rcp_f32 v109, v109\n\t"
    "v_rcp_f32 v110, v110\n\t"
    "v_rcp_f32 v111, v111\n\t"
    "v_pk_fma_f32 v[104:105], v[104:105], v[108:109], v[108:109] neg_lo:[0,0,1] neg_hi:[0,0,1]\n\t"
    "v_pk_fma_f32 v[106:107], v[106:107], v[110:111], v[110:111] neg_lo:[0,0,1] neg_hi:[0,0,1]\n\t"
    "v_cvt_pk_bf16_f32 v238, v104, v105\n\t"
    "v_cvt_pk_bf16_f32 v239, v106, v107\n\t"
    "v_add_u32 v241, s28, v241\n\t"
    "s_waitcnt lgkmcnt(1)\n\t"
    "buffer_store_dwordx4 v[228:231], v241, s[12:15], 0 offen sc1\n\t"
    "ds_write2st64_b64 v243, v[236:237], v[238:239] offset0:72 offset1:76\n\t"
    "v_pk_fma_f32 v[40:41], v[40:41], s[60:61], v[128:129] op_sel:[0,0,0] op_sel_hi:[1,0,0] neg_lo:[1,0,0] neg_hi:[1,0,0]\n\t"
    "v_pk_fma_f32 v[42:43], v[42:43], s[60:61], v[128:129] op_sel:[0,0,0] op_sel_hi:[1,0,0] neg_lo:[1,0,0] neg_hi:[1,0,0]\n\t"
    "v_exp_f32 v44, v44\n\t"
    "v_exp_f32 v45, v45\n\t"
    "v_exp_f32 v46, v46\n\t"
    "v_exp_f32 v47, v47\n\t"
    "v_exp_f32 v40, v40 clamp\n\t"
    "v_exp_f32 v41, v41 clamp\n\t"
    "v_exp_f32 v42, v42 clamp\n\t"
    "v_exp_f32 v43, v43 clamp\n\t"
    "v_pk_fma_f32 v[40:41], v[40:41], s[52:53], 0.5 op_sel:[0,1,0] op_sel_hi:[1,1,0]\n\t"
    "v_pk_fma_f32 v[42:43], v[42:43], s[52:53], 0.5 op_sel:[0,1,0] op_sel_hi:[1,1,0]\n\t"
    "v_pk_fma_f32 v[44:45], v[40:41], v[44:45], v[40:41]\n\t"
    "v_pk_fma_f32 v[46:47], v[42:43], v[46:47], v[42:43]\n\t"
    "v_rcp_f32 v44, v44\n\t"
    "v_rcp_f32 v45, v45\n\t"
    "v_rcp_f32 v46, v46\n\t"
    "v_rcp_f32 v47, v47\n\t"
    "v_pk_fma_f32 v[40:41], v[40:41], v[44:45], v[44:45] neg_lo:[0,0,1] neg_hi:[0,0,1]\n\t"
    "v_pk_fma_f32 v[42:43], v[42:43], v[46:47], v[46:47] neg_lo:[0,0,1] neg_hi:[0,0,1]\n\t"
    "v_cvt_pk_bf16_f32 v232, v40, v41\n\t"
    "v_cvt_pk_bf16_f32 v233, v42, v43\n\t"
    "v_mfma_f32_32x32x16_bf16 a[128:143], a[208:211], a[208:211], 0\n\t"
    "s_waitcnt lgkmcnt(0)\n\t"
    "s_barrier\n\t"
    "ds_read_b128 v[224:227], v244 offset:24576\n\t"
    "v_pk_fma_f32 v[200:201], v[200:201], s[58:59], v[128:129] op_sel:[0,1,0] op_sel_hi:[1,1,0]\n\t"
    "v_pk_fma_f32 v[202:203], v[202:203], s[58:59], v[128:129] op_sel:[0,1,0] op_sel_hi:[1,1,0]\n\t"
    "v_exp_f32 v204, v204\n\t"
    "v_exp_f32 v205, v205\n\t"
    "v_exp_f32 v206, v206\n\t"
    "v_exp_f32 v207, v207\n\t"
    "v_exp_f32 v200, v200 clamp\n\t"
    "v_exp_f32 v201, v201 clamp\n\t"
    "v_exp_f32 v202, v202 clamp\n\t"
    "v_exp_f32 v203, v203 clamp\n\t"
    "v_pk_fma_f32 v[200:201], v[200:201], s[52:53], 0.5 op_sel:[0,1,0] op_sel_hi:[1,1,0]\n\t"
    "v_pk_fma_f32 v[202:203], v[202:203], s[52:53], 0.5 op_sel:[0,1,0] op_sel_hi:[1,1,0]\n\t"
    "v_pk_fma_f32 v[204:205], v[200:201], v[204:205], v[200:201]\n\t"
    "v_pk_fma_f32 v[206:207], v[202:203], v[206:207], v[202:203]\n\t"
    "v_rcp_f32 v204, v204\n\t"
    "v_rcp_f32 v205, v205\n\t"
    "v_rcp_f32 v206, v206\n\t"
    "v_rcp_f32 v207, v207\n\t"
    "v_pk_fma_f32 v[200:201], v[200:201], v[204:205], v[204:205] neg_lo:[0,0,1] neg_hi:[0,0,1]\n\t"
    "v_pk_fma_f32 v[202:203], v[202:203], v[206:207], v[206:207] neg_lo:[0,0,1] neg_hi:[0,0,1]\n\t"
    "v_cvt_pk_bf16_f32 v234, v200, v201\n\t"
    "v_cvt_pk_bf16_f32 v235, v202, v203\n\t"
    "v_add_u32 v240, s28, v240\n\t"
    "s_waitcnt lgkmcnt(0)\n\t"
    "buffer_store_dwordx4 v[224:227], v240, s[12:15], 0 offen sc1\n\t"
    "v_pk_fma_f32 v[208:209], v[208:209], s[60:61], v[128:129] op_sel:[0,0,0] op_sel_hi:[1,0,0]\n\t"
    "v_pk_fma_f32 v[210:211], v[210:211], s[60:61], v[128:129] op_sel:[0,0,0] op_sel_hi:[1,0,0]\n\t"
    "v_exp_f32 v212, v212\n\t"
    "v_exp_f32 v213, v213\n\t"
    "v_exp_f32 v214, v214\n\t"
    "v_exp_f32 v215, v215\n\t"
    "v_exp_f32 v208, v208 clamp\n\t"
    "v_exp_f32 v209, v209 clamp\n\t"
    "v_exp_f32 v210, v210 clamp\n\t"
    "v_exp_f32 v211, v211 clamp\n\t"
    "v_pk_fma_f32 v[208:209], v[208:209], s[52:53], 0.5 op_sel:[0,1,0] op_sel_hi:[1,1,0]\n\t"
    "v_pk_fma_f32 v[210:211], v[210:211], s[52:53], 0.5 op_sel:[0,1,0] op_sel_hi:[1,1,0]\n\t"
    "v_pk_fma_f32 v[212:213], v[208:209], v[212:213], v[208:209]\n\t"
    "v_pk_fma_f32 v[214:215], v[210:211], v[214:215], v[210:211]\n\t"
    "v_rcp_f32 v212, v212\n\t"
    "v_rcp_f32 v213, v213\n\t"
    "v_rcp_f32 v214, v214\n\t"
    "v_rcp_f32 v215, v215\n\t"
    "v_pk_fma_f32 v[208:209], v[208:209], v[212:213], v[212:213] neg_lo:[0,0,1] neg_hi:[0,0,1]\n\t"
    "v_pk_fma_f32 v[210:211], v[210:211], v[214:215], v[214:215] neg_lo:[0,0,1] neg_hi:[0,0,1]\n\t"
    "v_cvt_pk_bf16_f32 v236, v208, v209\n\t"
    "v_cvt_pk_bf16_f32 v237, v210, v211\n\t"
    "ds_read_b128 v[228:231], v244 offset:25600\n\t"
    "ds_write2st64_b64 v243, v[232:233], v[234:235] offset0:80 offset1:84\n\t"
    "v_accvgpr_read_b32 v200, a48\n\t"
    "v_accvgpr_read_b32 v201, a49\n\t"
    "v_accvgpr_read_b32 v202, a50\n\t"
    "v_accvgpr_read_b32 v203, a51\n\t"
    "v_accvgpr_read_b32 v204, a52\n\t"
    "v_accvgpr_read_b32 v205, a53\n\t"
    "v_accvgpr_read_b32 v206, a54\n\t"
    "v_accvgpr_read_b32 v207, a55\n\t"
    "v_accvgpr_read_b32 v208, a112\n\t"
    "v_accvgpr_read_b32 v209, a113\n\t"
    "v_accvgpr_read_b32 v210, a114\n\t"
    "v_accvgpr_read_b32 v211, a115\n\t"
    "v_accvgpr_read_b32 v212, a116\n\t"
    "v_accvgpr_read_b32 v213, a117\n\t"
    "v_accvgpr_read_b32 v214, a118\n\t"
    "v_accvgpr_read_b32 v215, a119\n\t"
    "v_pk_mul_f32 v[52:53], v[52:53], s[58:59] op_sel:[0,1] op_sel_hi:[1,1] neg_lo:[1,0] neg_hi:[1,0]\n\t"
    "v_pk_mul_f32 v[54:55], v[54:55], s[58:59] op_sel:[0,1] op_sel_hi:[1,1] neg_lo:[1,0] neg_hi:[1,0]\n\t"
    "v_pk_add_f32 v[216:217], v[48:49], v[112:113] neg_lo:[0,1] neg_hi:[0,1]\n\t"
    "v_pk_add_f32 v[218:219], v[50:51], v[114:115] neg_lo:[0,1] neg_hi:[0,1]\n\t"
    "v_pk_fma_f32 v[220:221], v[116:117], s[58:59], v[52:53] op_sel:[0,1,0] op_sel_hi:[1,1,1]\n\t"
    "v_pk_fma_f32 v[222:223], v[118:119], s[58:59], v[54:55] op_sel:[0,1,0] op_sel_hi:[1,1,1]\n\t"
    "v_pk_add_f32 v[48:49], v[48:49], v[112:113]\n\t"
    "v_pk_add_f32 v[50:51], v[50:51], v[114:115]\n\t"
    "v_pk_fma_f32 v[52:53], v[116:117], s[58:59], v[52:53] op_sel:[0,1,0] op_sel_hi:[1,1,1] neg_lo:[1,0,0] neg_hi:[1,0,0]\n\t"
    "v_pk_fma_f32 v[54:55], v[118:119], s[58:59], v[54:55] op_sel:[0,1,0] op_sel_hi:[1,1,1] neg_lo:[1,0,0] neg_hi:[1,0,0]\n\t"
    "v_pk_add_f32 v[112:113], v[200:201], v[208:209]\n\t"
    "v_pk_add_f32 v[114:115], v[202:203], v[210:211]\n\t"
    "v_pk_add_f32 v[116:117], v[204:205], v[212:213]\n\t"
    "v_pk_add_f32 v[118:119], v[206:207], v[214:215]\n\t"
    "v_pk_add_f32 v[200:201], v[200:201], v[208:209] neg_lo:[0,1] neg_hi:[0,1]\n\t"
    "v_pk_add_f32 v[202:203], v[202:203], v[210:211] neg_lo:[0,1] neg_hi:[0,1]\n\t"
    "v_pk_add_f32 v[204:205], v[204:205], v[212:213] neg_lo:[0,1] neg_hi:[0,1]\n\t"
    "v_pk_add_f32 v[206:207], v[206:207], v[214:215] neg_lo:[0,1] neg_hi:[0,1]\n\t"
    "v_pk_fma_f32 v[208:209], v[48:49], -4.0, v[112:113] op_sel:[0,0,0] op_sel_hi:[1,0,1]\n\t"
    "v_pk_fma_f32 v[210:211], v[50:51], -4.0, v[114:115] op_sel:[0,0,0] op_sel_hi:[1,0,1]\n\t"
    "v_pk_fma_f32 v[212:213], v[116:117], s[60:61], v[52:53] op_sel:[0,1,0] op_sel_hi:[1,1,1] neg_lo:[1,0,1] neg_hi:[1,0,1]\n\t"
    "v_pk_fma_f32 v[214:215], v[118:119], s[60:61], v[54:55] op_sel:[0,1,0] op_sel_hi:[1,1,1] neg_lo:[1,0,1] neg_hi:[1,0,1]\n\t"
    "v_pk_fma_f32 v[48:49], v[48:49], s[50:51], v[112:113] op_sel:[0,1,0] op_sel_hi:[1,1,1] neg_lo:[1,0,0] neg_hi:[1,0,0]\n\t"
    "v_pk_fma_f32 v[50:51], v[50:51], s[50:51], v[114:115] op_sel:[0,1,0] op_sel_hi:[1,1,1] neg_lo:[1,0,0] neg_hi:[1,0,0]\n\t"
    "v_pk_fma_f32 v[52:53], v[52:53], s[50:51], v[212:213] op_sel:[0,0,0] op_sel_hi:[1,0,1] neg_lo:[1,0,1] neg_hi:[1,0,1]\n\t"
    "v_pk_fma_f32 v[54:55], v[54:55], s[50:51], v[214:215] op_sel:[0,0,0] op_sel_hi:[1,0,1] neg_lo:[1,0,1] neg_hi:[1,0,1]\n\t"
    "v_pk_fma_f32 v[184:185], v[216:217], s[48:49], v[184:185] op_sel:[0,1,0] op_sel_hi:[1,1,1]\n\t"
    "v_pk_fma_f32 v[186:187], v[218:219], s[48:49], v[186:187] op_sel:[0,1,0] op_sel_hi:[1,1,1]\n\t"
    "v_pk_fma_f32 v[188:189], v[220:221], s[68:69], v[188:189] op_sel:[0,0,0] op_sel_hi:[1,0,1]\n\t"
    "v_pk_fma_f32 v[190:191], v[222:223], s[68:69], v[190:191] op_sel:[0,0,0] op_sel_hi:[1,0,1]\n\t"
    "v_pk_fma_f32 v[200:201], v[216:217], -2.0, v[200:201] op_sel:[0,0,0] op_sel_hi:[1,0,1]\n\t"
    "v_pk_fma_f32 v[202:203], v[218:219], -2.0, v[202:203] op_sel:[0,0,0] op_sel_hi:[1,0,1]\n\t"
    "v_pk_fma_f32 v[204:205], v[204:205], s[60:61], v[220:221] op_sel:[0,0,0] op_sel_hi:[1,0,1] neg_lo:[1,0,1] neg_hi:[1,0,1]\n\t"
    "v_pk_fma_f32 v[206:207], v[206:207], s[60:61], v[222:223] op_sel:[0,0,0] op_sel_hi:[1,0,1] neg_lo:[1,0,1] neg_hi:[1,0,1]\n\t"
    "v_pk_fma_f32 v[184:185], v[208:209], s[62:63], v[184:185] op_sel:[0,0,0] op_sel_hi:[1,0,1]\n\t"
    "v_pk_fma_f32 v[186:187], v[210:211], s[62:63], v[186:187] op_sel:[0,0,0] op_sel_hi:[1,0,1]\n\t"
    "v_pk_fma_f32 v[188:189], v[212:213], s[68:69], v[188:189] op_sel:[0,1,0] op_sel_hi:[1,1,1]\n\t"
    "v_pk_fma_f32 v[190:191], v[214:215], s[68:69], v[190:191] op_sel:[0,1,0] op_sel_hi:[1,1,1]\n\t"
    "v_pk_fma_f32 v[184:185], v[48:49], s[62:63], v[184:185] op_sel:[0,1,0] op_sel_hi:[1,1,1]\n\t"
    "v_pk_fma_f32 v[186:187], v[50:51], s[62:63], v[186:187] op_sel:[0,1,0] op_sel_hi:[1,1,1]\n\t"
    "v_pk_fma_f32 v[188:189], v[52:53], s[70:71], v[188:189] op_sel:[0,0,0] op_sel_hi:[1,0,1]\n\t"
    "v_pk_fma_f32 v[190:191], v[54:55], s[70:71], v[190:191] op_sel:[0,0,0] op_sel_hi:[1,0,1]\n\t"
    "v_pk_fma_f32 v[184:185], v[200:201], s[64:65], v[184:185] op_sel:[0,0,0] op_sel_hi:[1,0,1]\n\t"
    "v_pk_fma_f32 v[186:187], v[202:203], s[64:65], v[186:187] op_sel:[0,0,0] op_sel_hi:[1,0,1]\n\t"
    "v_pk_fma_f32 v[188:189], v[204:205], s[74:75], v[188:189] op_sel:[0,0,0] op_sel_hi:[1,0,1]\n\t"
    "v_pk_fma_f32 v[190:191], v[206:207], s[74:75], v[190:191] op_sel:[0,0,0] op_sel_hi:[1,0,1]\n\t"
    "v_pk_fma_f32 v[200:201], v[184:185], s[64:65], v[200:201] op_sel:[0,1,0] op_sel_hi:[1,1,1]\n\t"
    "v_pk_fma_f32 v[202:203], v[186:187], s[64:65], v[202:203] op_sel:[0,1,0] op_sel_hi:[1,1,1]\n\t"
    "v_pk_fma_f32 v[204:205], v[188:189], s[74:75], v[204:205] op_sel:[0,1,0] op_sel_hi:[1,1,1]\n\t"
    "v_pk_fma_f32 v[206:207], v[190:191], s[74:75], v[206:207] op_sel:[0,1,0] op_sel_hi:[1,1,1]\n\t"
    "v_pk_fma_f32 v[216:217], v[184:185], s[66:67], v[216:217] op_sel:[0,1,0] op_sel_hi:[1,1,1]\n\t"
    "v_pk_fma_f32 v[218:219], v[186:187], s[66:67], v[218:219] op_sel:[0,1,0] op_sel_hi:[1,1,1]\n\t"
    "v_pk_fma_f32 v[220:221], v[188:189], s[76:77], v[220:221] op_sel:[0,0,0] op_sel_hi:[1,0,1]\n\t"
    "v_pk_fma_f32 v[222:223], v[190:191], s[76:77], v[222:223] op_sel:[0,0,0] op_sel_hi:[1,0,1]\n\t"
    "v_pk_fma_f32 v[112:113], v[200:201], s[66:67], v[216:217] op_sel:[0,0,0] op_sel_hi:[1,0,1]\n\t"
    "v_pk_fma_f32 v[114:115], v[202:203], s[66:67], v[218:219] op_sel:[0,0,0] op_sel_hi:[1,0,1]\n\t"
    "v_pk_fma_f32 v[116:117], v[220:221], s[50:51], v[204:205] op_sel:[0,0,0] op_sel_hi:[1,0,1] neg_lo:[1,0,1] neg_hi:[1,0,1]\n\t"
    "v_pk_fma_f32 v[118:119], v[222:223], s[50:51], v[206:207] op_sel:[0,0,0] op_sel_hi:[1,0,1] neg_lo:[1,0,1] neg_hi:[1,0,1]\n\t"
    "v_pk_fma_f32 v[112:113], v[176:177], s[66:67], v[112:113] op_sel:[0,1,0] op_sel_hi:[1,1,1] neg_lo:[1,0,0] neg_hi:[1,0,0]\n\t"
    "v_pk_fma_f32 v[114:115], v[178:179], s[66:67], v[114:115] op_sel:[0,1,0] op_sel_hi:[1,1,1] neg_lo:[1,0,0] neg_hi:[1,0,0]\n\t"
    "v_pk_fma_f32 v[116:117], v[180:181], s[76:77], v[116:117] op_sel:[0,1,0] op_sel_hi:[1,1,1]\n\t"
    "v_pk_fma_f32 v[118:119], v[182:183], s[76:77], v[118:119] op_sel:[0,1,0] op_sel_hi:[1,1,1]\n\t"
    "v_pk_fma_f32 v[112:113], v[112:113], s[56:57], v[128:129] op_sel:[0,0,0] op_sel_hi:[1,0,0]\n\t"
    "v_pk_fma_f32 v[114:115], v[114:115], s[56:57], v[128:129] op_sel:[0,0,0] op_sel_hi:[1,0,0]\n\t"
    "v_exp_f32 v116, v116\n\t"
    "v_exp_f32 v117, v117\n\t"
    "v_exp_f32 v118, v118\n\t"
    "v_exp_f32 v119, v119\n\t"
    "v_exp_f32 v112, v112 clamp\n\t"
    "v_exp_f32 v113, v113 clamp\n\t"
    "v_exp_f32 v114, v114 clamp\n\t"
    "v_exp_f32 v115, v115 clamp\n\t"
    "v_pk_fma_f32 v[112:113], v[112:113], s[52:53], 0.5 op_sel:[0,1,0] op_sel_hi:[1,1,0]\n\t"
    "v_pk_fma_f32 v[114:115], v[114:115], s[52:53], 0.5 op_sel:[0,1,0] op_sel_hi:[1,1,0]\n\t"
    "v_pk_fma_f32 v[116:117], v[112:113], v[116:117], v[112:113]\n\t"
    "v_pk_fma_f32 v[118:119], v[114:115], v[118:119], v[114:115]\n\t"
    "v_rcp_f32 v116, v116\n\t"
    "v_rcp_f32 v117, v117\n\t"
    "v_rcp_f32 v118, v118\n\t"
    "v_rcp_f32 v119, v119\n\t"
    "v_pk_fma_f32 v[112:113], v[112:113], v[116:117], v[116:117] neg_lo:[0,0,1] neg_hi:[0,0,1]\n\t"
    "v_pk_fma_f32 v[114:115], v[114:115], v[118:119], v[118:119] neg_lo:[0,0,1] neg_hi:[0,0,1]\n\t"
    "v_cvt_pk_bf16_f32 v238, v112, v113\n\t"
    "v_cvt_pk_bf16_f32 v239, v114, v115\n\t"
    "v_add_u32 v241, s28, v241\n\t"
    "s_waitcnt lgkmcnt(1)\n\t"
    "buffer_store_dwordx4 v[228:231], v241, s[12:15], 0 offen sc1\n\t"
    "ds_write2st64_b64 v243, v[236:237], v[238:239] offset0:88 offset1:92\n\t"
    "v_pk_fma_f32 v[48:49], v[48:49], s[60:61], v[128:129] op_sel:[0,0,0] op_sel_hi:[1,0,0] neg_lo:[1,0,0] neg_hi:[1,0,0]\n\t"
    "v_pk_fma_f32 v[50:51], v[50:51], s[60:61], v[128:129] op_sel:[0,0,0] op_sel_hi:[1,0,0] neg_lo:[1,0,0] neg_hi:[1,0,0]\n\t"
    "v_exp_f32 v52, v52\n\t"
    "v_exp_f32 v53, v53\n\t"
    "v_exp_f32 v54, v54\n\t"
    "v_exp_f32 v55, v55\n\t"
    "v_exp_f32 v48, v48 clamp\n\t"
    "v_exp_f32 v49, v49 clamp\n\t"
    "v_exp_f32 v50, v50 clamp\n\t"
    "v_exp_f32 v51, v51 clamp\n\t"
    "v_pk_fma_f32 v[48:49], v[48:49], s[52:53], 0.5 op_sel:[0,1,0] op_sel_hi:[1,1,0]\n\t"
    "v_pk_fma_f32 v[50:51], v[50:51], s[52:53], 0.5 op_sel:[0,1,0] op_sel_hi:[1,1,0]\n\t"
    "v_pk_fma_f32 v[52:53], v[48:49], v[52:53], v[48:49]\n\t"
    "v_pk_fma_f32 v[54:55], v[50:51], v[54:55], v[50:51]\n\t"
    "v_rcp_f32 v52, v52\n\t"
    "v_rcp_f32 v53, v53\n\t"
    "v_rcp_f32 v54, v54\n\t"
    "v_rcp_f32 v55, v55\n\t"
    "v_pk_fma_f32 v[48:49], v[48:49], v[52:53], v[52:53] neg_lo:[0,0,1] neg_hi:[0,0,1]\n\t"
    "v_pk_fma_f32 v[50:51], v[50:51], v[54:55], v[54:55] neg_lo:[0,0,1] neg_hi:[0,0,1]\n\t"
    "v_cvt_pk_bf16_f32 v232, v48, v49\n\t"
    "v_cvt_pk_bf16_f32 v233, v50, v51\n\t"
    "ds_read_b128 v[224:227], v244 offset:32768\n\t"
    "v_pk_fma_f32 v[200:201], v[200:201], s[58:59], v[128:129] op_sel:[0,1,0] op_sel_hi:[1,1,0]\n\t"
    "v_pk_fma_f32 v[202:203], v[202:203], s[58:59], v[128:129] op_sel:[0,1,0] op_sel_hi:[1,1,0]\n\t"
    "v_exp_f32 v204, v204\n\t"
    "v_exp_f32 v205, v205\n\t"
    "v_exp_f32 v206, v206\n\t"
    "v_exp_f32 v207, v207\n\t"
    "v_exp_f32 v200, v200 clamp\n\t"
    "v_exp_f32 v201, v201 clamp\n\t"
    "v_exp_f32 v202, v202 clamp\n\t"
    "v_exp_f32 v203, v203 clamp\n\t"
    "v_pk_fma_f32 v[200:201], v[200:201], s[52:53], 0.5 op_sel:[0,1,0] op_sel_hi:[1,1,0]\n\t"
    "v_pk_fma_f32 v[202:203], v[202:203], s[52:53], 0.5 op_sel:[0,1,0] op_sel_hi:[1,1,0]\n\t"
    "v_pk_fma_f32 v[204:205], v[200:201], v[204:205], v[200:201]\n\t"
    "v_pk_fma_f32 v[206:207], v[202:203], v[206:207], v[202:203]\n\t"
    "v_rcp_f32 v204, v204\n\t"
    "v_rcp_f32 v205, v205\n\t"
    "v_rcp_f32 v206, v206\n\t"
    "v_rcp_f32 v207, v207\n\t"
    "v_pk_fma_f32 v[200:201], v[200:201], v[204:205], v[204:205] neg_lo:[0,0,1] neg_hi:[0,0,1]\n\t"
    "v_pk_fma_f32 v[202:203], v[202:203], v[206:207], v[206:207] neg_lo:[0,0,1] neg_hi:[0,0,1]\n\t"
    "v_cvt_pk_bf16_f32 v234, v200, v201\n\t"
    "v_cvt_pk_bf16_f32 v235, v202, v203\n\t"
    "v_add_u32 v240, s28, v240\n\t"
    "s_waitcnt lgkmcnt(0)\n\t"
    "buffer_store_dwordx4 v[224:227], v240, s[12:15], 0 offen sc1\n\t"
    "v_pk_fma_f32 v[208:209], v[208:209], s[60:61], v[128:129] op_sel:[0,0,0] op_sel_hi:[1,0,0]\n\t"
    "v_pk_fma_f32 v[210:211], v[210:211], s[60:61], v[128:129] op_sel:[0,0,0] op_sel_hi:[1,0,0]\n\t"
    "v_exp_f32 v212, v212\n\t"
    "v_exp_f32 v213, v213\n\t"
    "v_exp_f32 v214, v214\n\t"
    "v_exp_f32 v215, v215\n\t"
    "v_exp_f32 v208, v208 clamp\n\t"
    "v_exp_f32 v209, v209 clamp\n\t"
    "v_exp_f32 v210, v210 clamp\n\t"
    "v_exp_f32 v211, v211 clamp\n\t"
    "v_pk_fma_f32 v[208:209], v[208:209], s[52:53], 0.5 op_sel:[0,1,0] op_sel_hi:[1,1,0]\n\t"
    "v_pk_fma_f32 v[210:211], v[210:211], s[52:53], 0.5 op_sel:[0,1,0] op_sel_hi:[1,1,0]\n\t"
    "v_pk_fma_f32 v[212:213], v[208:209], v[212:213], v[208:209]\n\t"
    "v_pk_fma_f32 v[214:215], v[210:211], v[214:215], v[210:211]\n\t"
    "v_rcp_f32 v212, v212\n\t"
    "v_rcp_f32 v213, v213\n\t"
    "v_rcp_f32 v214, v214\n\t"
    "v_rcp_f32 v215, v215\n\t"
    "v_pk_fma_f32 v[208:209], v[208:209], v[212:213], v[212:213] neg_lo:[0,0,1] neg_hi:[0,0,1]\n\t"
    "v_pk_fma_f32 v[210:211], v[210:211], v[214:215], v[214:215] neg_lo:[0,0,1] neg_hi:[0,0,1]\n\t"
    "v_cvt_pk_bf16_f32 v236, v208, v209\n\t"
    "v_cvt_pk_bf16_f32 v237, v210, v211\n\t"
    "ds_read_b128 v[228:231], v244 offset:33792\n\t"
    "ds_write2st64_b64 v243, v[232:233], v[234:235] offset0:96 offset1:100\n\t"
    "v_accvgpr_read_b32 v200, a56\n\t"
    "v_accvgpr_read_b32 v201, a57\n\t"
    "v_accvgpr_read_b32 v202, a58\n\t"
    "v_accvgpr_read_b32 v203, a59\n\t"
    "v_accvgpr_read_b32 v204, a60\n\t"
    "v_accvgpr_read_b32 v205, a61\n\t"
    "v_accvgpr_read_b32 v206, a62\n\t"
    "v_accvgpr_read_b32 v207, a63\n\t"
    "v_accvgpr_read_b32 v208, a120\n\t"
    "v_accvgpr_read_b32 v209, a121\n\t"
    "v_accvgpr_read_b32 v210, a122\n\t"
    "v_accvgpr_read_b32 v211, a123\n\t"
    "v_accvgpr_read_b32 v212, a124\n\t"
    "v_accvgpr_read_b32 v213, a125\n\t"
    "v_accvgpr_read_b32 v214, a126\n\t"
    "v_accvgpr_read_b32 v215, a127\n\t"
    "v_pk_mul_f32 v[60:61], v[60:61], s[58:59] op_sel:[0,1] op_sel_hi:[1,1] neg_lo:[1,0] neg_hi:[1,0]\n\t"
    "v_pk_mul_f32 v[62:63], v[62:63], s[58:59] op_sel:[0,1] op_sel_hi:[1,1] neg_lo:[1,0] neg_hi:[1,0]\n\t"
    "v_pk_add_f32 v[216:217], v[56:57], v[120:121] neg_lo:[0,1] neg_hi:[0,1]\n\t"
    "v_pk_add_f32 v[218:219], v[58:59], v[122:123] neg_lo:[0,1] neg_hi:[0,1]\n\t"
    "v_pk_fma_f32 v[220:221], v[124:125], s[58:59], v[60:61] op_sel:[0,1,0] op_sel_hi:[1,1,1]\n\t"
    "v_pk_fma_f32 v[222:223], v[126:127], s[58:59], v[62:63] op_sel:[0,1,0] op_sel_hi:[1,1,1]\n\t"
    "v_pk_add_f32 v[56:57], v[56:57], v[120:121]\n\t"
    "v_pk_add_f32 v[58:59], v[58:59], v[122:123]\n\t"
    "v_pk_fma_f32 v[60:61], v[124:125], s[58:59], v[60:61] op_sel:[0,1,0] op_sel_hi:[1,1,1] neg_lo:[1,0,0] neg_hi:[1,0,0]\n\t"
    "v_pk_fma_f32 v[62:63], v[126:127], s[58:59], v[62:63] op_sel:[0,1,0] op_sel_hi:[1,1,1] neg_lo:[1,0,0] neg_hi:[1,0,0]\n\t"
    "v_pk_add_f32 v[120:121], v[200:201], v[208:209]\n\t"
    "v_pk_add_f32 v[122:123], v[202:203], v[210:211]\n\t"
    "v_pk_add_f32 v[124:125], v[204:205], v[212:213]\n\t"
    "v_pk_add_f32 v[126:127], v[206:207], v[214:215]\n\t"
    "v_pk_add_f32 v[200:201], v[200:201], v[208:209] neg_lo:[0,1] neg_hi:[0,1]\n\t"
    "v_pk_add_f32 v[202:203], v[202:203], v[210:211] neg_lo:[0,1] neg_hi:[0,1]\n\t"
    "v_pk_add_f32 v[204:205], v[204:205], v[212:213] neg_lo:[0,1] neg_hi:[0,1]\n\t"
    "v_pk_add_f32 v[206:207], v[206:207], v[214:215] neg_lo:[0,1] neg_hi:[0,1]\n\t"
    "v_pk_fma_f32 v[208:209], v[56:57], -4.0, v[120:121] op_sel:[0,0,0] op_sel_hi:[1,0,1]\n\t"
    "v_pk_fma_f32 v[210:211], v[58:59], -4.0, v[122:123] op_sel:[0,0,0] op_sel_hi:[1,0,1]\n\t"
    "v_pk_fma_f32 v[212:213], v[124:125], s[60:61], v[60:61] op_sel:[0,1,0] op_sel_hi:[1,1,1] neg_lo:[1,0,1] neg_hi:[1,0,1]\n\t"
    "v_pk_fma_f32 v[214:215], v[126:127], s[60:61], v[62:63] op_sel:[0,1,0] op_sel_hi:[1,1,1] neg_lo:[1,0,1] neg_hi:[1,0,1]\n\t"
    "v_pk_fma_f32 v[56:57], v[56:57], s[50:51], v[120:121] op_sel:[0,1,0] op_sel_hi:[1,1,1] neg_lo:[1,0,0] neg_hi:[1,0,0]\n\t"
    "v_pk_fma_f32 v[58:59], v[58:59], s[50:51], v[122:123] op_sel:[0,1,0] op_sel_hi:[1,1,1] neg_lo:[1,0,0] neg_hi:[1,0,0]\n\t"
    "v_pk_fma_f32 v[60:61], v[60:61], s[50:51], v[212:213] op_sel:[0,0,0] op_sel_hi:[1,0,1] neg_lo:[1,0,1] neg_hi:[1,0,1]\n\t"
    "v_pk_fma_f32 v[62:63], v[62:63], s[50:51], v[214:215] op_sel:[0,0,0] op_sel_hi:[1,0,1] neg_lo:[1,0,1] neg_hi:[1,0,1]\n\t"
    "v_pk_fma_f32 v[192:193], v[216:217], s[48:49], v[192:193] op_sel:[0,1,0] op_sel_hi:[1,1,1]\n\t"
    "v_pk_fma_f32 v[194:195], v[218:219], s[48:49], v[194:195] op_sel:[0,1,0] op_sel_hi:[1,1,1]\n\t"
    "v_pk_fma_f32 v[196:197], v[220:221], s[68:69], v[196:197] op_sel:[0,0,0] op_sel_hi:[1,0,1]\n\t"
    "v_pk_fma_f32 v[198:199], v[222:223], s[68:69], v[198:199] op_sel:[0,0,0] op_sel_hi:[1,0,1]\n\t"
    "v_pk_fma_f32 v[200:201], v[216:217], -2.0, v[200:201] op_sel:[0,0,0] op_sel_hi:[1,0,1]\n\t"
    "v_pk_fma_f32 v[202:203], v[218:219], -2.0, v[202:203] op_sel:[0,0,0] op_sel_hi:[1,0,1]\n\t"
    "v_pk_fma_f32 v[204:205], v[204:205], s[60:61], v[220:221] op_sel:[0,0,0] op_sel_hi:[1,0,1] neg_lo:[1,0,1] neg_hi:[1,0,1]\n\t"
    "v_pk_fma_f32 v[206:207], v[206:207], s[60:61], v[222:223] op_sel:[0,0,0] op_sel_hi:[1,0,1] neg_lo:[1,0,1] neg_hi:[1,0,1]\n\t"
    "v_pk_fma_f32 v[192:193], v[208:209], s[62:63], v[192:193] op_sel:[0,0,0] op_sel_hi:[1,0,1]\n\t"
    "v_pk_fma_f32 v[194:195], v[210:211], s[62:63], v[194:195] op_sel:[0,0,0] op_sel_hi:[1,0,1]\n\t"
    "v_pk_fma_f32 v[196:197], v[212:213], s[68:69], v[196:197] op_sel:[0,1,0] op_sel_hi:[1,1,1]\n\t"
    "v_pk_fma_f32 v[198:199], v[214:215], s[68:69], v[198:199] op_sel:[0,1,0] op_sel_hi:[1,1,1]\n\t"
    "v_pk_fma_f32 v[192:193], v[56:57], s[62:63], v[192:193] op_sel:[0,1,0] op_sel_hi:[1,1,1]\n\t"
    "v_pk_fma_f32 v[194:195], v[58:59], s[62:63], v[194:195] op_sel:[0,1,0] op_sel_hi:[1,1,1]\n\t"
    "v_pk_fma_f32 v[196:197], v[60:61], s[70:71], v[196:197] op_sel:[0,0,0] op_sel_hi:[1,0,1]\n\t"
    "v_pk_fma_f32 v[198:199], v[62:63], s[70:71], v[198:199] op_sel:[0,0,0] op_sel_hi:[1,0,1]\n\t"
    "v_pk_fma_f32 v[192:193], v[200:201], s[64:65], v[192:193] op_sel:[0,0,0] op_sel_hi:[1,0,1]\n\t"
    "v_pk_fma_f32 v[194:195], v[202:203], s[64:65], v[194:195] op_sel:[0,0,0] op_sel_hi:[1,0,1]\n\t"
    "v_pk_fma_f32 v[196:197], v[204:205], s[74:75], v[196:197] op_sel:[0,0,0] op_sel_hi:[1,0,1]\n\t"
    "v_pk_fma_f32 v[198:199], v[206:207], s[74:75], v[198:199] op_sel:[0,0,0] op_sel_hi:[1,0,1]\n\t"
    "v_pk_fma_f32 v[200:201], v[192:193], s[64:65], v[200:201] op_sel:[0,1,0] op_sel_hi:[1,1,1]\n\t"
    "v_pk_fma_f32 v[202:203], v[194:195], s[64:65], v[202:203] op_sel:[0,1,0] op_sel_hi:[1,1,1]\n\t"
    "v_pk_fma_f32 v[204:205], v[196:197], s[74:75], v[204:205] op_sel:[0,1,0] op_sel_hi:[1,1,1]\n\t"
    "v_pk_fma_f32 v[206:207], v[198:199], s[74:75], v[206:207] op_sel:[0,1,0] op_sel_hi:[1,1,1]\n\t"
    "v_pk_fma_f32 v[216:217], v[192:193], s[66:67], v[216:217] op_sel:[0,1,0] op_sel_hi:[1,1,1]\n\t"
    "v_pk_fma_f32 v[218:219], v[194:195], s[66:67], v[218:219] op_sel:[0,1,0] op_sel_hi:[1,1,1]\n\t"
    "v_pk_fma_f32 v[220:221], v[196:197], s[76:77], v[220:221] op_sel:[0,0,0] op_sel_hi:[1,0,1]\n\t"
    "v_pk_fma_f32 v[222:223], v[198:199], s[76:77], v[222:223] op_sel:[0,0,0] op_sel_hi:[1,0,1]\n\t"
    "v_pk_fma_f32 v[120:121], v[200:201], s[66:67], v[216:217] op_sel:[0,0,0] op_sel_hi:[1,0,1]\n\t"
    "v_pk_fma_f32 v[122:123], v[202:203], s[66:67], v[218:219] op_sel:[0,0,0] op_sel_hi:[1,0,1]\n\t"
    "v_pk_fma_f32 v[124:125], v[220:221], s[50:51], v[204:205] op_sel:[0,0,0] op_sel_hi:[1,0,1] neg_lo:[1,0,1] neg_hi:[1,0,1]\n\t"
    "v_pk_fma_f32 v[126:127], v[222:223], s[50:51], v[206:207] op_sel:[0,0,0] op_sel_hi:[1,0,1] neg_lo:[1,0,1] neg_hi:[1,0,1]\n\t"
    "v_pk_fma_f32 v[120:121], v[184:185], s[66:67], v[120:121] op_sel:[0,1,0] op_sel_hi:[1,1,1] neg_lo:[1,0,0] neg_hi:[1,0,0]\n\t"
    "v_pk_fma_f32 v[122:123], v[186:187], s[66:67], v[122:123] op_sel:[0,1,0] op_sel_hi:[1,1,1] neg_lo:[1,0,0] neg_hi:[1,0,0]\n\t"
    "v_pk_fma_f32 v[124:125], v[188:189], s[76:77], v[124:125] op_sel:[0,1,0] op_sel_hi:[1,1,1]\n\t"
    "v_pk_fma_f32 v[126:127], v[190:191], s[76:77], v[126:127] op_sel:[0,1,0] op_sel_hi:[1,1,1]\n\t"
    "v_pk_fma_f32 v[120:121], v[120:121], s[56:57], v[128:129] op_sel:[0,0,0] op_sel_hi:[1,0,0]\n\t"
    "v_pk_fma_f32 v[122:123], v[122:123], s[56:57], v[128:129] op_sel:[0,0,0] op_sel_hi:[1,0,0]\n\t"
    "v_exp_f32 v124, v124\n\t"
    "v_exp_f32 v125, v125\n\t"
    "v_exp_f32 v126, v126\n\t"
    "v_exp_f32 v127, v127\n\t"
    "v_exp_f32 v120, v120 clamp\n\t"
    "v_exp_f32 v121, v121 clamp\n\t"
    "v_exp_f32 v122, v122 clamp\n\t"
    "v_exp_f32 v123, v123 clamp\n\t"
    "v_pk_fma_f32 v[120:121], v[120:121], s[52:53], 0.5 op_sel:[0,1,0] op_sel_hi:[1,1,0]\n\t"
    "v_pk_fma_f32 v[122:123], v[122:123], s[52:53], 0.5 op_sel:[0,1,0] op_sel_hi:[1,1,0]\n\t"
    "v_pk_fma_f32 v[124:125], v[120:121], v[124:125], v[120:121]\n\t"
    "v_pk_fma_f32 v[126:127], v[122:123], v[126:127], v[122:123]\n\t"
    "v_rcp_f32 v124, v124\n\t"
    "v_rcp_f32 v125, v125\n\t"
    "v_rcp_f32 v126, v126\n\t"
    "v_rcp_f32 v127, v127\n\t"
    "v_pk_fma_f32 v[120:121], v[120:121], v[124:125], v[124:125] neg_lo:[0,0,1] neg_hi:[0,0,1]\n\t"
    "v_pk_fma_f32 v[122:123], v[122:123], v[126:127], v[126:127] neg_lo:[0,0,1] neg_hi:[0,0,1]\n\t"
    "v_cvt_pk_bf16_f32 v238, v120, v121\n\t"
    "v_cvt_pk_bf16_f32 v239, v122, v123\n\t"
    "v_add_u32 v241, s28, v241\n\t"
    "s_waitcnt lgkmcnt(1)\n\t"
    "buffer_store_dwordx4 v[228:231], v241, s[12:15], 0 offen sc1\n\t"
    "ds_write2st64_b64 v243, v[236:237], v[238:239] offset0:104 offset1:108\n\t"
    "v_pk_fma_f32 v[56:57], v[56:57], s[60:61], v[128:129] op_sel:[0,0,0] op_sel_hi:[1,0,0] neg_lo:[1,0,0] neg_hi:[1,0,0]\n\t"
    "v_pk_fma_f32 v[58:59], v[58:59], s[60:61], v[128:129] op_sel:[0,0,0] op_sel_hi:[1,0,0] neg_lo:[1,0,0] neg_hi:[1,0,0]\n\t"
    "v_exp_f32 v60, v60\n\t"
    "v_exp_f32 v61, v61\n\t"
    "v_exp_f32 v62, v62\n\t"
    "v_exp_f32 v63, v63\n\t"
    "v_exp_f32 v56, v56 clamp\n\t"
    "v_exp_f32 v57, v57 clamp\n\t"
    "v_exp_f32 v58, v58 clamp\n\t"
    "v_exp_f32 v59, v59 clamp\n\t"
    "v_pk_fma_f32 v[56:57], v[56:57], s[52:53], 0.5 op_sel:[0,1,0] op_sel_hi:[1,1,0]\n\t"
    "v_pk_fma_f32 v[58:59], v[58:59], s[52:53], 0.5 op_sel:[0,1,0] op_sel_hi:[1,1,0]\n\t"
    "v_pk_fma_f32 v[60:61], v[56:57], v[60:61], v[56:57]\n\t"
    "v_pk_fma_f32 v[62:63], v[58:59], v[62:63], v[58:59]\n\t"
    "v_rcp_f32 v60, v60\n\t"
    "v_rcp_f32 v61, v61\n\t"
    "v_rcp_f32 v62, v62\n\t"
    "v_rcp_f32 v63, v63\n\t"
    "v_pk_fma_f32 v[56:57], v[56:57], v[60:61], v[60:61] neg_lo:[0,0,1] neg_hi:[0,0,1]\n\t"
    "v_pk_fma_f32 v[58:59], v[58:59], v[62:63], v[62:63] neg_lo:[0,0,1] neg_hi:[0,0,1]\n\t"
    "v_cvt_pk_bf16_f32 v232, v56, v57\n\t"
    "v_cvt_pk_bf16_f32 v233, v58, v59\n\t"
    "v_mfma_f32_32x32x16_bf16 a[128:143], a[208:211], a[208:211], 0\n\t"
    "s_waitcnt lgkmcnt(0)\n\t"
    "s_barrier\n\t"
    "ds_read_b128 v[224:227], v244 offset:40960\n\t"
    "v_pk_fma_f32 v[200:201], v[200:201], s[58:59], v[128:129] op_sel:[0,1,0] op_sel_hi:[1,1,0]\n\t"
    "v_pk_fma_f32 v[202:203], v[202:203], s[58:59], v[128:129] op_sel:[0,1,0] op_sel_hi:[1,1,0]\n\t"
    "v_exp_f32 v204, v204\n\t"
    "v_exp_f32 v205, v205\n\t"
    "v_exp_f32 v206, v206\n\t"
    "v_exp_f32 v207, v207\n\t"
    "v_exp_f32 v200, v200 clamp\n\t"
    "v_exp_f32 v201, v201 clamp\n\t"
    "v_exp_f32 v202, v202 clamp\n\t"
    "v_exp_f32 v203, v203 clamp\n\t"
    "v_pk_fma_f32 v[200:201], v[200:201], s[52:53], 0.5 op_sel:[0,1,0] op_sel_hi:[1,1,0]\n\t"
    "v_pk_fma_f32 v[202:203], v[202:203], s[52:53], 0.5 op_sel:[0,1,0] op_sel_hi:[1,1,0]\n\t"
    "v_pk_fma_f32 v[204:205], v[200:201], v[204:205], v[200:201]\n\t"
    "v_pk_fma_f32 v[206:207], v[202:203], v[206:207], v[202:203]\n\t"
    "v_rcp_f32 v204, v204\n\t"
    "v_rcp_f32 v205, v205\n\t"
    "v_rcp_f32 v206, v206\n\t"
    "v_rcp_f32 v207, v207\n\t"
    "v_pk_fma_f32 v[200:201], v[200:201], v[204:205], v[204:205] neg_lo:[0,0,1] neg_hi:[0,0,1]\n\t"
    "v_pk_fma_f32 v[202:203], v[202:203], v[206:207], v[206:207] neg_lo:[0,0,1] neg_hi:[0,0,1]\n\t"
    "v_cvt_pk_bf16_f32 v234, v200, v201\n\t"
    "v_cvt_pk_bf16_f32 v235, v202, v203\n\t"
    "v_add_u32 v240, s28, v240\n\t"
    "s_waitcnt lgkmcnt(0)\n\t"
    "buffer_store_dwordx4 v[224:227], v240, s[12:15], 0 offen sc1\n\t"
    "ds_read_b128 v[228:231], v244 offset:41984\n\t"
    "v_pk_fma_f32 v[208:209], v[208:209], s[60:61], v[128:129] op_sel:[0,0,0] op_sel_hi:[1,0,0]\n\t"
    "v_pk_fma_f32 v[210:211], v[210:211], s[60:61], v[128:129] op_sel:[0,0,0] op_sel_hi:[1,0,0]\n\t"
    "v_exp_f32 v212, v212\n\t"
    "v_exp_f32 v213, v213\n\t"
    "v_exp_f32 v214, v214\n\t"
    "v_exp_f32 v215, v215\n\t"
    "v_exp_f32 v208, v208 clamp\n\t"
    "v_exp_f32 v209, v209 clamp\n\t"
    "v_exp_f32 v210, v210 clamp\n\t"
    "v_exp_f32 v211, v211 clamp\n\t"
    "v_pk_fma_f32 v[208:209], v[208:209], s[52:53], 0.5 op_sel:[0,1,0] op_sel_hi:[1,1,0]\n\t"
    "v_pk_fma_f32 v[210:211], v[210:211], s[52:53], 0.5 op_sel:[0,1,0] op_sel_hi:[1,1,0]\n\t"
    "v_pk_fma_f32 v[212:213], v[208:209], v[212:213], v[208:209]\n\t"
    "v_pk_fma_f32 v[214:215], v[210:211], v[214:215], v[210:211]\n\t"
    "v_rcp_f32 v212, v212\n\t"
    "v_rcp_f32 v213, v213\n\t"
    "v_rcp_f32 v214, v214\n\t"
    "v_rcp_f32 v215, v215\n\t"
    "v_pk_fma_f32 v[208:209], v[208:209], v[212:213], v[212:213] neg_lo:[0,0,1] neg_hi:[0,0,1]\n\t"
    "v_pk_fma_f32 v[210:211], v[210:211], v[214:215], v[214:215] neg_lo:[0,0,1] neg_hi:[0,0,1]\n\t"
    "v_cvt_pk_bf16_f32 v236, v208, v209\n\t"
    "v_cvt_pk_bf16_f32 v237, v210, v211\n\t"
    "v_add_u32 v241, s28, v241\n\t"
    "s_waitcnt lgkmcnt(0)\n\t"
    "buffer_store_dwordx4 v[228:231], v241, s[12:15], 0 offen sc1\n\t"
    "ds_read_b128 v[224:227], v244 offset:49152\n\t"
    "ds_write2st64_b64 v243, v[232:233], v[234:235] offset0:112 offset1:116\n\t"
    "v_fmac_f32_dpp v64, v192, v129 row_shr:1 row_mask:0xf bank_mask:0xf bound_ctrl:1\n\t"
    "v_fmac_f32_dpp v65, v193, v129 row_shr:1 row_mask:0xf bank_mask:0xf bound_ctrl:1\n\t"
    "v_fmac_f32_dpp v66, v194, v129 row_shr:1 row_mask:0xf bank_mask:0xf bound_ctrl:1\n\t"
    "v_fmac_f32_dpp v67, v195, v129 row_shr:1 row_mask:0xf bank_mask:0xf bound_ctrl:1\n\t"
    "v_fmac_f32_dpp v68, v196, v130 row_shr:1 row_mask:0xf bank_mask:0xf bound_ctrl:1\n\t"
    "v_fmac_f32_dpp v69, v197, v130 row_shr:1 row_mask:0xf bank_mask:0xf bound_ctrl:1\n\t"
    "v_fmac_f32_dpp v70, v198, v130 row_shr:1 row_mask:0xf bank_mask:0xf bound_ctrl:1\n\t"
    "v_fmac_f32_dpp v71, v199, v130 row_shr:1 row_mask:0xf bank_mask:0xf bound_ctrl:1\n\t"
    "v_pk_fma_f32 v[64:65], v[64:65], s[56:57], v[128:129] op_sel:[0,0,0] op_sel_hi:[1,0,0]\n\t"
    "v_pk_fma_f32 v[66:67], v[66:67], s[56:57], v[128:129] op_sel:[0,0,0] op_sel_hi:[1,0,0]\n\t"
    "v_exp_f32 v68, v68\n\t"
    "v_exp_f32 v69, v69\n\t"
    "v_exp_f32 v70, v70\n\t"
    "v_exp_f32 v71, v71\n\t"
    "v_exp_f32 v64, v64 clamp\n\t"
    "v_exp_f32 v65, v65 clamp\n\t"
    "v_exp_f32 v66, v66 clamp\n\t"
    "v_exp_f32 v67, v67 clamp\n\t"
    "v_pk_fma_f32 v[64:65], v[64:65], s[52:53], 0.5 op_sel:[0,1,0] op_sel_hi:[1,1,0]\n\t"
    "v_pk_fma_f32 v[66:67], v[66:67], s[52:53], 0.5 op_sel:[0,1,0] op_sel_hi:[1,1,0]\n\t"
    "v_pk_fma_f32 v[68:69], v[64:65], v[68:69], v[64:65]\n\t"
    "v_pk_fma_f32 v[70:71], v[66:67], v[70:71], v[66:67]\n\t"
    "v_rcp_f32 v68, v68\n\t"
    "v_rcp_f32 v69, v69\n\t"
    "v_rcp_f32 v70, v70\n\t"
    "v_rcp_f32 v71, v71\n\t"
    "v_pk_fma_f32 v[64:65], v[64:65], v[68:69], v[68:69] neg_lo:[0,0,1] neg_hi:[0,0,1]\n\t"
    "v_pk_fma_f32 v[66:67], v[66:67], v[70:71], v[70:71] neg_lo:[0,0,1] neg_hi:[0,0,1]\n\t"
    "v_cvt_pk_bf16_f32 v238, v64, v65\n\t"
    "v_cvt_pk_bf16_f32 v239, v66, v67\n\t"
    "v_add_u32 v240, s28, v240\n\t"
    "s_waitcnt lgkmcnt(1)\n\t"
    "buffer_store_dwordx4 v[224:227], v240, s[12:15], 0 offen sc1\n\t"
    "ds_read_b128 v[228:231], v244 offset:50176\n\t"
    "ds_write2st64_b64 v243, v[236:237], v[238:239] offset0:120 offset1:124\n\t"
    "v_add_u32 v241, s28, v241\n\t"
    "s_waitcnt lgkmcnt(1)\n\t"
    "buffer_store_dwordx4 v[228:231], v241, s[12:15], 0 offen sc1\n\t"
    "s_waitcnt lgkmcnt(0)\n\t"
    "s_barrier\n\t"
    "ds_read_b128 v[200:203], v244 offset:57344\n\t"
    "ds_read_b128 v[204:207], v244 offset:58368\n\t"
    "v_add_u32 v240, s25, v240\n\t"
    "v_add_u32 v241, s25, v241\n\t"
    "s_waitcnt lgkmcnt(1)\n\t"
    "s_nop 0\n\t"
    "buffer_store_dwordx4 v[200:203], v240, s[12:15], 0 offen sc1\n\t"
    "s_waitcnt lgkmcnt(0)\n\t"
    "s_nop 0\n\t"
    "buffer_store_dwordx4 v[204:207], v241, s[12:15], 0 offen sc1\n\t"
    "s_branch L_end_%=\n\t"
    "L_codeend:\n\t"
    "L_slow_%=:\n\t"
    "s_mov_b32 s74, 0\n\t"
    "s_waitcnt vmcnt(0)\n\t"
    "v_mov_b32 v250, v199\n\t"
    "v_xor_b32 v197, s31, v250\n\t"
    "v_and_b32 v197, s94, v197\n\t"
    "v_cmp_ne_u32 vcc, 0, v197\n\t"
    "s_cbranch_vccz L_back_%=\n\t"
    "L_sl1_%=:\n\t"
    "buffer_load_dword v197, v193, s[88:91], 0 offen offset:0 sc1\n\t"
    "s_waitcnt vmcnt(0)\n\t"
    "v_bfi_b32 v197, v198, s70, v197\n\t"
    "buffer_store_dword v197, v196, s[88:91], 0 offen sc1\n\t"
    "buffer_load_dword v250, v194, s[88:91], 0 offen offset:0 sc1\n\t"
    "s_waitcnt vmcnt(0)\n\t"
    "v_xor_b32 v197, s31, v250\n\t"
    "v_and_b32 v197, s94, v197\n\t"
    "v_cmp_ne_u32 vcc, 0, v197\n\t"
    "s_cbranch_vccz L_back_%=\n\t"
    "s_sleep 4\n\t"
    "s_add_u32 s74, s74, 1\n\t"
    "s_cmp_lt_u32 s74, 0x80000\n\t"
    "s_cbranch_scc1 L_sl1_%=\n\t"
    "s_branch L_back_%=\n\t"
    "L_sc_%=:\n\t"
    "v_pk_mul_f32 v[0:1], v[0:1], s[48:49] op_sel:[0,0] op_sel_hi:[1,0]\n\t"
    "v_pk_mul_f32 v[2:3], v[2:3], s[48:49] op_sel:[0,0] op_sel_hi:[1,0]\n\t"
    "v_pk_mul_f32 v[4:5], v[4:5], s[48:49] op_sel:[0,0] op_sel_hi:[1,0]\n\t"
    "v_pk_mul_f32 v[6:7], v[6:7], s[48:49] op_sel:[0,0] op_sel_hi:[1,0]\n\t"
    "v_pk_mul_f32 v[8:9], v[8:9], s[48:49] op_sel:[0,0] op_sel_hi:[1,0]\n\t"
    "v_pk_mul_f32 v[10:11], v[10:11], s[48:49] op_sel:[0,0] op_sel_hi:[1,0]\n\t"
    "v_pk_mul_f32 v[12:13], v[12:13], s[48:49] op_sel:[0,0] op_sel_hi:[1,0]\n\t"
    "v_pk_mul_f32 v[14:15], v[14:15], s[48:49] op_sel:[0,0] op_sel_hi:[1,0]\n\t"
    "v_pk_mul_f32 v[16:17], v[16:17], s[48:49] op_sel:[0,0] op_sel_hi:[1,0]\n\t"
    "v_pk_mul_f32 v[18:19], v[18:19], s[48:49] op_sel:[0,0] op_sel_hi:[1,0]\n\t"
    "v_pk_mul_f32 v[20:21], v[20:21], s[48:49] op_sel:[0,0] op_sel_hi:[1,0]\n\t"
    "v_pk_mul_f32 v[22:23], v[22:23], s[48:49] op_sel:[0,0] op_sel_hi:[1,0]\n\t"
    "v_pk_mul_f32 v[24:25], v[24:25], s[48:49] op_sel:[0,0] op_sel_hi:[1,0]\n\t"
    "v_pk_mul_f32 v[26:27], v[26:27], s[48:49] op_sel:[0,0] op_sel_hi:[1,0]\n\t"
    "v_pk_mul_f32 v[28:29], v[28:29], s[48:49] op_sel:[0,0] op_sel_hi:[1,0]\n\t"
    "v_pk_mul_f32 v[30:31], v[30:31], s[48:49] op_sel:[0,0] op_sel_hi:[1,0]\n\t"
    "v_pk_mul_f32 v[32:33], v[32:33], s[48:49] op_sel:[0,0] op_sel_hi:[1,0]\n\t"
    "v_pk_mul_f32 v[34:35], v[34:35], s[48:49] op_sel:[0,0] op_sel_hi:[1,0]\n\t"
    "v_pk_mul_f32 v[36:37], v[36:37], s[48:49] op_sel:[0,0] op_sel_hi:[1,0]\n\t"
    "v_pk_mul_f32 v[38:39], v[38:39], s[48:49] op_sel:[0,0] op_sel_hi:[1,0]\n\t"
    "v_pk_mul_f32 v[40:41], v[40:41], s[48:49] op_sel:[0,0] op_sel_hi:[1,0]\n\t"
    "v_pk_mul_f32 v[42:43], v[42:43], s[48:49] op_sel:[0,0] op_sel_hi:[1,0]\n\t"
    "v_pk_mul_f32 v[44:45], v[44:45], s[48:49] op_sel:[0,0] op_sel_hi:[1,0]\n\t"
    "v_pk_mul_f32 v[46:47], v[46:47], s[48:49] op_sel:[0,0] op_sel_hi:[1,0]\n\t"
    "v_pk_mul_f32 v[48:49], v[48:49], s[48:49] op_sel:[0,0] op_sel_hi:[1,0]\n\t"
    "v_pk_mul_f32 v[50:51], v[50:51], s[48:49] op_sel:[0,0] op_sel_hi:[1,0]\n\t"
    "v_pk_mul_f32 v[52:53], v[52:53], s[48:49] op_sel:[0,0] op_sel_hi:[1,0]\n\t"
    "v_pk_mul_f32 v[54:55], v[54:55], s[48:49] op_sel:[0,0] op_sel_hi:[1,0]\n\t"
    "v_pk_mul_f32 v[56:57], v[56:57], s[48:49] op_sel:[0,0] op_sel_hi:[1,0]\n\t"
    "v_pk_mul_f32 v[58:59], v[58:59], s[48:49] op_sel:[0,0] op_sel_hi:[1,0]\n\t"
    "v_pk_mul_f32 v[60:61], v[60:61], s[48:49] op_sel:[0,0] op_sel_hi:[1,0]\n\t"
    "v_pk_mul_f32 v[62:63], v[62:63], s[48:49] op_sel:[0,0] op_sel_hi:[1,0]\n\t"
    "v_pk_mul_f32 v[72:73], v[72:73], s[48:49] op_sel:[0,0] op_sel_hi:[1,0]\n\t"
    "v_pk_mul_f32 v[74:75], v[74:75], s[48:49] op_sel:[0,0] op_sel_hi:[1,0]\n\t"
    "v_pk_mul_f32 v[76:77], v[76:77], s[48:49] op_sel:[0,0] op_sel_hi:[1,0]\n\t"
    "v_pk_mul_f32 v[78:79], v[78:79], s[48:49] op_sel:[0,0] op_sel_hi:[1,0]\n\t"
    "v_pk_mul_f32 v[80:81], v[80:81], s[48:49] op_sel:[0,0] op_sel_hi:[1,0]\n\t"
    "v_pk_mul_f32 v[82:83], v[82:83], s[48:49] op_sel:[0,0] op_sel_hi:[1,0]\n\t"
    "v_pk_mul_f32 v[84:85], v[84:85], s[48:49] op_sel:[0,0] op_sel_hi:[1,0]\n\t"
    "v_pk_mul_f32 v[86:87], v[86:87], s[48:49] op_sel:[0,0] op_sel_hi:[1,0]\n\t"
    "v_pk_mul_f32 v[88:89], v[88:89], s[48:49] op_sel:[0,0] op_sel_hi:[1,0]\n\t"
    "v_pk_mul_f32 v[90:91], v[90:91], s[48:49] op_sel:[0,0] op_sel_hi:[1,0]\n\t"
    "v_pk_mul_f32 v[92:93], v[92:93], s[48:49] op_sel:[0,0] op_sel_hi:[1,0]\n\t"
    "v_pk_mul_f32 v[94:95], v[94:95], s[48:49] op_sel:[0,0] op_sel_hi:[1,0]\n\t"
    "v_pk_mul_f32 v[96:97], v[96:97], s[48:49] op_sel:[0,0] op_sel_hi:[1,0]\n\t"
    "v_pk_mul_f32 v[98:99], v[98:99], s[48:49] op_sel:[0,0] op_sel_hi:[1,0]\n\t"
    "v_pk_mul_f32 v[100:101], v[100:101], s[48:49] op_sel:[0,0] op_sel_hi:[1,0]\n\t"
    "v_pk_mul_f32 v[102:103], v[102:103], s[48:49] op_sel:[0,0] op_sel_hi:[1,0]\n\t"
    "v_pk_mul_f32 v[104:105], v[104:105], s[48:49] op_sel:[0,0] op_sel_hi:[1,0]\n\t"
    "v_pk_mul_f32 v[106:107], v[106:107], s[48:49] op_sel:[0,0] op_sel_hi:[1,0]\n\t"
    "v_pk_mul_f32 v[108:109], v[108:109], s[48:49] op_sel:[0,0] op_sel_hi:[1,0]\n\t"
    "v_pk_mul_f32 v[110:111], v[110:111], s[48:49] op_sel:[0,0] op_sel_hi:[1,0]\n\t"
    "v_pk_mul_f32 v[112:113], v[112:113], s[48:49] op_sel:[0,0] op_sel_hi:[1,0]\n\t"
    "v_pk_mul_f32 v[114:115], v[114:115], s[48:49] op_sel:[0,0] op_sel_hi:[1,0]\n\t"
    "v_pk_mul_f32 v[116:117], v[116:117], s[48:49] op_sel:[0,0] op_sel_hi:[1,0]\n\t"
    "v_pk_mul_f32 v[118:119], v[118:119], s[48:49] op_sel:[0,0] op_sel_hi:[1,0]\n\t"
    "v_pk_mul_f32 v[120:121], v[120:121], s[48:49] op_sel:[0,0] op_sel_hi:[1,0]\n\t"
    "v_pk_mul_f32 v[122:123], v[122:123], s[48:49] op_sel:[0,0] op_sel_hi:[1,0]\n\t"
    "v_pk_mul_f32 v[124:125], v[124:125], s[48:49] op_sel:[0,0] op_sel_hi:[1,0]\n\t"
    "v_pk_mul_f32 v[126:127], v[126:127], s[48:49] op_sel:[0,0] op_sel_hi:[1,0]\n\t"
    "v_pk_mul_f32 v[128:129], v[128:129], s[48:49] op_sel:[0,0] op_sel_hi:[1,0]\n\t"
    "v_pk_mul_f32 v[130:131], v[130:131], s[48:49] op_sel:[0,0] op_sel_hi:[1,0]\n\t"
    "v_pk_mul_f32 v[132:133], v[132:133], s[48:49] op_sel:[0,0] op_sel_hi:[1,0]\n\t"
    "v_pk_mul_f32 v[134:135], v[134:135], s[48:49] op_sel:[0,0] op_sel_hi:[1,0]\n\t"
    "v_pk_mul_f32 v[64:65], v[64:65], s[48:49] op_sel:[0,0] op_sel_hi:[1,0]\n\t"
    "v_pk_mul_f32 v[66:67], v[66:67], s[48:49] op_sel:[0,0] op_sel_hi:[1,0]\n\t"
    "v_pk_mul_f32 v[68:69], v[68:69], s[48:49] op_sel:[0,0] op_sel_hi:[1,0]\n\t"
    "v_pk_mul_f32 v[70:71], v[70:71], s[48:49] op_sel:[0,0] op_sel_hi:[1,0]\n\t"
    "v_pk_mul_f32 v[232:233], v[232:233], s[48:49] op_sel:[0,0] op_sel_hi:[1,0]\n\t"
    "v_pk_mul_f32 v[234:235], v[234:235], s[48:49] op_sel:[0,0] op_sel_hi:[1,0]\n\t"
    "v_pk_mul_f32 v[236:237], v[236:237], s[48:49] op_sel:[0,0] op_sel_hi:[1,0]\n\t"
    "v_pk_mul_f32 v[238:239], v[238:239], s[48:49] op_sel:[0,0] op_sel_hi:[1,0]\n\t"
    "s_nop 1\n\t"
    "s_branch L_scb_%=\n\t"
    "L_end_%=:\n\t"
            : "+{v242}"(vW), "+{v243}"(vXt0), "+{v244}"(vXt1), "+{v245}"(vB0), "+{v246}"(vB1), "+{v248}"(vM15), "+{v251}"(vOut), "+{v176}"(vXr0), "+{v177}"(vXr1), "+{v178}"(vXr2), "+{v179}"(vXr3), "+{v180}"(vXd), "+{v181}"(vA0), "+{v182}"(vA1), "+{v183}"(vA2), "+{v169}"(vHwR), "+{v184}"(vHwT), "+{v186}"(vWd), "+{v187}"(vTr0), "+{v188}"(vTr1), "+{v172}"(vP), "+{v173}"(vP3), "+{v174}"(vPr), "+{v190}"(vPs), "+{v193}"(vQa), "+{v194}"(vCa), "+{v196}"(vRa), "+{v195}"(vQs), "+{v175}"(vL16), "+{v189}"(vTr2), "+{v170}"(vDum), "+{v[136:139]}"(hx0), "={s44}"(lim), "={s45}"(mx), "={s47}"(ew)
            : "{s20}"(w4352), "{s92}"(w1024), "{s96}"(rot64), "{s[8:11]}"(xd), "{s[84:87]}"(wd), "{s[12:15]}"(od), "{s[16:17]}"(wsp), "{s[18:19]}"(wso), "{s21}"(w2112), "{s22}"(force), "{s23}"(forcew), "{s[88:91]}"(hd), "{s93}"(mt), "{s94}"(pm), "{s95}"(first), "s"(ldsbase)
            : "memory", "scc", "vcc", "v0", "v1", "v2", "v3", "v4", "v5", "v6", "v7", "v8", "v9", "v10", "v11", "v12", "v13", "v14", "v15", "v16", "v17", "v18", "v19", "v20", "v21", "v22", "v23", "v24", "v25", "v26", "v27", "v28", "v29", "v30", "v31", "v32", "v33", "v34", "v35", "v36", "v37", "v38", "v39", "v40", "v41", "v42", "v43", "v44", "v45", "v46", "v47", "v48", "v49", "v50", "v51", "v52", "v53", "v54", "v55", "v56", "v57", "v58", "v59", "v60", "v61", "v62", "v63", "v64", "v65", "v66", "v67", "v68", "v69", "v70", "v71", "v72", "v73", "v74", "v75", "v76", "v77", "v78", "v79", "v80", "v81", "v82", "v83", "v84", "v85", "v86", "v87", "v88", "v89", "v90", "v91", "v92", "v93", "v94", "v95", "v96", "v97", "v98", "v99", "v100", "v101", "v102", "v103", "v104", "v105", "v106", "v107", "v108", "v109", "v110", "v111", "v112", "v113", "v114", "v115", "v116", "v117", "v118", "v119", "v120", "v121", "v122", "v123", "v124", "v125", "v126", "v127", "v128", "v129", "v130", "v131", "v132", "v133", "v134", "v135", "v140", "v141", "v142", "v143", "v144", "v145", "v146", "v147", "v148", "v149", "v150", "v151", "v152", "v153", "v154", "v155", "v156", "v157", "v158", "v159", "v160", "v161", "v162", "v163", "v164", "v165", "v166", "v167", "v168", "v171", "v185", "v191", "v192", "v197", "v198", "v199", "v200", "v201", "v202", "v203", "v204", "v205", "v206", "v207", "v208", "v209", "v210", "v211", "v212", "v213", "v214", "v215", "v216", "v217", "v218", "v219", "v220", "v221", "v222", "v223", "v224", "v225", "v226", "v227", "v228", "v229", "v230", "v231", "v232", "v233", "v234", "v235", "v236", "v237", "v238", "v239", "v240", "v241", "v247", "v249", "v250", "v252", "a0", "a1", "a2", "a3", "a4", "a5", "a6", "a7", "a8", "a9", "a10", "a11", "a12", "a13", "a14", "a15", "a16", "a17", "a18", "a19", "a20", "a21", "a22", "a23", "a24", "a25", "a26", "a27", "a28", "a29", "a30", "a31", "a32", "a33", "a34", "a35", "a36", "a37", "a38", "a39", "a40", "a41", "a42", "a43", "a44", "a45", "a46", "a47", "a48", "a49", "a50", "a51", "a52", "a53", "a54", "a55", "a56", "a57", "a58", "a59", "a60", "a61", "a62", "a63", "a64", "a65", "a66", "a67", "a68", "a69", "a70", "a71", "a72", "a73", "a74", "a75", "a76", "a77", "a78", "a79", "a80", "a81", "a82", "a83", "a84", "a85", "a86", "a87", "a88", "a89", "a90", "a91", "a92", "a93", "a94", "a95", "a96", "a97", "a98", "a99", "a100", "a101", "a102", "a103", "a104", "a105", "a106", "a107", "a108", "a109", "a110", "a111", "a112", "a113", "a114", "a115", "a116", "a117", "a118", "a119", "a120", "a121", "a122", "a123", "a124", "a125", "a126", "a127", "a128", "a129", "a130", "a131", "a132", "a133", "a134", "a135", "a136", "a137", "a138", "a139", "a140", "a141", "a142", "a143", "a144", "a145", "a146", "a147", "a148", "a149", "a150", "a151", "a152", "a153", "a154", "a155", "a156", "a157", "a158", "a159", "a160", "a161", "a162", "a163", "a164", "a165", "a166", "a167", "a168", "a169", "a170", "a171", "a172", "a173", "a174", "a175", "a176", "a177", "a178", "a179", "a180", "a181", "a182", "a183", "a184", "a185", "a186", "a187", "a188", "a189", "a190", "a191", "a192", "a193", "a194", "a195", "a196", "a197", "a198", "a199", "a200", "a201", "a202", "a203", "a204", "a205", "a206", "a207", "a208", "a209", "a210", "a211", "a212", "a213", "a214", "a215", "a216", "a217", "a218", "a219", "a220", "a221", "a222", "a223", "a224", "a225", "a226", "a227", "a228", "a229", "a230", "a231", "a232", "a233", "a234", "a235", "a236", "a237", "a238", "a239", "a240", "a241", "a242", "a243", "a244", "a245", "a246", "a247", "a248", "a249", "a250", "a251", "a252", "a253", "a254", "a255", "s24", "s25", "s26", "s27", "s28", "s29", "s30", "s31", "s36", "s37", "s38", "s39", "s40", "s41", "s42", "s43", "s46", "s48", "s49", "s50", "s51", "s52", "s53", "s54", "s55", "s56", "s57", "s58", "s59", "s60", "s61", "s62", "s63", "s64", "s65", "s66", "s67", "s68", "s69", "s70", "s71", "s72", "s73", "s74", "s75", "s76", "s77");
        mx = __builtin_amdgcn_readfirstlane(mx); lim = __builtin_amdgcn_readfirstlane(lim); ew = __builtin_amdgcn_readfirstlane(ew);
        if (!first || ((mx & 0xffffu) <= lim && (mx >> 16) <= ew)) break;
        force = mx & 0xffffu;
        if ((mx >> 16) > ew) forcew = mx >> 16;
        asm volatile("s_waitcnt vmcnt(0) lgkmcnt(0)\n\ts_barrier\n\ts_load_dwordx8 %0, %1, 0" : "={s[48:55]}"(ka) : "s"(kp) : "memory");
    }
}

extern "C" void kernel_launch(void* const* d_in, const int* in_sizes, int n_in,
                              void* d_out, int out_size, void* d_ws, size_t ws_size,
                              hipStream_t stream) {
    conv_main<<<256, 256, 0, stream>>>((const unsigned char*)d_in[0], (const unsigned char*)d_in[1], (unsigned char*)d_ws, (unsigned char*)d_out
    );
}
```
